```python
import torch

def _softmax(x, axis=-1):
    return torch.softmax(x, dim=axis)

def _gelu(x):
    return torch.nn.functional.gelu(x, approximate='tanh')

def _silu(x):
    return torch.nn.functional.silu(x)

def _sigmoid(x):
    return torch.sigmoid(x)

def _layernorm(x, axis=-1, eps=1e-05):
    m = x.mean(dim=axis, keepdim=True)
    v = x.var(dim=axis, keepdim=True, unbiased=False)
    return (x - m) / torch.sqrt(v + eps)

def _scatter_add(tgt, idx, src, axis=0):
    return tgt.clone().index_add_(axis, idx, src)

def _segment_sum(src, seg, n):
    out = torch.zeros((n,) + src.shape[1:], dtype=src.dtype)
    return out.index_add_(0, seg, src)

def _pad(x, pw):
    flat = tuple((v for pair in reversed(pw) for v in pair))
    return torch.nn.functional.pad(x, flat)
kernel_name = 'adaLN_cond__compressed_kv_cla__dense_activation_relu__decode'

def setup_inputs():

    _w = lambda a: (lambda t: t.to(torch.bfloat16) if t.is_floating_point() else t)(torch.as_tensor(a))

    def _A_setup():
        B, H, Sq, Sk, D, M = (1, 32, 1, 8192, 192, 640)
        Dc = 4 * D
        return {'x': _w((lambda *s: torch.randn(*s, dtype=torch.bfloat16) * 0.1)(B, Sq, M)), 'c_kv': _w((lambda *s: torch.randn(*s, dtype=torch.bfloat16) * 3.2)(B, Sk, Dc)), 'w_q': _w((lambda *s: torch.randn(*s, dtype=torch.bfloat16) * 1.6)(M, H, D) * M ** (-0.5)), 'w_uk': _w((lambda *s: torch.randn(*s, dtype=torch.bfloat16) * 1.6)(Dc, H, D) * Dc ** (-0.5)), 'w_uv': _w((lambda *s: torch.randn(*s, dtype=torch.bfloat16) * 3.2)(Dc, H, D) * Dc ** (-0.5)), 'w_o': _w((lambda *s: torch.randn(*s, dtype=torch.bfloat16) * 3.2)(H, D, M) * (H * D) ** (-0.5))}
    _A_in = _A_setup()

    def _B_setup():
        B, S, D, Dff = (1, 1, 640, 1536)
        return {'x': _w((lambda *s: torch.randn(*s, dtype=torch.bfloat16) * 0.1)(B, S, D)), 'w1': _w((lambda *s: torch.randn(*s, dtype=torch.bfloat16) * 0.1)(D, Dff)), 'w2': _w((lambda *s: torch.randn(*s, dtype=torch.bfloat16) * 0.1)(Dff, D))}
    _B_in = _B_setup()
    return {'A_x': _A_in['x'], 'A_c_kv': _A_in['c_kv'], 'A_w_q': _A_in['w_q'], 'A_w_uk': _A_in['w_uk'], 'A_w_uv': _A_in['w_uv'], 'A_w_o': _A_in['w_o'], 'B_x': _B_in['x'], 'B_w1': _B_in['w1'], 'B_w2': _B_in['w2'], 'c': _w((lambda *s: torch.randn(*s, dtype=torch.bfloat16) * 0.8)(1, 640) * 8.0), 'w_mod': _w((lambda *s: torch.randn(*s, dtype=torch.bfloat16) * 0.8)(640, 6 * 640) * 640 ** (-0.5) * 8.0)}

def reference(A_x, A_c_kv, A_w_q, A_w_uk, A_w_uv, A_w_o, B_x, B_w1, B_w2, c, w_mod):

    def _A_ref(x, c_kv, w_q, w_uk, w_uv, w_o):
        B, Sq, M = (x.shape[0], x.shape[1], x.shape[2])
        Sk = c_kv.shape[1]
        H, D = (w_q.shape[1], w_q.shape[2])
        q = torch.einsum('bqm,mhd->bhqd', x, w_q)
        k = torch.einsum('bkc,chd->bhkd', c_kv, w_uk)
        v = torch.einsum('bkc,chd->bhkd', c_kv, w_uv)
        scores = torch.einsum('bhqd,bhkd->bhqk', q, k) * D ** (-0.5)
        off = Sk - Sq
        qi = torch.arange(Sq)[:, None]
        kj = torch.arange(Sk)[None, :]
        cmask = kj <= qi + off
        scores = torch.where(cmask[None, None], scores, -1000000000.0)
        p = _softmax(scores, axis=-1)
        ctx = torch.einsum('bhqk,bhkd->bhqd', p, v)
        return torch.einsum('bhqd,hdm->bqm', ctx, w_o)

    def _B_ref(x, w1, w2):
        h = torch.einsum('bsd,df->bsf', x, w1)
        zero = torch.zeros_like(h)
        h = torch.maximum(h, zero)
        return torch.einsum('bsf,fd->bsd', h, w2)
    x = A_x
    mod = (_silu(c) @ w_mod).reshape(c.shape[0], 6, 1, -1)
    sa, ca, ga, sb, cb, gb = (mod[:, 0].clone(), mod[:, 1].clone(), mod[:, 2].clone(), mod[:, 3].clone(), mod[:, 4].clone(), mod[:, 5].clone())
    h = _layernorm(x, axis=-1) * (1 + ca) + sa
    x = x + ga * _A_ref(h, A_c_kv, A_w_q, A_w_uk, A_w_uv, A_w_o)
    h = _layernorm(x, axis=-1) * (1 + cb) + sb
    x = x + gb * _B_ref(h, B_w1, B_w2)
    return x
```

```cpp
#include <hip/hip_runtime.h>
#include <hip/hip_bf16.h>
#include <cstdio>
#define MIMIC 1
#define DBG 1
typedef __hip_bfloat16 bf16;
__device__ inline float b2f(bf16 v) { return __bfloat162float(v); }
__device__ inline float rb(float v) {
    return __bfloat162float(__float2bfloat16(v));
}
constexpr int M = 640, H = 32, D = 192, DC = 768, SK = 8192, DFF = 1536;

__global__ void k_mod(const bf16* c, const bf16* wmod, float* mod) {
    int n = blockIdx.x * blockDim.x + threadIdx.x;
    if (n >= 6 * M) return;
    float acc = 0;
    for (int m = 0; m < M; m++) {
        float cv = b2f(c[m]);
        float s = rb(cv / (1.f + expf(-cv)));
        acc += s * b2f(wmod[m * 6 * M + n]);
    }
    mod[n] = rb(acc);
}
__device__ void ln_mod(const float* x, const float* shift, const float* scale, float* h) {
    __shared__ float red[32];
    int t = threadIdx.x;
    float v = x[t];
    __shared__ float sm, sv;
    __shared__ float buf[M];
    buf[t] = v;
    __syncthreads();
    if (t == 0) {
        double s = 0; for (int i = 0; i < M; i++) s += buf[i];
        double mean = s / M;
        double q = 0; for (int i = 0; i < M; i++) q += (buf[i] - mean) * (buf[i] - mean);
        sm = rb((float)mean); sv = rb((float)(q / M));
    }
    __syncthreads();
    float d = rb(v - sm);
    float den = rb(sqrtf(rb(sv + 1e-5f)));
    float ln = rb(d / den);
    float o = rb(rb(ln * rb(1.f + scale[t])) + shift[t]);
    h[t] = o;
}
__global__ void k_ln1(const bf16* x, const float* mod, float* xf, float* h) {
    int t = threadIdx.x;
    xf[t] = b2f(x[t]);
    __syncthreads();
    ln_mod(xf, mod + 0 * M, mod + 1 * M, h);
}
__global__ void k_q(const float* h, const bf16* wq, float* q) {
    int n = blockIdx.x * blockDim.x + threadIdx.x;
    float acc = 0;
    for (int m = 0; m < M; m++) acc += h[m] * b2f(wq[m * H * D + n]);
    q[n] = rb(acc);
}
__global__ void k_qabs(const float* q, const bf16* wuk, float* qabs) {
    int n = blockIdx.x * blockDim.x + threadIdx.x;
    int hh = n / DC, c = n % DC;
    float acc = 0;
    for (int d = 0; d < D; d++) acc += q[hh * D + d] * b2f(wuk[(c * H + hh) * D + d]);
    qabs[n] = acc;
}
__global__ void k_scores(const float* qabs, const bf16* ckv, float* sc) {
    int n = blockIdx.x * blockDim.x + threadIdx.x;
    int k = n / H, hh = n % H;
    float acc = 0;
    for (int c = 0; c < DC; c++) acc += qabs[hh * DC + c] * b2f(ckv[k * DC + c]);
    sc[hh * SK + k] = rb(rb(acc) * 0.07216878364870322f);
}
__global__ void k_softmax(float* sc) {
    int hh = blockIdx.x;
    __shared__ float red[256];
    int t = threadIdx.x;
    float mx = -INFINITY, mx2 = -INFINITY;
    for (int k = t; k < SK; k += 256) mx = fmaxf(mx, sc[hh * SK + k]);
    red[t] = mx; __syncthreads();
    for (int s = 128; s > 0; s >>= 1) { if (t < s) red[t] = fmaxf(red[t], red[t + s]); __syncthreads(); }
    mx = red[0]; __syncthreads();
    for (int k = t; k < SK; k += 256) { float v = sc[hh * SK + k]; if (v < mx) mx2 = fmaxf(mx2, v); }
    red[t] = mx2; __syncthreads();
    for (int s = 128; s > 0; s >>= 1) { if (t < s) red[t] = fmaxf(red[t], red[t + s]); __syncthreads(); }
    mx2 = red[0]; __syncthreads();
    if (t == 0) printf("head %d max %f second %f gap %f\n", hh, mx, mx2, mx - mx2);
    float sum = 0;
    for (int k = t; k < SK; k += 256) sum += expf(sc[hh * SK + k] - mx);
    red[t] = sum; __syncthreads();
    for (int s = 128; s > 0; s >>= 1) { if (t < s) red[t] += red[t + s]; __syncthreads(); }
    sum = red[0];
    for (int k = t; k < SK; k += 256) sc[hh * SK + k] = rb(expf(sc[hh * SK + k] - mx) / sum);
}
__global__ void k_ctxlat(const float* p, const bf16* ckv, float* cl) {
    int n = blockIdx.x * blockDim.x + threadIdx.x;
    int hh = n / DC, c = n % DC;
    float acc = 0;
    for (int k = 0; k < SK; k++) acc += p[hh * SK + k] * b2f(ckv[k * DC + c]);
    cl[n] = acc;
}
__global__ void k_ctx(const float* cl, const bf16* wuv, float* ctx) {
    int n = blockIdx.x * blockDim.x + threadIdx.x;
    int hh = n / D, d = n % D;
    float acc = 0;
    for (int c = 0; c < DC; c++) acc += cl[hh * DC + c] * b2f(wuv[(c * H + hh) * D + d]);
    ctx[n] = rb(acc);
}
__global__ void k_o(const float* ctx, const bf16* wo, const bf16* x, const float* mod, float* x1) {
    int m = blockIdx.x * blockDim.x + threadIdx.x;
    if (m >= M) return;
    float acc = 0;
    for (int i = 0; i < H * D; i++) acc += ctx[i] * b2f(wo[i * M + m]);
    x1[m] = rb(b2f(x[m]) + rb(mod[2 * M + m] * rb(acc)));
}
__global__ void k_ln2(const float* x1, const float* mod, float* h) {
    ln_mod(x1, mod + 3 * M, mod + 4 * M, h);
}
__global__ void k_f1(const float* h, const bf16* w1, float* a) {
    int n = blockIdx.x * blockDim.x + threadIdx.x;
    float acc = 0;
    for (int m = 0; m < M; m++) acc += h[m] * b2f(w1[m * DFF + n]);
    a[n] = fmaxf(rb(acc), 0.f);
}
__global__ void k_f2(const float* a, const bf16* w2, const float* x1, const float* mod, bf16* out) {
    int m = blockIdx.x * blockDim.x + threadIdx.x;
    if (m >= M) return;
    float acc = 0;
    for (int f = 0; f < DFF; f++) acc += a[f] * b2f(w2[f * M + m]);
    out[m] = __float2bfloat16(x1[m] + rb(mod[5 * M + m] * rb(acc)));
}

extern "C" void kernel_launch(void* const* d_in, const int* in_sizes, int n_in,
                              void* d_out, int out_size, void* d_ws, size_t ws_size,
                              hipStream_t stream) {
    const bf16* x = (const bf16*)d_in[0];
    const bf16* ckv = (const bf16*)d_in[1];
    const bf16* wq = (const bf16*)d_in[2];
    const bf16* wuk = (const bf16*)d_in[3];
    const bf16* wuv = (const bf16*)d_in[4];
    const bf16* wo = (const bf16*)d_in[5];
    const bf16* w1 = (const bf16*)d_in[7];
    const bf16* w2 = (const bf16*)d_in[8];
    const bf16* c = (const bf16*)d_in[9];
    const bf16* wmod = (const bf16*)d_in[10];
    float* ws = (float*)d_ws;
    float* mod = ws; ws += 4096;
    float* xf = ws; ws += 1024;
    float* h = ws; ws += 1024;
    float* q = ws; ws += H * D;
    float* qabs = ws; ws += H * DC;
    float* sc = ws; ws += H * SK;
    float* cl = ws; ws += H * DC;
    float* ctx = ws; ws += H * D;
    float* x1 = ws; ws += 1024;
    float* h2 = ws; ws += 1024;
    float* a = ws; ws += 2048;
    k_mod<<<15, 256, 0, stream>>>(c, wmod, mod);
    k_ln1<<<1, M, 0, stream>>>(x, mod, xf, h);
    k_q<<<H * D / 256, 256, 0, stream>>>(h, wq, q);
    k_qabs<<<H * DC / 256, 256, 0, stream>>>(q, wuk, qabs);
    k_scores<<<H * SK / 256, 256, 0, stream>>>(qabs, ckv, sc);
    k_softmax<<<H, 256, 0, stream>>>(sc);
    k_ctxlat<<<H * DC / 256, 256, 0, stream>>>(sc, ckv, cl);
    k_ctx<<<H * D / 256, 256, 0, stream>>>(cl, wuv, ctx);
    k_o<<<3, 256, 0, stream>>>(ctx, wo, x, mod, x1);
    k_ln2<<<1, M, 0, stream>>>(x1, mod, h2);
    k_f1<<<DFF / 256, 256, 0, stream>>>(h2, w1, a);
    k_f2<<<3, 256, 0, stream>>>(a, w2, x1, mod, (bf16*)d_out);
}
```

```cpp
#include <hip/hip_runtime.h>
#include <hip/hip_bf16.h>
#define MIMIC 0
typedef __hip_bfloat16 bf16;
__device__ inline float b2f(bf16 v) { return __bfloat162float(v); }
__device__ inline float rb(float v) {
    return v;
}
constexpr int M = 640, H = 32, D = 192, DC = 768, SK = 8192, DFF = 1536;

__global__ void k_mod(const bf16* c, const bf16* wmod, float* mod) {
    int n = blockIdx.x * blockDim.x + threadIdx.x;
    if (n >= 6 * M) return;
    float acc = 0;
    for (int m = 0; m < M; m++) {
        float cv = b2f(c[m]);
        float s = rb(cv / (1.f + expf(-cv)));
        acc += s * b2f(wmod[m * 6 * M + n]);
    }
    mod[n] = rb(acc);
}
__device__ void ln_mod(const float* x, const float* shift, const float* scale, float* h) {
    __shared__ float red[32];
    int t = threadIdx.x;
    float v = x[t];
    __shared__ float sm, sv;
    __shared__ float buf[M];
    buf[t] = v;
    __syncthreads();
    if (t == 0) {
        double s = 0; for (int i = 0; i < M; i++) s += buf[i];
        double mean = s / M;
        double q = 0; for (int i = 0; i < M; i++) q += (buf[i] - mean) * (buf[i] - mean);
        sm = rb((float)mean); sv = rb((float)(q / M));
    }
    __syncthreads();
    float d = rb(v - sm);
    float den = rb(sqrtf(rb(sv + 1e-5f)));
    float ln = rb(d / den);
    float o = rb(rb(ln * rb(1.f + scale[t])) + shift[t]);
    h[t] = o;
}
__global__ void k_ln1(const bf16* x, const float* mod, float* xf, float* h) {
    int t = threadIdx.x;
    xf[t] = b2f(x[t]);
    __syncthreads();
    ln_mod(xf, mod + 0 * M, mod + 1 * M, h);
}
__global__ void k_q(const float* h, const bf16* wq, float* q) {
    int n = blockIdx.x * blockDim.x + threadIdx.x;
    float acc = 0;
    for (int m = 0; m < M; m++) acc += h[m] * b2f(wq[m * H * D + n]);
    q[n] = rb(acc);
}
__global__ void k_qabs(const float* q, const bf16* wuk, float* qabs) {
    int n = blockIdx.x * blockDim.x + threadIdx.x;
    int hh = n / DC, c = n % DC;
    float acc = 0;
    for (int d = 0; d < D; d++) acc += q[hh * D + d] * b2f(wuk[(c * H + hh) * D + d]);
    qabs[n] = acc;
}
__global__ void k_scores(const float* qabs, const bf16* ckv, float* sc) {
    int n = blockIdx.x * blockDim.x + threadIdx.x;
    int k = n / H, hh = n % H;
    float acc = 0;
    for (int c = 0; c < DC; c++) acc += qabs[hh * DC + c] * b2f(ckv[k * DC + c]);
    sc[hh * SK + k] = rb(rb(acc) * 0.07216878364870322f);
}
__global__ void k_softmax(float* sc) {
    int hh = blockIdx.x;
    __shared__ float red[256];
    int t = threadIdx.x;
    float mx = -INFINITY;
    for (int k = t; k < SK; k += 256) mx = fmaxf(mx, sc[hh * SK + k]);
    red[t] = mx; __syncthreads();
    for (int s = 128; s > 0; s >>= 1) { if (t < s) red[t] = fmaxf(red[t], red[t + s]); __syncthreads(); }
    mx = red[0]; __syncthreads();
    float sum = 0;
    for (int k = t; k < SK; k += 256) sum += expf(sc[hh * SK + k] - mx);
    red[t] = sum; __syncthreads();
    for (int s = 128; s > 0; s >>= 1) { if (t < s) red[t] += red[t + s]; __syncthreads(); }
    sum = red[0];
    for (int k = t; k < SK; k += 256) sc[hh * SK + k] = rb(expf(sc[hh * SK + k] - mx) / sum);
}
__global__ void k_ctxlat(const float* p, const bf16* ckv, float* cl) {
    int n = blockIdx.x * blockDim.x + threadIdx.x;
    int hh = n / DC, c = n % DC;
    float acc = 0;
    for (int k = 0; k < SK; k++) acc += p[hh * SK + k] * b2f(ckv[k * DC + c]);
    cl[n] = acc;
}
__global__ void k_ctx(const float* cl, const bf16* wuv, float* ctx) {
    int n = blockIdx.x * blockDim.x + threadIdx.x;
    int hh = n / D, d = n % D;
    float acc = 0;
    for (int c = 0; c < DC; c++) acc += cl[hh * DC + c] * b2f(wuv[(c * H + hh) * D + d]);
    ctx[n] = rb(acc);
}
__global__ void k_o(const float* ctx, const bf16* wo, const bf16* x, const float* mod, float* x1) {
    int m = blockIdx.x * blockDim.x + threadIdx.x;
    if (m >= M) return;
    float acc = 0;
    for (int i = 0; i < H * D; i++) acc += ctx[i] * b2f(wo[i * M + m]);
    x1[m] = rb(b2f(x[m]) + rb(mod[2 * M + m] * rb(acc)));
}
__global__ void k_ln2(const float* x1, const float* mod, float* h) {
    ln_mod(x1, mod + 3 * M, mod + 4 * M, h);
}
__global__ void k_f1(const float* h, const bf16* w1, float* a) {
    int n = blockIdx.x * blockDim.x + threadIdx.x;
    float acc = 0;
    for (int m = 0; m < M; m++) acc += h[m] * b2f(w1[m * DFF + n]);
    a[n] = fmaxf(rb(acc), 0.f);
}
__global__ void k_f2(const float* a, const bf16* w2, const float* x1, const float* mod, bf16* out) {
    int m = blockIdx.x * blockDim.x + threadIdx.x;
    if (m >= M) return;
    float acc = 0;
    for (int f = 0; f < DFF; f++) acc += a[f] * b2f(w2[f * M + m]);
    out[m] = __float2bfloat16(x1[m] + rb(mod[5 * M + m] * rb(acc)));
}

extern "C" void kernel_launch(void* const* d_in, const int* in_sizes, int n_in,
                              void* d_out, int out_size, void* d_ws, size_t ws_size,
                              hipStream_t stream) {
    const bf16* x = (const bf16*)d_in[0];
    const bf16* ckv = (const bf16*)d_in[1];
    const bf16* wq = (const bf16*)d_in[2];
    const bf16* wuk = (const bf16*)d_in[3];
    const bf16* wuv = (const bf16*)d_in[4];
    const bf16* wo = (const bf16*)d_in[5];
    const bf16* w1 = (const bf16*)d_in[7];
    const bf16* w2 = (const bf16*)d_in[8];
    const bf16* c = (const bf16*)d_in[9];
    const bf16* wmod = (const bf16*)d_in[10];
    float* ws = (float*)d_ws;
    float* mod = ws; ws += 4096;
    float* xf = ws; ws += 1024;
    float* h = ws; ws += 1024;
    float* q = ws; ws += H * D;
    float* qabs = ws; ws += H * DC;
    float* sc = ws; ws += H * SK;
    float* cl = ws; ws += H * DC;
    float* ctx = ws; ws += H * D;
    float* x1 = ws; ws += 1024;
    float* h2 = ws; ws += 1024;
    float* a = ws; ws += 2048;
    k_mod<<<15, 256, 0, stream>>>(c, wmod, mod);
    k_ln1<<<1, M, 0, stream>>>(x, mod, xf, h);
    k_q<<<H * D / 256, 256, 0, stream>>>(h, wq, q);
    k_qabs<<<H * DC / 256, 256, 0, stream>>>(q, wuk, qabs);
    k_scores<<<H * SK / 256, 256, 0, stream>>>(qabs, ckv, sc);
    k_softmax<<<H, 256, 0, stream>>>(sc);
    k_ctxlat<<<H * DC / 256, 256, 0, stream>>>(sc, ckv, cl);
    k_ctx<<<H * D / 256, 256, 0, stream>>>(cl, wuv, ctx);
    k_o<<<3, 256, 0, stream>>>(ctx, wo, x, mod, x1);
    k_ln2<<<1, M, 0, stream>>>(x1, mod, h2);
    k_f1<<<DFF / 256, 256, 0, stream>>>(h2, w1, a);
    k_f2<<<3, 256, 0, stream>>>(a, w2, x1, mod, (bf16*)d_out);
}
```

```cpp
#include <hip/hip_runtime.h>
#include <hip/hip_bf16.h>
#include <stdint.h>

#define LAS __attribute__((address_space(3)))
#define DI __device__ __forceinline__
typedef unsigned int u32;
typedef unsigned long long u64;
typedef u32 u32x2 __attribute__((ext_vector_type(2)));
typedef u32 u32x4 __attribute__((ext_vector_type(4)));
typedef float f32x2 __attribute__((ext_vector_type(2)));
typedef float f32x4 __attribute__((ext_vector_type(4)));
typedef short s16x4 __attribute__((ext_vector_type(4)));
typedef __bf16 bf16x2 __attribute__((ext_vector_type(2)));
typedef __bf16 bf16x8 __attribute__((ext_vector_type(8)));
typedef __amdgpu_buffer_rsrc_t rsrc_t;

#define WIN 24
#define NTW 0

constexpr int M_ = 640, H_ = 32, D_ = 192, DC = 768, SK = 8192, DFF = 1536;
constexpr u32 SENT = 0xFFFFFFFFu;
constexpr u32 OOB = 0x80000000u;

constexpr u32 W_EPOCH = 0;
constexpr u32 W_FA = 256;
constexpr u32 W_FB = W_FA + 2048;
constexpr u32 W_G1 = W_FB + 2048;
constexpr u32 W_SMALL = 16384;
constexpr u32 W_Q2 = W_SMALL;
constexpr u32 W_QA = W_Q2 + 98304;
constexpr u32 W_ML = W_QA + 98304;
constexpr u32 W_CTX = W_ML + 32768;
constexpr u32 W_AT = W_CTX + 98304;
constexpr u32 W_FP = W_AT + 10240;
constexpr u32 W_MR = W_FP + 122880;
constexpr u32 W_SMALL_END = W_MR + 20480;
constexpr u32 SMALL_CH = (W_SMALL_END - W_SMALL) / 16;
constexpr u32 SMALL_PER = (SMALL_CH + 255) / 256;
constexpr u32 W_O = 524288;
constexpr u32 O_BYTES = 32u * 8 * 128 * 96 * 2;
constexpr u32 W_PROF = W_O + O_BYTES;
constexpr u32 W_END = W_PROF + 256 * 64 * 8;
static_assert(W_SMALL_END <= W_O, "ws map");
static_assert(SMALL_PER <= 128, "prefill");

constexpr u32 L_A = 0;
constexpr u32 BLK = 12288;
constexpr u32 L_S1W = 5 * BLK;
constexpr u32 L_X = 98304;
constexpr u32 L_Y = 135168;
constexpr u32 L_FLG = L_Y;
constexpr u32 L_S0A = L_Y + 256;
constexpr u32 L_FR = L_Y + 320;
constexpr u32 L_GR = L_Y + 512;
constexpr u32 L_SS = L_Y + 1024;
constexpr u32 L_ZEND = L_Y + 5120;
constexpr u32 L_PP = L_Y + 5120;
constexpr u32 L_HX = L_Y + 7168;
constexpr u32 L_QH = L_Y + 7936;
constexpr u32 L_AM = L_Y + 8704;
constexpr u32 L_SIL = L_Y + 11776;
constexpr u32 LDS_BYTES = 163840;
constexpr u32 B_W = 3072, B_W2 = 6144, B_GA = 9216, B_X1 = 9216 + 1280, B_GB = B_X1 + 320;

enum { F_LAND = 0, F_RDYA, F_RDYB, F_S0CNT, F_HX, F_S1DONE, F_QH, F_S2DONE, F_SADD, F_PCNT, F_PVDONE, F_AM, F_FRC, F_GRC };

constexpr u32 CAP_G = 20000;
constexpr u32 CAP_L = 400000;

constexpr u32 T_S1 = 30, T_S2 = 66, T_TILE0 = 66, T_TILE = 162, T_UV = 198;

DI rsrc_t mkrs(const void* p, u32 bytes) { return __builtin_amdgcn_make_buffer_rsrc((void*)p, (short)0, (int)bytes, 0x00020000); }
template <int AUX> DI u32x4 ld128(rsrc_t r, u32 off) { return __builtin_amdgcn_raw_buffer_load_b128(r, off, 0, AUX); }
template <int AUX> DI u32x2 ld64(rsrc_t r, u32 off) { return __builtin_amdgcn_raw_buffer_load_b64(r, off, 0, AUX); }
template <int AUX> DI u32 ld32(rsrc_t r, u32 off) { return __builtin_amdgcn_raw_buffer_load_b32(r, off, 0, AUX); }
DI u32 ld16(rsrc_t r, u32 off) { return (u32)__builtin_amdgcn_raw_buffer_load_b16(r, off, 0, 0); }
template <int AUX> DI void st128(u32x4 v, rsrc_t r, u32 off) { __builtin_amdgcn_raw_buffer_store_b128(v, r, off, 0, AUX); }
template <int AUX> DI void st64(u32x2 v, rsrc_t r, u32 off) { __builtin_amdgcn_raw_buffer_store_b64(v, r, off, 0, AUX); }
constexpr int SC1 = 16;

DI float f_lo(u32 v) { return __uint_as_float(v << 16); }
DI float f_hi(u32 v) { return __uint_as_float(v & 0xFFFF0000u); }
DI u32 pk2(float a, float b) { bf16x2 v; v[0] = (__bf16)a; v[1] = (__bf16)b; return __builtin_bit_cast(u32, v); }
DI u32 bf1(float a) { return pk2(a, 0.f) & 0xFFFFu; }
DI u32 san(u32 v) { return v < 0xFFFFFFFEu ? v : 0xFFFFFFFEu; }
DI u32x4 san4(u32x4 v) { u32x4 r; r[0] = san(v[0]); r[1] = san(v[1]); r[2] = san(v[2]); r[3] = san(v[3]); return r; }
DI u32 umax(u32 a, u32 b) { return a > b ? a : b; }
DI u32 mx4(u32 acc, u32x4 v) { return umax(umax(acc, umax(v[0], v[1])), umax(v[2], v[3])); }

template <int CTRL> DI u32 dppu(u32 v) { return (u32)__builtin_amdgcn_update_dpp(0, (int)v, CTRL, 0xF, 0xF, true); }
template <int CTRL> DI float dppf(float v) { return __uint_as_float(dppu<CTRL>(__float_as_uint(v))); }
constexpr int ROR(int n) { return 0x120 + n; }
constexpr int SHL(int n) { return 0x100 + n; }
DI float row_sum(float v) { v += dppf<ROR(8)>(v); v += dppf<ROR(4)>(v); v += dppf<ROR(2)>(v); v += dppf<ROR(1)>(v); return v; }
DI float row_max(float v) { v = fmaxf(v, dppf<ROR(8)>(v)); v = fmaxf(v, dppf<ROR(4)>(v)); v = fmaxf(v, dppf<ROR(2)>(v)); v = fmaxf(v, dppf<ROR(1)>(v)); return v; }
DI void swap16(float& a, float& b) { auto r = __builtin_amdgcn_permlane16_swap(__float_as_uint(a), __float_as_uint(b), false, false); a = __uint_as_float(r[0]); b = __uint_as_float(r[1]); }
DI void swap32(float& a, float& b) { auto r = __builtin_amdgcn_permlane32_swap(__float_as_uint(a), __float_as_uint(b), false, false); a = __uint_as_float(r[0]); b = __uint_as_float(r[1]); }
DI float xg_sum(float v) { float a = v, b = v; swap16(a, b); v = a + b; a = v; b = v; swap32(a, b); return a + b; }
DI float xg_max(float v) { float a = v, b = v; swap16(a, b); v = fmaxf(a, b); a = v; b = v; swap32(a, b); return fmaxf(a, b); }
DI float wave_sum(float v) { return xg_sum(row_sum(v)); }
DI float wave_max(float v) { return xg_max(row_max(v)); }
DI float sc32(float a, float b) { swap32(a, b); return a + b; }
DI float sc16(float a, float b) { swap16(a, b); return a + b; }

DI f32x4 mfma32(u32x4 a, u32x4 b, f32x4 c) { return __builtin_amdgcn_mfma_f32_16x16x32_bf16(__builtin_bit_cast(bf16x8, a), __builtin_bit_cast(bf16x8, b), c, 0, 0, 0); }
DI f32x4 mfma16(u32x2 a, u32x2 b, f32x4 c) { return __builtin_amdgcn_mfma_f32_16x16x16bf16_1k(__builtin_bit_cast(s16x4, a), __builtin_bit_cast(s16x4, b), c, 0, 0, 0); }
DI u32x2 trd(u32 addr) { return __builtin_bit_cast(u32x2, __builtin_amdgcn_ds_read_tr16_b64_v4i16((LAS s16x4*)addr)); }
DI u32x4 cat(u32x2 a, u32x2 b) { u32x4 r; r[0] = a[0]; r[1] = a[1]; r[2] = b[0]; r[3] = b[1]; return r; }
template <class T> DI T lds_ld(u32 addr) { return *(LAS T*)addr; }
template <class T> DI void lds_st(u32 addr, T v) { *(LAS T*)addr = v; }
DI void lds_addf(u32 addr, float v) { __hip_atomic_fetch_add((LAS float*)addr, v, __ATOMIC_RELAXED, __HIP_MEMORY_SCOPE_WORKGROUP); }
#define CBAR() asm volatile("" ::: "memory")

DI u32 flag_ld(int i) { return (u32)__builtin_amdgcn_readfirstlane((int)*(volatile LAS u32*)(L_FLG + 4 * i)); }
DI void flag_st(int i, u32 v, int lane) { CBAR(); if (lane == 0) *(volatile LAS u32*)(L_FLG + 4 * i) = v; CBAR(); }
DI void flag_add(int i, int lane) { CBAR(); if (lane == 0) __hip_atomic_fetch_add((LAS u32*)(L_FLG + 4 * i), 1u, __ATOMIC_RELAXED, __HIP_MEMORY_SCOPE_WORKGROUP); CBAR(); }
DI u32 flag_add_ret(int i, int lane) {
    CBAR(); u32 o = 0;
    if (lane == 0) o = __hip_atomic_fetch_add((LAS u32*)(L_FLG + 4 * i), 1u, __ATOMIC_RELAXED, __HIP_MEMORY_SCOPE_WORKGROUP);
    o = (u32)__builtin_amdgcn_readfirstlane((int)o); CBAR(); return o;
}
template <bool SLEEP> DI void wait_ge(int i, u32 target, bool& dead) {
    u32 n = 0;
    while (!dead && flag_ld(i) < target) { if (++n > CAP_L) dead = true; if (SLEEP) __builtin_amdgcn_s_sleep(1); }
    CBAR();
}
DI bool none_sent(u32 mx) { return __builtin_amdgcn_ballot_w64(mx == SENT) == 0ull; }
#define POLL_STEP() { if (++spins > CAP_G) { dead = true; break; } __builtin_amdgcn_s_sleep(1); }

#define TS(i) do { } while (0)

DI u32x4 pack_row(u32 v16) {
    u32 pr = v16 | (dppu<SHL(1)>(v16) << 16);
    u32x4 r; r[0] = pr; r[1] = dppu<SHL(2)>(pr); r[2] = dppu<SHL(4)>(pr); r[3] = dppu<SHL(6)>(pr);
    return san4(r);
}

template <int NT> DI void dma(u32 lds, u32 voff, rsrc_t rs, u32 soff) {
    if (NT) asm volatile("s_nop 4\n\ts_mov_b32 m0, %0\n\ts_nop 0\n\tbuffer_load_dwordx4 %1, %2, %3 offen nt lds" ::"s"(lds), "v"(voff), "s"(rs), "s"(soff) : "memory");
    else asm volatile("s_nop 4\n\ts_mov_b32 m0, %0\n\ts_nop 0\n\tbuffer_load_dwordx4 %1, %2, %3 offen lds" ::"s"(lds), "v"(voff), "s"(rs), "s"(soff) : "memory");
}
template <int N> DI void waitvm() { asm volatile("s_waitcnt vmcnt(%0)" ::"i"(N) : "memory"); }
struct Ldr { int issued; int lane; bool go; bool dead; };
DI void publish(Ldr& s, int landed) { if (s.go && landed > 0) flag_st(F_LAND, (u32)landed, s.lane); }
DI void step3(Ldr& s) { s.issued += 3; waitvm<WIN>(); publish(s, s.issued - WIN); }
template <int N> DI void drain(Ldr& s) { waitvm<N>(); publish(s, s.issued - N); if constexpr (N >= 3) drain<N - 3>(s); }
DI void park(Ldr& s, int flag, u32 target) {
    if (flag_ld(flag) < target) { drain<WIN - 3>(s); wait_ge<true>(flag, target, s.dead); }
    CBAR();
}
template <int NT> DI void seg3(Ldr& s, u32 lds0, const u32 (&v)[3], rsrc_t rs, u32 soff0, u32 sstep, int ng) {
    for (int g = 0; g < ng; ++g) {
        const u32 so = soff0 + (u32)g * sstep, ld = lds0 + 3072u * (u32)g;
        dma<NT>(ld, v[0], rs, so); dma<NT>(ld + 1024, v[1], rs, so); dma<NT>(ld + 2048, v[2], rs, so);
        step3(s);
    }
}

extern __shared__ __attribute__((aligned(16))) char smem_dyn[];

__global__ void __launch_bounds__(640, 3)
adaln_mla_ffn_kernel(const void* __restrict__ p_x, const void* __restrict__ p_ckv, const void* __restrict__ p_wq, const void* __restrict__ p_wuk,
                     const void* __restrict__ p_wuv, const void* __restrict__ p_wo, const void* __restrict__ p_w1, const void* __restrict__ p_w2,
                     const void* __restrict__ p_c, const void* __restrict__ p_wmod, void* __restrict__ p_out, void* __restrict__ p_ws)
{
    const int tid = threadIdx.x, lane = tid & 63;
    const int wave = __builtin_amdgcn_readfirstlane(tid >> 6);
    const int b = blockIdx.x, x = b & 7, r = b >> 3;
    const bool typeA = r < 20;
    const int g = lane >> 4, i16 = lane & 15, q4 = i16 >> 2, p4 = lane & 3;
    bool dead = false;
    const rsrc_t rWS = mkrs(p_ws, W_END);
    (void)smem_dyn;

    if (wave == 8) {
        Ldr s; s.issued = 0; s.lane = lane; s.go = false; s.dead = false;
        u32 v1[3], vt[3], vu[3];
#pragma unroll
        for (int k = 0; k < 3; ++k) {
            const u32 p = 64 * k + lane;
            const u32 row = p / 12, ch = (p % 12) ^ (((row >> 2) & 1) << 1);
            v1[k] = row * 12288u + ch * 16; vt[k] = row * 1536u + ch * 16;
            const u32 r2 = p / 24, c2 = (p % 24) ^ (((r2 >> 1) & 3) << 1);
            vu[k] = r2 * 12288u + c2 * 16;
        }
        const rsrc_t rq = mkrs(p_wq, M_ * H_ * D_ * 2), ruk = mkrs(p_wuk, DC * H_ * D_ * 2), ruv = mkrs(p_wuv, DC * H_ * D_ * 2), rkv = mkrs(p_ckv, SK * DC * 2);
        const u32 ks = r >> 3, h1 = 4 * x + ((r >> 1) & 3), half = r & 1;
        const u32 so1 = 160u * ks * 12288u + (h1 * 192 + 96 * half) * 2;
        seg3<NTW>(s, L_S1W, v1, rq, so1, 16u * 12288u, (WIN / 3) < 10 ? (WIN / 3) : 10);
        __builtin_amdgcn_s_barrier();
        s.go = true;
        if ((WIN / 3) < 10) seg3<NTW>(s, L_S1W + 3072u * (WIN / 3), v1, rq, so1 + (WIN / 3) * 16u * 12288u, 16u * 12288u, 10 - (WIN / 3));
        const u32 so2 = 96u * x * 12288u + (u32)r * 384u;
        seg3<NTW>(s, L_X, vu, ruk, so2, 8u * 12288u, 12);
        const u32 jt = x + 8 * (r >> 1);
        const u32 sot = 64u * jt * 1536u;
        for (int w = 0; w < 5; ++w) seg3<0>(s, L_A + BLK * w, vt, rkv, sot + 192u * w, 16u * 1536u, 4);
        park(s, F_S1DONE, 6);
        for (int w = 5; w < 8; ++w) seg3<0>(s, L_A + BLK * w, vt, rkv, sot + 192u * w, 16u * 1536u, 4);
        park(s, F_S2DONE, 6);
        seg3<NTW>(s, L_X, vu, ruv, so2, 8u * 12288u, 12);
        park(s, F_PVDONE, 8);
        if (typeA) {
            const rsrc_t ro = mkrs(p_wo, H_ * D_ * M_ * 2);
            const u32 row = lane >> 2, ch = (lane & 3) ^ (((row >> 3) & 1) << 1);
            const u32 vf = row * 1280u + ch * 16;
            for (int w = 0; w < 8; ++w) {
                const u32 so = (768u * x + 96u * w) * 1280u + 64u * r, ld = L_A + BLK * w + B_W;
                for (int k = 0; k < 6; k += 3) {
                    dma<NTW>(ld + 1024 * k, vf, ro, so + 16u * 1280u * k); dma<NTW>(ld + 1024 * (k + 1), vf, ro, so + 16u * 1280u * (k + 1));
                    dma<NTW>(ld + 1024 * (k + 2), vf, ro, so + 16u * 1280u * (k + 2));
                    step3(s);
                }
            }
        } else {
            const rsrc_t r1 = mkrs(p_w1, M_ * DFF * 2), r2 = mkrs(p_w2, DFF * M_ * 2);
            const u32 fb = x * 12 + (r - 20);
            const u32 va = (lane >> 1) * 3072u + (lane & 1) * 16, va2 = lane < 32 ? va : OOB;
            u32 vb[3];
#pragma unroll
            for (int k = 0; k < 3; ++k) { const u32 p = 64 * k + lane; vb[k] = p < 160 ? (p / 10) * 1280u + (p % 10) * 16 : OOB; }
            for (int w = 0; w < 8; ++w) {
                const u32 ld = L_A + BLK * w;
                const u32 sa = 80u * w * 3072u + fb * 32u, sb = 16u * fb * 1280u + 160u * w;
                dma<NTW>(ld + B_W, va, r1, sa); dma<NTW>(ld + B_W + 1024, va, r1, sa + 32u * 3072u); dma<NTW>(ld + B_W + 2048, va2, r1, sa + 64u * 3072u);
                step3(s);
                dma<NTW>(ld + B_W2, vb[0], r2, sb); dma<NTW>(ld + B_W2 + 1024, vb[1], r2, sb); dma<NTW>(ld + B_W2 + 2048, vb[2], r2, sb);
                step3(s);
            }
        }
        drain<WIN - 3>(s);
        return;
    }

    const u32x2 ep = ld64<SC1>(rWS, W_EPOCH);

    if (wave == 9) {
        const u64 tag = (((u64)ep[1] << 32) | ep[0]) + 1ull;
        u32x2 tg; tg[0] = (u32)tag; tg[1] = (u32)(tag >> 32);
        u32x4 sv; sv[0] = SENT; sv[1] = SENT; sv[2] = SENT; sv[3] = SENT;
        {
            const u32 c0 = (u32)b * SMALL_PER + lane, c1 = c0 + 64;
            st128<SC1>(sv, rWS, c0 < SMALL_CH ? W_SMALL + c0 * 16 : OOB);
            st128<SC1>(sv, rWS, (lane + 64 < (int)SMALL_PER && c1 < SMALL_CH) ? W_SMALL + c1 * 16 : OOB);
        }
        waitvm<0>();
        if (lane == 0) st64<SC1>(tg, rWS, W_FA + 8 * b);
#pragma unroll
        for (int k = 0; k < 24; ++k) st128<SC1>(sv, rWS, W_O + (u32)b * 24576u + k * 1024u + lane * 16u);
        __builtin_amdgcn_s_barrier();
        waitvm<0>();
        if (lane == 0) st64<SC1>(tg, rWS, W_FB + 8 * b);
        for (int pass = 0; pass < 2; ++pass) {
            const u32 base = pass ? W_FB : W_FA;
            u32 spins = 0;
            for (;;) {
                CBAR();
                const u32x4 a = ld128<SC1>(rWS, base + lane * 32), c = ld128<SC1>(rWS, base + lane * 32 + 16);
                const bool ok = a[0] == tg[0] && a[1] == tg[1] && a[2] == tg[0] && a[3] == tg[1] && c[0] == tg[0] && c[1] == tg[1] && c[2] == tg[0] && c[3] == tg[1];
                if (__builtin_amdgcn_ballot_w64(!ok) == 0ull) break;
                POLL_STEP();
            }
            flag_st(pass ? F_RDYB : F_RDYA, 1, lane);
            TS(40 + pass);
        }
        const int it = 255 - b;
        if (it < 160) {
            const int cb = it >> 1, kh = it & 1;
            const rsrc_t rc = mkrs(p_c, M_ * 2), rm = mkrs(p_wmod, M_ * 6 * M_ * 2);
#pragma unroll
            for (int k = 0; k < 5; ++k) {
                const float cv = __uint_as_float(ld16(rc, (320 * kh + 64 * k + lane) * 2) << 16);
                lds_st<float>(L_SIL + (64 * k + lane) * 4, cv * __builtin_amdgcn_rcpf(1.f + __expf(-cv)));
            }
            wait_ge<true>(F_LAND, T_TILE, dead);
            TS(42);
            u32x4 wv[20];
            const u32 vo = (u32)(320 * kh + (lane >> 2)) * 7680u + (1280u + 32u * cb) * 2u + (lane & 3) * 16u;
#pragma unroll
            for (int k = 0; k < 20; ++k) wv[k] = ld128<0>(rm, vo + (u32)k * 16u * 7680u);
            float acc[8];
#pragma unroll
            for (int j = 0; j < 8; ++j) acc[j] = 0.f;
#pragma unroll
            for (int k = 0; k < 20; ++k) {
                const float sl = lds_ld<float>(L_SIL + (16 * k + (lane >> 2)) * 4);
#pragma unroll
                for (int j = 0; j < 4; ++j) { acc[2 * j] += sl * f_lo(wv[k][j]); acc[2 * j + 1] += sl * f_hi(wv[k][j]); }
            }
            float t4[4], t2[2];
#pragma unroll
            for (int j = 0; j < 4; ++j) t4[j] = sc32(acc[j], acc[j + 4]);
#pragma unroll
            for (int j = 0; j < 2; ++j) t2[j] = sc16(t4[j], t4[j + 2]);
#pragma unroll
            for (int j = 0; j < 2; ++j) { t2[j] += dppf<ROR(8)>(t2[j]); t2[j] += dppf<ROR(4)>(t2[j]); }
            u32x2 o; o[0] = san(__float_as_uint(t2[0])); o[1] = san(__float_as_uint(t2[1]));
            const u32 col = 32u * cb + 8 * (lane & 3) + 4 * (lane >> 5) + 2 * ((lane >> 4) & 1);
            st64<SC1>(o, rWS, (lane & 12) == 0 ? W_MR + (kh * 2560u + col) * 4u : OOB);
            TS(43);
        }
        return;
    }

    const int w = wave;
    const rsrc_t rX = mkrs(p_x, M_ * 2);
    u32x4 m0v, m1v; u32 c0v = 0, c1v = 0;
    const bool doS0 = typeA && w < 5;
    if (doS0) {
        const rsrc_t rc = mkrs(p_c, M_ * 2), rm = mkrs(p_wmod, M_ * 6 * M_ * 2);
        const u32 row0 = 128 * w + lane, colb = (x * 160 + 8 * r) * 2;
        m0v = ld128<0>(rm, row0 * 7680u + colb); m1v = ld128<0>(rm, (row0 + 64) * 7680u + colb);
        c0v = ld16(rc, row0 * 2); c1v = ld16(rc, (row0 + 64) * 2);
    }
    u32 xs[5]; u32 xr[3];
    const u32 ks = r >> 3;
    if (w == 7) {
#pragma unroll
        for (int k = 0; k < 5; ++k) xs[k] = ld32<0>(rX, (64 * k + lane) * 4);
#pragma unroll
        for (int k = 0; k < 3; ++k) xr[k] = ld16(rX, (64 * k + lane) < 160 ? (160 * ks + 64 * k + lane) * 2 : OOB);
    }
    if (tid < (int)((L_ZEND - L_Y) / 16)) { u32x4 z; z[0] = 0; z[1] = 0; z[2] = 0; z[3] = 0; lds_st<u32x4>(L_Y + tid * 16, z); }
    asm volatile("s_waitcnt lgkmcnt(0)" ::: "memory");
    __builtin_amdgcn_s_barrier();
    CBAR();
    const u64 tag = (((u64)ep[1] << 32) | ep[0]) + 1ull;
    const u32 tagl = (u32)tag & 0xFFu, tagh = (u32)(tag >> 8);
    if (w == 0) TS(0);

    if (doS0) {
        const float cA = __uint_as_float(c0v << 16), cB = __uint_as_float(c1v << 16);
        const float sA = cA * __builtin_amdgcn_rcpf(1.f + __expf(-cA)), sB = cB * __builtin_amdgcn_rcpf(1.f + __expf(-cB));
        float acc[8];
#pragma unroll
        for (int j = 0; j < 4; ++j) { acc[2 * j] = sA * f_lo(m0v[j]) + sB * f_lo(m1v[j]); acc[2 * j + 1] = sA * f_hi(m0v[j]) + sB * f_hi(m1v[j]); }
        float t4[4], t2[2];
#pragma unroll
        for (int j = 0; j < 4; ++j) t4[j] = sc32(acc[j], acc[j + 4]);
#pragma unroll
        for (int j = 0; j < 2; ++j) t2[j] = row_sum(sc16(t4[j], t4[j + 2]));
        if (i16 == 0) { const u32 a = L_S0A + (4 * (g >> 1) + 2 * (g & 1)) * 4; lds_addf(a, t2[0]); lds_addf(a + 4, t2[1]); }
        if (flag_add_ret(F_S0CNT, lane) == 4) {
            const float v = lds_ld<float>(L_S0A + (lane & 7) * 4);
            const u32 v24 = (__float_as_uint(v) + 0x80u) >> 8;
            u32x2 gr; gr[0] = (tagl << 24) | v24; gr[1] = tagh;
            const u32 n = x * 160 + 8 * r + lane;
            const u32 off = W_G1 + (n < 640 ? n * 16 : (n - 640) * 16 + 8);
            st64<SC1>(gr, rWS, lane < 8 ? off : OOB);
            TS(1);
        }
    }

    const u32 h1 = 4 * x + ((r >> 1) & 3), half = r & 1;
    if (w == 7) {
        float sum = 0.f;
#pragma unroll
        for (int k = 0; k < 5; ++k) sum += f_lo(xs[k]) + f_hi(xs[k]);
        const float mu = wave_sum(sum) * (1.f / 640.f);
        float sq = 0.f;
#pragma unroll
        for (int k = 0; k < 5; ++k) { const float a = f_lo(xs[k]) - mu, c = f_hi(xs[k]) - mu; sq += a * a + c * c; }
        const float rstd = 1.f / sqrtf(wave_sum(sq) * (1.f / 640.f) + 1e-5f);
        u32x4 gv[3]; u32 spins = 0;
        for (;;) {
            CBAR();
            bool ok = true;
#pragma unroll
            for (int k = 0; k < 3; ++k) {
                const bool act = 64 * k + lane < 160;
                gv[k] = ld128<SC1>(rWS, act ? W_G1 + (160 * ks + 64 * k + lane) * 16 : OOB);
                ok = ok && (!act || ((gv[k][0] >> 24) == tagl && gv[k][1] == tagh && (gv[k][2] >> 24) == tagl && gv[k][3] == tagh));
            }
            if (__builtin_amdgcn_ballot_w64(!ok) == 0ull) break;
            POLL_STEP();
        }
        TS(2);
#pragma unroll
        for (int k = 0; k < 3; ++k) {
            const u32 m = 64 * k + lane;
            const float sa = __uint_as_float(gv[k][0] << 8), ca = __uint_as_float(gv[k][2] << 8);
            const float hv = (__uint_as_float(xr[k] << 16) - mu) * rstd * (1.f + ca) + sa;
            const u32 hi = bf1(hv), lo = bf1(hv - __uint_as_float(hi << 16));
            const u32 pos = (m & ~31u) + 8 * ((m >> 2) & 3) + 4 * ((m >> 4) & 1) + (m & 3);
            if (m < 160) { lds_st<unsigned short>(L_HX + pos * 2, (unsigned short)hi); lds_st<unsigned short>(L_HX + 320 + pos * 2, (unsigned short)lo); }
        }
        flag_st(F_HX, 1, lane);
    }
    if (w < 6) {
        wait_ge<true>(F_LAND, T_S1, dead);
        u32x4 bw[5];
        const u32 ab = L_S1W + (4 * g + q4) * 192 + (((2 * w + (p4 >> 1)) ^ ((g & 1) << 1)) * 16) + 8 * (p4 & 1);
#pragma unroll
        for (int t = 0; t < 5; ++t) bw[t] = cat(trd(ab + (32 * t) * 192), trd(ab + (32 * t + 16) * 192));
        flag_add(F_S1DONE, lane);
        wait_ge<false>(F_HX, 1, dead);
        f32x4 acc = {0.f, 0.f, 0.f, 0.f};
#pragma unroll
        for (int t = 0; t < 5; ++t) acc = mfma32(lds_ld<u32x4>(L_HX + (lane & 1) * 320 + 64 * t + 16 * g), bw[t], acc);
        const float qv = acc[0] + acc[1];
        u32x4 o; o[0] = __float_as_uint(qv); o[1] = dppu<SHL(1)>(o[0]); o[2] = dppu<SHL(2)>(o[0]); o[3] = dppu<SHL(3)>(o[0]);
        wait_ge<false>(F_RDYA, 1, dead);
        st128<SC1>(san4(o), rWS, (lane < 16 && (lane & 3) == 0) ? W_Q2 + (ks * 6144u + h1 * 192 + 96 * half + 16 * w + lane) * 4u : OOB);
        if (w == 0) TS(3);
    }

    if (w == 7) {
        wait_ge<false>(F_RDYA, 1, dead);
        u32x4 qv[4]; u32 spins = 0;
        for (;;) {
            CBAR();
            u32 mx = 0;
#pragma unroll
            for (int k = 0; k < 4; ++k) { qv[k] = ld128<SC1>(rWS, lane < 48 ? W_Q2 + (k * 6144u + r * 192u + 4 * lane) * 4u : OOB); mx = mx4(mx, qv[k]); }
            if (none_sent(mx)) break;
            POLL_STEP();
        }
        TS(4);
        u32x2 hi, lo; float qs[4];
#pragma unroll
        for (int e = 0; e < 4; ++e) qs[e] = (__uint_as_float(qv[0][e]) + __uint_as_float(qv[1][e])) + (__uint_as_float(qv[2][e]) + __uint_as_float(qv[3][e]));
        hi[0] = pk2(qs[0], qs[1]); hi[1] = pk2(qs[2], qs[3]);
        lo[0] = pk2(qs[0] - f_lo(hi[0]), qs[1] - f_hi(hi[0])); lo[1] = pk2(qs[2] - f_lo(hi[1]), qs[3] - f_hi(hi[1]));
        if (lane < 48) { lds_st<u32x2>(L_QH + lane * 8, hi); lds_st<u32x2>(L_QH + 384 + lane * 8, lo); }
        flag_st(F_QH, 1, lane);
    }
    if (w < 6) {
        wait_ge<true>(F_LAND, T_S2, dead);
        u32x4 bw[6];
        const u32 rowb = L_X + (16 * w + i16) * 384, sw = ((i16 >> 1) & 3) << 1;
#pragma unroll
        for (int t = 0; t < 6; ++t) bw[t] = lds_ld<u32x4>(rowb + (((4 * t + g) ^ sw) * 16));
        flag_add(F_S2DONE, lane);
        wait_ge<false>(F_QH, 1, dead);
        f32x4 a0 = {0.f, 0.f, 0.f, 0.f}, a1 = {0.f, 0.f, 0.f, 0.f};
#pragma unroll
        for (int t = 0; t < 6; t += 2) {
            a0 = mfma32(lds_ld<u32x4>(L_QH + (lane & 1) * 384 + 64 * t + 16 * g), bw[t], a0);
            a1 = mfma32(lds_ld<u32x4>(L_QH + (lane & 1) * 384 + 64 * (t + 1) + 16 * g), bw[t + 1], a1);
        }
        const float qa = ((a0[0] + a0[1]) + (a1[0] + a1[1])) * (0.07216878364870322f * 1.4426950408889634f);
        const u32 hi = bf1(qa), lo = bf1(qa - __uint_as_float(hi << 16));
        const u32x4 o = pack_row(g == 0 ? hi : lo);
        const u32 dst = W_QA + ((((r >> 4) * 8 + x) * 16 + (r & 15)) * 192u + (g & 1) * 96 + 16 * w + (lane & 8)) * 2u;
        st128<SC1>(o, rWS, (g < 2 && (lane & 7) == 0) ? dst : OOB);
        if (w == 0) TS(5);
    }

    const u32 jt = x + 8 * (r >> 1), g2 = r & 1;
    const u32 blk = L_A + BLK * w;
    {
        u32x4 bq[6];
        const u32 qoff = W_QA + (((g2 * 8 + w) * 16 + i16) * 192u + 8 * g) * 2u;
        wait_ge<false>(F_RDYA, 1, dead);
        CBAR();
#pragma unroll
        for (int k = 0; k < 6; ++k) bq[k] = ld128<SC1>(rWS, qoff + ((k / 3) * 96 + (k % 3) * 32) * 2);
        wait_ge<true>(F_LAND, T_TILE0 + 12 * (w + 1), dead);
        if (w == 0) TS(6);
        if (w == 7) TS(27);
        u32x4 av[4][3];
        const u32 ar = blk + i16 * 192, asw = ((i16 >> 2) & 1) << 1;
#pragma unroll
        for (int kb = 0; kb < 4; ++kb)
#pragma unroll
            for (int t = 0; t < 3; ++t) av[kb][t] = lds_ld<u32x4>(ar + kb * 16 * 192 + ((4 * t + g) ^ asw) * 16);
        u32 spins = 0;
        for (;;) {
            u32 mx = 0;
#pragma unroll
            for (int k = 0; k < 6; ++k) mx = mx4(mx, bq[k]);
            if (none_sent(mx)) break;
            POLL_STEP();
            CBAR();
#pragma unroll
            for (int k = 0; k < 6; ++k) bq[k] = ld128<SC1>(rWS, qoff + ((k / 3) * 96 + (k % 3) * 32) * 2);
        }
        if (w == 0) TS(7);
        if (w == 7) TS(26);
#pragma unroll
        for (int kb = 0; kb < 4; ++kb) {
            f32x4 acc = {0.f, 0.f, 0.f, 0.f};
#pragma unroll
            for (int t = 0; t < 3; ++t) { acc = mfma32(av[kb][t], bq[t], acc); acc = mfma32(av[kb][t], bq[3 + t], acc); }
#pragma unroll
            for (int e = 0; e < 4; ++e) lds_addf(L_SS + ((kb * 4 + e) * 64 + lane) * 4, acc[e]);
        }
        flag_add(F_SADD, lane);
        if (w == 0) TS(20);
    }
    {
        u32x4 vt[6][2];
        const u32 vb = blk + (4 * g + q4) * 192 + (p4 >> 1) * 16 + 8 * (p4 & 1);
#pragma unroll
        for (int u = 0; u < 6; ++u) {
            const u32 cu = vb + (u ^ (g & 1)) * 32;
#pragma unroll
            for (int t = 0; t < 2; ++t) vt[u][t] = cat(trd(cu + (32 * t) * 192), trd(cu + (32 * t + 16) * 192));
        }
        if (w == 0) TS(25);
        wait_ge<false>(F_SADD, 8, dead);
        if (w == 0) TS(21);
        float mloc = -INFINITY;
#pragma unroll
        for (int p = 0; p < 16; ++p) mloc = fmaxf(mloc, lds_ld<float>(L_SS + (p * 64 + lane) * 4));
        const float s0 = lds_ld<float>(L_SS + ((2 * w) * 64 + lane) * 4), s1 = lds_ld<float>(L_SS + ((2 * w + 1) * 64 + lane) * 4);
        mloc = xg_max(mloc);
        lds_st<u32>(L_PP + (w * 64 + lane) * 4, pk2(__builtin_amdgcn_exp2f(s0 - mloc), __builtin_amdgcn_exp2f(s1 - mloc)));
        flag_add(F_PCNT, lane);
        wait_ge<false>(F_PCNT, 8, dead);
        if (w == 0) TS(22);
        u32x4 pf[2];
#pragma unroll
        for (int t = 0; t < 2; ++t)
#pragma unroll
            for (int e = 0; e < 4; ++e) pf[t][e] = lds_ld<u32>(L_PP + ((4 * t + e) * 64 + lane) * 4);
        f32x4 oa[6];
#pragma unroll
        for (int u = 0; u < 6; ++u) { f32x4 z = {0.f, 0.f, 0.f, 0.f}; oa[u] = mfma32(vt[u][0], pf[0], z); oa[u] = mfma32(vt[u][1], pf[1], oa[u]); }
        flag_add(F_PVDONE, lane);
        if (w == 0) TS(23);
        wait_ge<false>(F_RDYB, 1, dead);
        const u32 hh = 16 * g2 + i16;
#pragma unroll
        for (int uu = 0; uu < 3; ++uu) {
            u32x4 o; o[0] = pk2(oa[2 * uu][0], oa[2 * uu][1]); o[1] = pk2(oa[2 * uu][2], oa[2 * uu][3]);
            o[2] = pk2(oa[2 * uu + 1][0], oa[2 * uu + 1][1]); o[3] = pk2(oa[2 * uu + 1][2], oa[2 * uu + 1][3]);
            st128<SC1>(san4(o), rWS, W_O + (((hh * 8 + w) * 128 + jt) * 96u + 32 * uu + 8 * g) * 2u);
        }
        if (w == 7) {
            u32x4 one; one[0] = 0x3F803F80u; one[1] = 0x3F803F80u; one[2] = 0x3F803F80u; one[3] = 0x3F803F80u;
            f32x4 z = {0.f, 0.f, 0.f, 0.f};
            z = mfma32(one, pf[0], z); z = mfma32(one, pf[1], z);
            u32x2 ml; ml[0] = san(__float_as_uint(mloc)); ml[1] = san(__float_as_uint(z[0]));
            st64<SC1>(ml, rWS, lane < 16 ? W_ML + (hh * 128 + jt) * 8u : OOB);
        }
        if (w == 0) TS(8);
    }

    {
        u32x4 ov[3], ma, mb0, mb1; u32 spins = 0;
        const u32 obase = W_O + ((r * 8 + x) * 128 + 16 * w) * 192u + lane * 16u;
        const u32 mlb = W_ML + r * 1024u;
        for (;;) {
            CBAR();
#pragma unroll
            for (int k = 0; k < 3; ++k) ov[k] = ld128<SC1>(rWS, obase + 1024 * k);
            ma = ld128<SC1>(rWS, mlb + lane * 16);
            mb0 = ld128<SC1>(rWS, mlb + (16 * w + 4 * g) * 8); mb1 = ld128<SC1>(rWS, mlb + (16 * w + 4 * g + 2) * 8);
            u32 mx = mx4(mx4(mx4(mx4(0, ov[0]), ov[1]), ov[2]), ma);
            mx = mx4(mx4(mx, mb0), mb1);
            if (none_sent(mx)) break;
            POLL_STEP();
        }
        if (w == 0) TS(9);
#pragma unroll
        for (int k = 0; k < 3; ++k) {
            const u32 ci = 64 * k + lane, row = ci / 12;
            lds_st<u32x4>(blk + ((ci ^ (((row >> 2) & 1) << 1)) * 16), ov[k]);
        }
        const float mA = __uint_as_float(ma[0]), lA = __uint_as_float(ma[1]), mB = __uint_as_float(ma[2]), lB = __uint_as_float(ma[3]);
        const float Mx = wave_max(fmaxf(mA, mB));
        float om[4];
        om[0] = __builtin_amdgcn_exp2f(__uint_as_float(mb0[0]) - Mx); om[1] = __builtin_amdgcn_exp2f(__uint_as_float(mb0[2]) - Mx);
        om[2] = __builtin_amdgcn_exp2f(__uint_as_float(mb1[0]) - Mx); om[3] = __builtin_amdgcn_exp2f(__uint_as_float(mb1[2]) - Mx);
        u32x2 af; { const u32 h0 = pk2(om[0], om[1]), h1b = pk2(om[2], om[3]);
            const u32 l0 = pk2(om[0] - f_lo(h0), om[1] - f_hi(h0)), l1 = pk2(om[2] - f_lo(h1b), om[3] - f_hi(h1b));
            af[0] = (lane & 1) ? l0 : h0; af[1] = (lane & 1) ? l1 : h1b; }
        const u32 tb = blk + (4 * g + q4) * 192 + (p4 >> 1) * 16 + 8 * (p4 & 1);
        const u32 amrow = 2 * w + (g & 1);
#pragma unroll
        for (int u = 0; u < 6; ++u) {
            f32x4 z = {0.f, 0.f, 0.f, 0.f};
            z = mfma16(af, trd(tb + (u ^ (g & 1)) * 32), z);
            const float v = z[0] + z[1];
            const u32 hi = bf1(v), lo = bf1(v - __uint_as_float(hi << 16));
            const u32 vi = 16 * u + i16;
            if (g < 2) lds_st<unsigned short>(L_AM + amrow * 192 + (((vi >> 3) ^ (((amrow >> 2) & 1) << 1)) * 16) + (vi & 7) * 2, (unsigned short)(g == 0 ? hi : lo));
        }
        flag_add(F_AM, lane);
        if (w < 6) {
            const float Lsum = wave_sum(__builtin_amdgcn_exp2f(mA - Mx) * lA + __builtin_amdgcn_exp2f(mB - Mx) * lB);
            const float rl = 1.f / Lsum;
            wait_ge<true>(F_LAND, T_UV, dead);
            u32x4 bw[2][3];
            const u32 sw = 2 * (g & 1) + (q4 >> 1);
#pragma unroll
            for (int d = 0; d < 2; ++d) {
                const u32 ub = L_X + (4 * g + q4) * 384 + (2 * ((2 * w + d) ^ sw) + (p4 >> 1)) * 16 + 8 * (p4 & 1);
#pragma unroll
                for (int t = 0; t < 3; ++t) bw[d][t] = cat(trd(ub + (32 * t) * 384), trd(ub + (32 * t + 16) * 384));
            }
            wait_ge<false>(F_AM, 8, dead);
            u32x4 aa[3];
#pragma unroll
            for (int t = 0; t < 3; ++t) aa[t] = lds_ld<u32x4>(L_AM + i16 * 192 + (((4 * t + g) ^ (((i16 >> 2) & 1) << 1)) * 16));
            float res[2];
#pragma unroll
            for (int d = 0; d < 2; ++d) {
                f32x4 z = {0.f, 0.f, 0.f, 0.f};
#pragma unroll
                for (int t = 0; t < 3; ++t) z = mfma32(aa[t], bw[d][t], z);
                res[d] = xg_sum((z[0] + z[1]) + (z[2] + z[3])) * rl;
            }
            const u32x4 o = pack_row(bf1(g == 0 ? res[0] : res[1]));
            const u32 dst = W_CTX + (x * 6144u + r * 192u + 16 * (2 * w + (g & 1)) + (lane & 8)) * 2u;
            st128<SC1>(o, rWS, (g < 2 && (lane & 7) == 0) ? dst : OOB);
            if (w == 0) TS(10);
        }
    }

    if (typeA) {
        wait_ge<true>(F_LAND, T_UV + 6 * (w + 1), dead);
        u32x4 bw[2][3];
        const u32 fbx = blk + B_W + (8 * g + q4) * 64 + (p4 >> 1) * 16 + 8 * (p4 & 1);
#pragma unroll
        for (int u = 0; u < 2; ++u) {
            const u32 ub = fbx + (u ^ (g & 1)) * 32;
#pragma unroll
            for (int t = 0; t < 3; ++t) bw[u][t] = cat(trd(ub + (32 * t) * 64), trd(ub + (32 * t + 4) * 64));
        }
        u32x4 ca[3]; u32 spins = 0;
        const u32 coff = i16 < 8 ? W_CTX + (i16 * 6144u + 768u * x + 96u * w + 8 * g) * 2u : OOB;
        for (;;) {
            CBAR();
            u32 mx = 0;
#pragma unroll
            for (int t = 0; t < 3; ++t) { ca[t] = ld128<SC1>(rWS, coff + 64 * t); mx = mx4(mx, ca[t]); }
            if (none_sent(mx)) break;
            POLL_STEP();
        }
        if (w == 0) TS(11);
        float res[2];
#pragma unroll
        for (int u = 0; u < 2; ++u) {
            f32x4 z = {0.f, 0.f, 0.f, 0.f};
#pragma unroll
            for (int t = 0; t < 3; ++t) z = mfma32(ca[t], bw[u][t], z);
            res[u] = xg_sum((z[0] + z[1]) + (z[2] + z[3]));
        }
        if (g < 2) lds_addf(L_FR + (16 * g + i16) * 4, g == 0 ? res[0] : res[1]);
        if (flag_add_ret(F_FRC, lane) == 7) {
            const float v = lds_ld<float>(L_FR + (lane & 31) * 4);
            const u32x4 o = pack_row(bf1(v));
            const u32 dst = W_AT + (x * 640u + 32u * r + 16 * (g & 1) + (lane & 8)) * 2u;
            st128<SC1>(o, rWS, (g < 2 && (lane & 7) == 0) ? dst : OOB);
            TS(12);
        }
    } else {
        const u32 fb = x * 12 + (r - 20);
        const u32 cA = 8 * (lane & 7) + (lane >> 3);
        const bool hasB = (lane & 7) < 2;
        const u32 colA = 80 * w + cA, colB = colA + 64;
        float ga[2], sb[2], cb1[2], gb[2], xv[2];
        {
            u32 mr[2][4][2]; u32 spins = 0;
            for (;;) {
                CBAR();
                u32 mx = 0;
#pragma unroll
                for (int kh = 0; kh < 2; ++kh)
#pragma unroll
                    for (int pm = 0; pm < 4; ++pm) {
                        mr[kh][pm][0] = ld32<SC1>(rWS, W_MR + (kh * 2560u + pm * 640u + colA) * 4u);
                        mr[kh][pm][1] = ld32<SC1>(rWS, hasB ? W_MR + (kh * 2560u + pm * 640u + colB) * 4u : OOB);
                        mx = umax(mx, umax(mr[kh][pm][0], mr[kh][pm][1]));
                    }
                if (none_sent(mx)) break;
                POLL_STEP();
            }
#pragma unroll
            for (int c = 0; c < 2; ++c) {
                ga[c] = __uint_as_float(mr[0][0][c]) + __uint_as_float(mr[1][0][c]);
                sb[c] = __uint_as_float(mr[0][1][c]) + __uint_as_float(mr[1][1][c]);
                cb1[c] = 1.f + (__uint_as_float(mr[0][2][c]) + __uint_as_float(mr[1][2][c]));
                gb[c] = __uint_as_float(mr[0][3][c]) + __uint_as_float(mr[1][3][c]);
            }
            xv[0] = __uint_as_float(ld16(rX, colA * 2) << 16); xv[1] = __uint_as_float(ld16(rX, hasB ? colB * 2 : OOB) << 16);
        }
        if (w == 0) TS(13);
        const u32 gab = blk + B_GA;
#pragma unroll
        for (int c = 0; c < 2; ++c) {
            if (c == 0 || hasB) {
                const u32 cc = cA + 64 * c;
                const u32 h = bf1(cb1[c]), l = bf1(cb1[c] - __uint_as_float(h << 16)), h2 = bf1(sb[c]), l2 = bf1(sb[c] - __uint_as_float(h2 << 16));
                lds_st<unsigned short>(gab + 2 * 160 + cc * 2, (unsigned short)h); lds_st<unsigned short>(gab + 3 * 160 + cc * 2, (unsigned short)l);
                lds_st<unsigned short>(gab + 4 * 160 + cc * 2, (unsigned short)h2); lds_st<unsigned short>(gab + 5 * 160 + cc * 2, (unsigned short)l2);
                lds_st<float>(blk + B_GB + cc * 4, gb[c]);
            }
        }
        wait_ge<true>(F_LAND, T_UV + 6 * (w + 1), dead);
        u32x2 b1[5], b2[5];
#pragma unroll
        for (int t = 0; t < 5; ++t) b1[t] = trd(blk + B_W + 512 * t + 8 * lane);
#pragma unroll
        for (int u = 0; u < 5; ++u) b2[u] = trd(blk + B_W2 + (4 * g + q4) * 160 + 32 * u + 8 * p4);
        u32x4 at0, at1; u32 spins = 0;
        const u32 aoff = W_AT + ((lane >> 3) * 640u + 80u * w + 8 * (lane & 7)) * 2u;
        for (;;) {
            CBAR();
            at0 = ld128<SC1>(rWS, aoff); at1 = ld128<SC1>(rWS, hasB ? aoff + 128 : OOB);
            if (none_sent(mx4(mx4(0, at0), at1))) break;
            POLL_STEP();
        }
        if (w == 0) TS(14);
        float x1[2];
#pragma unroll
        for (int c = 0; c < 2; ++c) {
            const u32x4 a = c ? at1 : at0;
            float v[8];
#pragma unroll
            for (int j = 0; j < 4; ++j) { v[2 * j] = f_lo(a[j]); v[2 * j + 1] = f_hi(a[j]); }
            float t4[4], t2[2];
#pragma unroll
            for (int j = 0; j < 4; ++j) t4[j] = sc32(v[j], v[j + 4]);
#pragma unroll
            for (int j = 0; j < 2; ++j) t2[j] = sc16(t4[j], t4[j + 2]);
            const bool b3 = (lane & 8) != 0;
            const float keep = b3 ? t2[1] : t2[0], give = b3 ? t2[0] : t2[1];
            const float attn = keep + dppf<ROR(8)>(give);
            x1[c] = xv[c] + ga[c] * attn;
        }
        if (!hasB) x1[1] = 0.f;
#pragma unroll
        for (int c = 0; c < 2; ++c) {
            if (c == 0 || hasB) {
                const u32 cc = cA + 64 * c;
                const float uv = x1[c] * cb1[c];
                const u32 h = bf1(uv), l = bf1(uv - __uint_as_float(h << 16));
                lds_st<unsigned short>(gab + cc * 2, (unsigned short)h); lds_st<unsigned short>(gab + 160 + cc * 2, (unsigned short)l);
                lds_st<float>(blk + B_X1 + cc * 4, x1[c]);
            }
        }
        CBAR();
        f32x4 z = {0.f, 0.f, 0.f, 0.f};
        const u32 arow = (lane & 7) < 6 ? (lane & 7) : 0;
#pragma unroll
        for (int t = 0; t < 5; ++t) z = mfma16(lds_ld<u32x2>(gab + arow * 160 + 32 * t + 8 * g), b1[t], z);
        const float s1 = wave_sum(x1[0] + x1[1]), s2 = wave_sum(x1[0] * x1[0] + x1[1] * x1[1]);
        if (g == 0) { lds_addf(L_GR + i16 * 4, z[0] + z[1]); lds_addf(L_GR + 64 + i16 * 4, z[2] + z[3]); }
        if (g == 1) lds_addf(L_GR + 128 + i16 * 4, z[0] + z[1]);
        if (lane == 32) { lds_addf(L_GR + 192, s1); lds_addf(L_GR + 196, s2); }
        flag_add(F_GRC, lane);
        wait_ge<false>(F_GRC, 8, dead);
        const f32x4 U = lds_ld<f32x4>(L_GR + 16 * g), V = lds_ld<f32x4>(L_GR + 64 + 16 * g), SB = lds_ld<f32x4>(L_GR + 128 + 16 * g);
        const f32x2 st = lds_ld<f32x2>(L_GR + 192);
        const float mu = st[0] * (1.f / 640.f);
        const float var = fmaxf(st[1] * (1.f / 640.f) - mu * mu, 0.f);
        const float rstd = 1.f / sqrtf(var + 1e-5f);
        float av[4];
#pragma unroll
        for (int e = 0; e < 4; ++e) av[e] = fmaxf(rstd * (U[e] - mu * V[e]) + SB[e], 0.f);
        u32x2 af; { const u32 h0 = pk2(av[0], av[1]), h1b = pk2(av[2], av[3]);
            const u32 l0 = pk2(av[0] - f_lo(h0), av[1] - f_hi(h0)), l1 = pk2(av[2] - f_lo(h1b), av[3] - f_hi(h1b));
            af[0] = (lane & 1) ? l0 : h0; af[1] = (lane & 1) ? l1 : h1b; }
        float fo[5];
#pragma unroll
        for (int u = 0; u < 5; ++u) { f32x4 y = {0.f, 0.f, 0.f, 0.f}; y = mfma16(af, b2[u], y); fo[u] = y[0] + y[1]; }
        {
            const float v = g == 0 ? fo[0] : (g == 1 ? fo[1] : (g == 2 ? fo[2] : fo[3]));
            const u32x4 o = pack_row(bf1(v)), o2 = pack_row(bf1(fo[4]));
            st128<SC1>(o, rWS, (lane & 7) == 0 ? W_FP + (((5 * w + g) * 96u + fb) * 16 + (lane & 8)) * 2u : OOB);
            st128<SC1>(o2, rWS, (lane & 55) == 0 ? W_FP + (((5 * w + 4) * 96u + fb) * 16 + (lane & 8)) * 2u : OOB);
        }
        if (w == 0) TS(15);
        const int ci = (r - 20) * 8 + x;
        if (ci < 40 && ci / 5 == w) {
            u32x4 fv[3]; u32 sp2 = 0;
            for (;;) {
                CBAR();
                u32 mx = 0;
#pragma unroll
                for (int k = 0; k < 3; ++k) { fv[k] = ld128<SC1>(rWS, W_FP + (u32)ci * 3072u + 1024 * k + lane * 16); mx = mx4(mx, fv[k]); }
                if (none_sent(mx)) break;
                if (++sp2 > CAP_G) { dead = true; break; }
                __builtin_amdgcn_s_sleep(1);
            }
            TS(16);
            float v[8];
#pragma unroll
            for (int j = 0; j < 4; ++j) { v[2 * j] = (f_lo(fv[0][j]) + f_lo(fv[1][j])) + f_lo(fv[2][j]); v[2 * j + 1] = (f_hi(fv[0][j]) + f_hi(fv[1][j])) + f_hi(fv[2][j]); }
            float t4[4], t2[2];
#pragma unroll
            for (int j = 0; j < 4; ++j) t4[j] = sc32(v[j], v[j + 4]);
#pragma unroll
            for (int j = 0; j < 2; ++j) t2[j] = sc16(t4[j], t4[j + 2]);
            const bool b3 = (lane & 8) != 0;
            const float keep = b3 ? t2[1] : t2[0], give = b3 ? t2[0] : t2[1];
            float f = keep + dppf<ROR(8)>(give);
            f += dppf<SHL(4)>(f);
            f += dppf<SHL(2)>(f);
            const u32 cc = 16 * (ci % 5) + 8 * (lane & 1) + (lane >> 3);
            const float o = lds_ld<float>(blk + B_X1 + cc * 4) + lds_ld<float>(blk + B_GB + cc * 4) * f;
            const rsrc_t rO = mkrs(p_out, M_ * 2);
            __builtin_amdgcn_raw_buffer_store_b16((unsigned short)bf1(o), rO, (lane & 6) == 0 ? (16 * ci + 8 * (lane & 1) + (lane >> 3)) * 2 : OOB, 0, 0);
            if (ci == 0 && lane == 0) { u32x2 e; e[0] = (u32)tag; e[1] = (u32)(tag >> 32); st64<SC1>(e, rWS, W_EPOCH); }
            TS(17);
        }
    }
}

extern "C" void kernel_launch(void* const* d_in, const int* in_sizes, int n_in, void* d_out, int out_size, void* d_ws, size_t ws_size, hipStream_t stream)
{
    (void)in_sizes; (void)n_in; (void)out_size; (void)ws_size;
    (void)hipFuncSetAttribute((const void*)adaln_mla_ffn_kernel, hipFuncAttributeMaxDynamicSharedMemorySize, (int)LDS_BYTES);
    hipLaunchKernelGGL(adaln_mla_ffn_kernel, dim3(256), dim3(640), LDS_BYTES, stream,
                       (const void*)d_in[0], (const void*)d_in[1], (const void*)d_in[2], (const void*)d_in[3], (const void*)d_in[4], (const void*)d_in[5],
                       (const void*)d_in[7], (const void*)d_in[8], (const void*)d_in[9], (const void*)d_in[10], d_out, d_ws);
}
```

```cpp
#include <hip/hip_runtime.h>
#include <hip/hip_bf16.h>
#include <stdint.h>

#define LAS __attribute__((address_space(3)))
#define DI __device__ __forceinline__
typedef unsigned int u32;
typedef unsigned long long u64;
typedef u32 u32x2 __attribute__((ext_vector_type(2)));
typedef u32 u32x4 __attribute__((ext_vector_type(4)));
typedef float f32x2 __attribute__((ext_vector_type(2)));
typedef float f32x4 __attribute__((ext_vector_type(4)));
typedef short s16x4 __attribute__((ext_vector_type(4)));
typedef __bf16 bf16x2 __attribute__((ext_vector_type(2)));
typedef __bf16 bf16x8 __attribute__((ext_vector_type(8)));
typedef __amdgpu_buffer_rsrc_t rsrc_t;

#define WIN 24
#define NTW 1

constexpr int M_ = 640, H_ = 32, D_ = 192, DC = 768, SK = 8192, DFF = 1536;
constexpr u32 SENT = 0xFFFFFFFFu;
constexpr u32 OOB = 0x80000000u;

constexpr u32 W_EPOCH = 0;
constexpr u32 W_FA = 256;
constexpr u32 W_FB = W_FA + 2048;
constexpr u32 W_G1 = W_FB + 2048;
constexpr u32 W_SMALL = 16384;
constexpr u32 W_Q2 = W_SMALL;
constexpr u32 W_QA = W_Q2 + 98304;
constexpr u32 W_ML = W_QA + 98304;
constexpr u32 W_CTX = W_ML + 32768;
constexpr u32 W_AT = W_CTX + 98304;
constexpr u32 W_FP = W_AT + 10240;
constexpr u32 W_MR = W_FP + 122880;
constexpr u32 W_SMALL_END = W_MR + 20480;
constexpr u32 SMALL_CH = (W_SMALL_END - W_SMALL) / 16;
constexpr u32 SMALL_PER = (SMALL_CH + 255) / 256;
constexpr u32 W_O = 524288;
constexpr u32 O_BYTES = 32u * 8 * 128 * 96 * 2;
constexpr u32 W_PROF = W_O + O_BYTES;
constexpr u32 W_END = W_PROF + 256 * 64 * 8;
static_assert(W_SMALL_END <= W_O, "ws map");
static_assert(SMALL_PER <= 128, "prefill");

constexpr u32 L_A = 0;
constexpr u32 BLK = 12288;
constexpr u32 L_S1W = 5 * BLK;
constexpr u32 L_X = 98304;
constexpr u32 L_Y = 135168;
constexpr u32 L_FLG = L_Y;
constexpr u32 L_S0A = L_Y + 256;
constexpr u32 L_FR = L_Y + 320;
constexpr u32 L_GR = L_Y + 512;
constexpr u32 L_SS = L_Y + 1024;
constexpr u32 L_ZEND = L_Y + 5120;
constexpr u32 L_PP = L_Y + 5120;
constexpr u32 L_HX = L_Y + 7168;
constexpr u32 L_QH = L_Y + 7936;
constexpr u32 L_AM = L_Y + 8704;
constexpr u32 L_SIL = L_Y + 11776;
constexpr u32 LDS_BYTES = 163840;
constexpr u32 B_W = 3072, B_W2 = 6144, B_GA = 9216, B_X1 = 9216 + 1280, B_GB = B_X1 + 320;

enum { F_LAND = 0, F_RDYA, F_RDYB, F_S0CNT, F_HX, F_S1DONE, F_QH, F_S2DONE, F_SADD, F_PCNT, F_PVDONE, F_AM, F_FRC, F_GRC };

constexpr u32 CAP_G = 20000;
constexpr u32 CAP_L = 400000;

constexpr u32 T_S1 = 30, T_S2 = 66, T_TILE0 = 66, T_TILE = 162, T_UV = 198;

DI rsrc_t mkrs(const void* p, u32 bytes) { return __builtin_amdgcn_make_buffer_rsrc((void*)p, (short)0, (int)bytes, 0x00020000); }
template <int AUX> DI u32x4 ld128(rsrc_t r, u32 off) { return __builtin_amdgcn_raw_buffer_load_b128(r, off, 0, AUX); }
template <int AUX> DI u32x2 ld64(rsrc_t r, u32 off) { return __builtin_amdgcn_raw_buffer_load_b64(r, off, 0, AUX); }
template <int AUX> DI u32 ld32(rsrc_t r, u32 off) { return __builtin_amdgcn_raw_buffer_load_b32(r, off, 0, AUX); }
DI u32 ld16(rsrc_t r, u32 off) { return (u32)__builtin_amdgcn_raw_buffer_load_b16(r, off, 0, 0); }
template <int AUX> DI void st128(u32x4 v, rsrc_t r, u32 off) { __builtin_amdgcn_raw_buffer_store_b128(v, r, off, 0, AUX); }
template <int AUX> DI void st64(u32x2 v, rsrc_t r, u32 off) { __builtin_amdgcn_raw_buffer_store_b64(v, r, off, 0, AUX); }
constexpr int SC1 = 16;

DI float f_lo(u32 v) { return __uint_as_float(v << 16); }
DI float f_hi(u32 v) { return __uint_as_float(v & 0xFFFF0000u); }
DI u32 pk2(float a, float b) { bf16x2 v; v[0] = (__bf16)a; v[1] = (__bf16)b; return __builtin_bit_cast(u32, v); }
DI u32 bf1(float a) { return pk2(a, 0.f) & 0xFFFFu; }
DI u32 san(u32 v) { return v < 0xFFFFFFFEu ? v : 0xFFFFFFFEu; }
DI u32x4 san4(u32x4 v) { u32x4 r; r[0] = san(v[0]); r[1] = san(v[1]); r[2] = san(v[2]); r[3] = san(v[3]); return r; }
DI u32 umax(u32 a, u32 b) { return a > b ? a : b; }
DI u32 mx4(u32 acc, u32x4 v) { return umax(umax(acc, umax(v[0], v[1])), umax(v[2], v[3])); }

template <int CTRL> DI u32 dppu(u32 v) { return (u32)__builtin_amdgcn_update_dpp(0, (int)v, CTRL, 0xF, 0xF, true); }
template <int CTRL> DI float dppf(float v) { return __uint_as_float(dppu<CTRL>(__float_as_uint(v))); }
constexpr int ROR(int n) { return 0x120 + n; }
constexpr int SHL(int n) { return 0x100 + n; }
DI float row_sum(float v) { v += dppf<ROR(8)>(v); v += dppf<ROR(4)>(v); v += dppf<ROR(2)>(v); v += dppf<ROR(1)>(v); return v; }
DI float row_max(float v) { v = fmaxf(v, dppf<ROR(8)>(v)); v = fmaxf(v, dppf<ROR(4)>(v)); v = fmaxf(v, dppf<ROR(2)>(v)); v = fmaxf(v, dppf<ROR(1)>(v)); return v; }
DI void swap16(float& a, float& b) { auto r = __builtin_amdgcn_permlane16_swap(__float_as_uint(a), __float_as_uint(b), false, false); a = __uint_as_float(r[0]); b = __uint_as_float(r[1]); }
DI void swap32(float& a, float& b) { auto r = __builtin_amdgcn_permlane32_swap(__float_as_uint(a), __float_as_uint(b), false, false); a = __uint_as_float(r[0]); b = __uint_as_float(r[1]); }
DI float xg_sum(float v) { float a = v, b = v; swap16(a, b); v = a + b; a = v; b = v; swap32(a, b); return a + b; }
DI float xg_max(float v) { float a = v, b = v; swap16(a, b); v = fmaxf(a, b); a = v; b = v; swap32(a, b); return fmaxf(a, b); }
DI float wave_sum(float v) { return xg_sum(row_sum(v)); }
DI float wave_max(float v) { return xg_max(row_max(v)); }
DI float sc32(float a, float b) { swap32(a, b); return a + b; }
DI float sc16(float a, float b) { swap16(a, b); return a + b; }

DI f32x4 mfma32(u32x4 a, u32x4 b, f32x4 c) { return __builtin_amdgcn_mfma_f32_16x16x32_bf16(__builtin_bit_cast(bf16x8, a), __builtin_bit_cast(bf16x8, b), c, 0, 0, 0); }
DI f32x4 mfma16(u32x2 a, u32x2 b, f32x4 c) { return __builtin_amdgcn_mfma_f32_16x16x16bf16_1k(__builtin_bit_cast(s16x4, a), __builtin_bit_cast(s16x4, b), c, 0, 0, 0); }
DI u32x2 trd(u32 addr) { return __builtin_bit_cast(u32x2, __builtin_amdgcn_ds_read_tr16_b64_v4i16((LAS s16x4*)addr)); }
DI u32x4 cat(u32x2 a, u32x2 b) { u32x4 r; r[0] = a[0]; r[1] = a[1]; r[2] = b[0]; r[3] = b[1]; return r; }
template <class T> DI T lds_ld(u32 addr) { return *(LAS T*)addr; }
template <class T> DI void lds_st(u32 addr, T v) { *(LAS T*)addr = v; }
DI void lds_addf(u32 addr, float v) { __hip_atomic_fetch_add((LAS float*)addr, v, __ATOMIC_RELAXED, __HIP_MEMORY_SCOPE_WORKGROUP); }
#define CBAR() asm volatile("" ::: "memory")

DI u32 flag_ld(int i) { return (u32)__builtin_amdgcn_readfirstlane((int)*(volatile LAS u32*)(L_FLG + 4 * i)); }
DI void flag_st(int i, u32 v, int lane) { CBAR(); if (lane == 0) *(volatile LAS u32*)(L_FLG + 4 * i) = v; CBAR(); }
DI void flag_add(int i, int lane) { CBAR(); if (lane == 0) __hip_atomic_fetch_add((LAS u32*)(L_FLG + 4 * i), 1u, __ATOMIC_RELAXED, __HIP_MEMORY_SCOPE_WORKGROUP); CBAR(); }
DI u32 flag_add_ret(int i, int lane) {
    CBAR(); u32 o = 0;
    if (lane == 0) o = __hip_atomic_fetch_add((LAS u32*)(L_FLG + 4 * i), 1u, __ATOMIC_RELAXED, __HIP_MEMORY_SCOPE_WORKGROUP);
    o = (u32)__builtin_amdgcn_readfirstlane((int)o); CBAR(); return o;
}
template <bool SLEEP> DI void wait_ge(int i, u32 target, bool& dead) {
    u32 n = 0;
    while (!dead && flag_ld(i) < target) { if (++n > CAP_L) dead = true; if (SLEEP) __builtin_amdgcn_s_sleep(1); }
    CBAR();
}
DI bool none_sent(u32 mx) { return __builtin_amdgcn_ballot_w64(mx == SENT) == 0ull; }
#define POLL_STEP() { if (++spins > CAP_G) { dead = true; break; } __builtin_amdgcn_s_sleep(1); }

#define TS(i) do { } while (0)

DI u32x4 pack_row(u32 v16) {
    u32 pr = v16 | (dppu<SHL(1)>(v16) << 16);
    u32x4 r; r[0] = pr; r[1] = dppu<SHL(2)>(pr); r[2] = dppu<SHL(4)>(pr); r[3] = dppu<SHL(6)>(pr);
    return san4(r);
}

template <int NT> DI void dma(u32 lds, u32 voff, rsrc_t rs, u32 soff) {
    if (NT) asm volatile("s_nop 4\n\ts_mov_b32 m0, %0\n\ts_nop 0\n\tbuffer_load_dwordx4 %1, %2, %3 offen nt lds" ::"s"(lds), "v"(voff), "s"(rs), "s"(soff) : "memory");
    else asm volatile("s_nop 4\n\ts_mov_b32 m0, %0\n\ts_nop 0\n\tbuffer_load_dwordx4 %1, %2, %3 offen lds" ::"s"(lds), "v"(voff), "s"(rs), "s"(soff) : "memory");
}
template <int N> DI void waitvm() { asm volatile("s_waitcnt vmcnt(%0)" ::"i"(N) : "memory"); }
struct Ldr { int issued; int lane; bool go; bool dead; };
DI void publish(Ldr& s, int landed) { if (s.go && landed > 0) flag_st(F_LAND, (u32)landed, s.lane); }
DI void step3(Ldr& s) { s.issued += 3; waitvm<WIN>(); publish(s, s.issued - WIN); }
template <int N> DI void drain(Ldr& s) { waitvm<N>(); publish(s, s.issued - N); if constexpr (N >= 3) drain<N - 3>(s); }
DI void park(Ldr& s, int flag, u32 target) {
    if (flag_ld(flag) < target) { drain<WIN - 3>(s); wait_ge<true>(flag, target, s.dead); }
    CBAR();
}
template <int NT> DI void seg3(Ldr& s, u32 lds0, const u32 (&v)[3], rsrc_t rs, u32 soff0, u32 sstep, int ng) {
    for (int g = 0; g < ng; ++g) {
        const u32 so = soff0 + (u32)g * sstep, ld = lds0 + 3072u * (u32)g;
        dma<NT>(ld, v[0], rs, so); dma<NT>(ld + 1024, v[1], rs, so); dma<NT>(ld + 2048, v[2], rs, so);
        step3(s);
    }
}

extern __shared__ __attribute__((aligned(16))) char smem_dyn[];

__global__ void __launch_bounds__(640, 3)
adaln_mla_ffn_kernel(const void* __restrict__ p_x, const void* __restrict__ p_ckv, const void* __restrict__ p_wq, const void* __restrict__ p_wuk,
                     const void* __restrict__ p_wuv, const void* __restrict__ p_wo, const void* __restrict__ p_w1, const void* __restrict__ p_w2,
                     const void* __restrict__ p_c, const void* __restrict__ p_wmod, void* __restrict__ p_out, void* __restrict__ p_ws)
{
    const int tid = threadIdx.x, lane = tid & 63;
    const int wave = __builtin_amdgcn_readfirstlane(tid >> 6);
    const int b = blockIdx.x, x = b & 7, r = b >> 3;
    const bool typeA = r < 20;
    const int g = lane >> 4, i16 = lane & 15, q4 = i16 >> 2, p4 = lane & 3;
    bool dead = false;
    const rsrc_t rWS = mkrs(p_ws, W_END);
    (void)smem_dyn;
    if (wave == 0) TS(30);
    if (wave == 9) TS(31);

    if (wave == 8) {
        Ldr s; s.issued = 0; s.lane = lane; s.go = false; s.dead = false;
        u32 v1[3], vt[3], vu[3];
#pragma unroll
        for (int k = 0; k < 3; ++k) {
            const u32 p = 64 * k + lane;
            const u32 row = p / 12, ch = (p % 12) ^ (((row >> 2) & 1) << 1);
            v1[k] = row * 12288u + ch * 16; vt[k] = row * 1536u + ch * 16;
            const u32 r2 = p / 24, c2 = (p % 24) ^ (((r2 >> 1) & 3) << 1);
            vu[k] = r2 * 12288u + c2 * 16;
        }
        const rsrc_t rq = mkrs(p_wq, M_ * H_ * D_ * 2), ruk = mkrs(p_wuk, DC * H_ * D_ * 2), ruv = mkrs(p_wuv, DC * H_ * D_ * 2), rkv = mkrs(p_ckv, SK * DC * 2);
        const u32 ks = r >> 3, h1 = 4 * x + ((r >> 1) & 3), half = r & 1;
        const u32 so1 = 160u * ks * 12288u + (h1 * 192 + 96 * half) * 2;
        seg3<NTW>(s, L_S1W, v1, rq, so1, 16u * 12288u, (WIN / 3) < 10 ? (WIN / 3) : 10);
        __builtin_amdgcn_s_barrier();
        s.go = true;
        if ((WIN / 3) < 10) seg3<NTW>(s, L_S1W + 3072u * (WIN / 3), v1, rq, so1 + (WIN / 3) * 16u * 12288u, 16u * 12288u, 10 - (WIN / 3));
        const u32 so2 = 96u * x * 12288u + (u32)r * 384u;
        seg3<NTW>(s, L_X, vu, ruk, so2, 8u * 12288u, 12);
        const u32 jt = x + 8 * (r >> 1);
        const u32 sot = 64u * jt * 1536u;
        for (int w = 0; w < 5; ++w) seg3<NTW>(s, L_A + BLK * w, vt, rkv, sot + 192u * w, 16u * 1536u, 4);
        park(s, F_S1DONE, 6);
        for (int w = 5; w < 8; ++w) seg3<NTW>(s, L_A + BLK * w, vt, rkv, sot + 192u * w, 16u * 1536u, 4);
        park(s, F_S2DONE, 6);
        seg3<NTW>(s, L_X, vu, ruv, so2, 8u * 12288u, 12);
        park(s, F_PVDONE, 8);
        if (typeA) {
            const rsrc_t ro = mkrs(p_wo, H_ * D_ * M_ * 2);
            const u32 row = lane >> 2, ch = (lane & 3) ^ (((row >> 3) & 1) << 1);
            const u32 vf = row * 1280u + ch * 16;
            for (int w = 0; w < 8; ++w) {
                const u32 so = (768u * x + 96u * w) * 1280u + 64u * r, ld = L_A + BLK * w + B_W;
                for (int k = 0; k < 6; k += 3) {
                    dma<NTW>(ld + 1024 * k, vf, ro, so + 16u * 1280u * k); dma<NTW>(ld + 1024 * (k + 1), vf, ro, so + 16u * 1280u * (k + 1));
                    dma<NTW>(ld + 1024 * (k + 2), vf, ro, so + 16u * 1280u * (k + 2));
                    step3(s);
                }
            }
        } else {
            const rsrc_t r1 = mkrs(p_w1, M_ * DFF * 2), r2 = mkrs(p_w2, DFF * M_ * 2);
            const u32 fb = x * 12 + (r - 20);
            const u32 va = (lane >> 1) * 3072u + (lane & 1) * 16, va2 = lane < 32 ? va : OOB;
            u32 vb[3];
#pragma unroll
            for (int k = 0; k < 3; ++k) { const u32 p = 64 * k + lane; vb[k] = p < 160 ? (p / 10) * 1280u + (p % 10) * 16 : OOB; }
            for (int w = 0; w < 8; ++w) {
                const u32 ld = L_A + BLK * w;
                const u32 sa = 80u * w * 3072u + fb * 32u, sb = 16u * fb * 1280u + 160u * w;
                dma<NTW>(ld + B_W, va, r1, sa); dma<NTW>(ld + B_W + 1024, va, r1, sa + 32u * 3072u); dma<NTW>(ld + B_W + 2048, va2, r1, sa + 64u * 3072u);
                step3(s);
                dma<NTW>(ld + B_W2, vb[0], r2, sb); dma<NTW>(ld + B_W2 + 1024, vb[1], r2, sb); dma<NTW>(ld + B_W2 + 2048, vb[2], r2, sb);
                step3(s);
            }
        }
        drain<WIN - 3>(s);
        return;
    }

    const u32x2 ep = ld64<SC1>(rWS, W_EPOCH);

    if (wave == 9) {
        const u64 tag = (((u64)ep[1] << 32) | ep[0]) + 1ull;
        u32x2 tg; tg[0] = (u32)tag; tg[1] = (u32)(tag >> 32);
        u32x4 sv; sv[0] = SENT; sv[1] = SENT; sv[2] = SENT; sv[3] = SENT;
        {
            const u32 c0 = (u32)b * SMALL_PER + lane, c1 = c0 + 64;
            st128<SC1>(sv, rWS, c0 < SMALL_CH ? W_SMALL + c0 * 16 : OOB);
            st128<SC1>(sv, rWS, (lane + 64 < (int)SMALL_PER && c1 < SMALL_CH) ? W_SMALL + c1 * 16 : OOB);
        }
        waitvm<0>();
        if (lane == 0) st64<SC1>(tg, rWS, W_FA + 8 * b);
#pragma unroll
        for (int k = 0; k < 24; ++k) st128<SC1>(sv, rWS, W_O + (u32)b * 24576u + k * 1024u + lane * 16u);
        __builtin_amdgcn_s_barrier();
        waitvm<0>();
        if (lane == 0) st64<SC1>(tg, rWS, W_FB + 8 * b);
        for (int pass = 0; pass < 2; ++pass) {
            const u32 base = pass ? W_FB : W_FA;
            u32 spins = 0;
            for (;;) {
                CBAR();
                const u32x4 a = ld128<SC1>(rWS, base + lane * 32), c = ld128<SC1>(rWS, base + lane * 32 + 16);
                const bool ok = a[0] == tg[0] && a[1] == tg[1] && a[2] == tg[0] && a[3] == tg[1] && c[0] == tg[0] && c[1] == tg[1] && c[2] == tg[0] && c[3] == tg[1];
                if (__builtin_amdgcn_ballot_w64(!ok) == 0ull) break;
                POLL_STEP();
            }
            flag_st(pass ? F_RDYB : F_RDYA, 1, lane);
            TS(40 + pass);
        }
        const int it = 255 - b;
        if (it < 160) {
            const int cb = it >> 1, kh = it & 1;
            const rsrc_t rc = mkrs(p_c, M_ * 2), rm = mkrs(p_wmod, M_ * 6 * M_ * 2);
#pragma unroll
            for (int k = 0; k < 5; ++k) {
                const float cv = __uint_as_float(ld16(rc, (320 * kh + 64 * k + lane) * 2) << 16);
                lds_st<float>(L_SIL + (64 * k + lane) * 4, cv * __builtin_amdgcn_rcpf(1.f + __expf(-cv)));
            }
            wait_ge<true>(F_LAND, T_TILE, dead);
            TS(42);
            u32x4 wv[20];
            const u32 vo = (u32)(320 * kh + (lane >> 2)) * 7680u + (1280u + 32u * cb) * 2u + (lane & 3) * 16u;
#pragma unroll
            for (int k = 0; k < 20; ++k) wv[k] = ld128<0>(rm, vo + (u32)k * 16u * 7680u);
            float acc[8];
#pragma unroll
            for (int j = 0; j < 8; ++j) acc[j] = 0.f;
#pragma unroll
            for (int k = 0; k < 20; ++k) {
                const float sl = lds_ld<float>(L_SIL + (16 * k + (lane >> 2)) * 4);
#pragma unroll
                for (int j = 0; j < 4; ++j) { acc[2 * j] += sl * f_lo(wv[k][j]); acc[2 * j + 1] += sl * f_hi(wv[k][j]); }
            }
            float t4[4], t2[2];
#pragma unroll
            for (int j = 0; j < 4; ++j) t4[j] = sc32(acc[j], acc[j + 4]);
#pragma unroll
            for (int j = 0; j < 2; ++j) t2[j] = sc16(t4[j], t4[j + 2]);
#pragma unroll
            for (int j = 0; j < 2; ++j) { t2[j] += dppf<ROR(8)>(t2[j]); t2[j] += dppf<ROR(4)>(t2[j]); }
            u32x2 o; o[0] = san(__float_as_uint(t2[0])); o[1] = san(__float_as_uint(t2[1]));
            const u32 col = 32u * cb + 8 * (lane & 3) + 4 * (lane >> 5) + 2 * ((lane >> 4) & 1);
            st64<SC1>(o, rWS, (lane & 12) == 0 ? W_MR + (kh * 2560u + col) * 4u : OOB);
            TS(43);
        }
        return;
    }

    const int w = wave;
    const rsrc_t rX = mkrs(p_x, M_ * 2);
    u32x4 m0v, m1v; u32 c0v = 0, c1v = 0;
    const bool doS0 = typeA && w < 5;
    if (doS0) {
        const rsrc_t rc = mkrs(p_c, M_ * 2), rm = mkrs(p_wmod, M_ * 6 * M_ * 2);
        const u32 row0 = 128 * w + lane, colb = (x * 160 + 8 * r) * 2;
        m0v = ld128<0>(rm, row0 * 7680u + colb); m1v = ld128<0>(rm, (row0 + 64) * 7680u + colb);
        c0v = ld16(rc, row0 * 2); c1v = ld16(rc, (row0 + 64) * 2);
    }
    u32 xs[5]; u32 xr[3];
    const u32 ks = r >> 3;
    if (w == 7) {
#pragma unroll
        for (int k = 0; k < 5; ++k) xs[k] = ld32<0>(rX, (64 * k + lane) * 4);
#pragma unroll
        for (int k = 0; k < 3; ++k) xr[k] = ld16(rX, (64 * k + lane) < 160 ? (160 * ks + 64 * k + lane) * 2 : OOB);
    }
    if (tid < (int)((L_ZEND - L_Y) / 16)) { u32x4 z; z[0] = 0; z[1] = 0; z[2] = 0; z[3] = 0; lds_st<u32x4>(L_Y + tid * 16, z); }
    asm volatile("s_waitcnt lgkmcnt(0)" ::: "memory");
    __builtin_amdgcn_s_barrier();
    CBAR();
    const u64 tag = (((u64)ep[1] << 32) | ep[0]) + 1ull;
    const u32 tagl = (u32)tag & 0xFFu, tagh = (u32)(tag >> 8);
    if (w == 0) TS(0);

    if (doS0) {
        const float cA = __uint_as_float(c0v << 16), cB = __uint_as_float(c1v << 16);
        const float sA = cA * __builtin_amdgcn_rcpf(1.f + __expf(-cA)), sB = cB * __builtin_amdgcn_rcpf(1.f + __expf(-cB));
        float acc[8];
#pragma unroll
        for (int j = 0; j < 4; ++j) { acc[2 * j] = sA * f_lo(m0v[j]) + sB * f_lo(m1v[j]); acc[2 * j + 1] = sA * f_hi(m0v[j]) + sB * f_hi(m1v[j]); }
        float t4[4], t2[2];
#pragma unroll
        for (int j = 0; j < 4; ++j) t4[j] = sc32(acc[j], acc[j + 4]);
#pragma unroll
        for (int j = 0; j < 2; ++j) t2[j] = row_sum(sc16(t4[j], t4[j + 2]));
        if (i16 == 0) { const u32 a = L_S0A + (4 * (g >> 1) + 2 * (g & 1)) * 4; lds_addf(a, t2[0]); lds_addf(a + 4, t2[1]); }
        if (flag_add_ret(F_S0CNT, lane) == 4) {
            const float v = lds_ld<float>(L_S0A + (lane & 7) * 4);
            const u32 v24 = (__float_as_uint(v) + 0x80u) >> 8;
            u32x2 gr; gr[0] = (tagl << 24) | v24; gr[1] = tagh;
            const u32 n = x * 160 + 8 * r + lane;
            const u32 off = W_G1 + (n < 640 ? n * 16 : (n - 640) * 16 + 8);
            st64<SC1>(gr, rWS, lane < 8 ? off : OOB);
            TS(1);
        }
    }

    const u32 h1 = 4 * x + ((r >> 1) & 3), half = r & 1;
    if (w == 7) {
        float sum = 0.f;
#pragma unroll
        for (int k = 0; k < 5; ++k) sum += f_lo(xs[k]) + f_hi(xs[k]);
        const float mu = wave_sum(sum) * (1.f / 640.f);
        float sq = 0.f;
#pragma unroll
        for (int k = 0; k < 5; ++k) { const float a = f_lo(xs[k]) - mu, c = f_hi(xs[k]) - mu; sq += a * a + c * c; }
        const float rstd = 1.f / sqrtf(wave_sum(sq) * (1.f / 640.f) + 1e-5f);
        u32x4 gv[3]; u32 spins = 0;
        for (;;) {
            CBAR();
            bool ok = true;
#pragma unroll
            for (int k = 0; k < 3; ++k) {
                const bool act = 64 * k + lane < 160;
                gv[k] = ld128<SC1>(rWS, act ? W_G1 + (160 * ks + 64 * k + lane) * 16 : OOB);
                ok = ok && (!act || ((gv[k][0] >> 24) == tagl && gv[k][1] == tagh && (gv[k][2] >> 24) == tagl && gv[k][3] == tagh));
            }
            if (__builtin_amdgcn_ballot_w64(!ok) == 0ull) break;
            POLL_STEP();
        }
        TS(2);
#pragma unroll
        for (int k = 0; k < 3; ++k) {
            const u32 m = 64 * k + lane;
            const float sa = __uint_as_float(gv[k][0] << 8), ca = __uint_as_float(gv[k][2] << 8);
            const float hv = (__uint_as_float(xr[k] << 16) - mu) * rstd * (1.f + ca) + sa;
            const u32 hi = bf1(hv), lo = bf1(hv - __uint_as_float(hi << 16));
            const u32 pos = (m & ~31u) + 8 * ((m >> 2) & 3) + 4 * ((m >> 4) & 1) + (m & 3);
            if (m < 160) { lds_st<unsigned short>(L_HX + pos * 2, (unsigned short)hi); lds_st<unsigned short>(L_HX + 320 + pos * 2, (unsigned short)lo); }
        }
        flag_st(F_HX, 1, lane);
    }
    if (w < 6) {
        wait_ge<true>(F_LAND, T_S1, dead);
        u32x4 bw[5];
        const u32 ab = L_S1W + (4 * g + q4) * 192 + (((2 * w + (p4 >> 1)) ^ ((g & 1) << 1)) * 16) + 8 * (p4 & 1);
#pragma unroll
        for (int t = 0; t < 5; ++t) bw[t] = cat(trd(ab + (32 * t) * 192), trd(ab + (32 * t + 16) * 192));
        flag_add(F_S1DONE, lane);
        wait_ge<false>(F_HX, 1, dead);
        f32x4 acc = {0.f, 0.f, 0.f, 0.f};
#pragma unroll
        for (int t = 0; t < 5; ++t) acc = mfma32(lds_ld<u32x4>(L_HX + (lane & 1) * 320 + 64 * t + 16 * g), bw[t], acc);
        const float qv = acc[0] + acc[1];
        u32x4 o; o[0] = __float_as_uint(qv); o[1] = dppu<SHL(1)>(o[0]); o[2] = dppu<SHL(2)>(o[0]); o[3] = dppu<SHL(3)>(o[0]);
        wait_ge<false>(F_RDYA, 1, dead);
        st128<SC1>(san4(o), rWS, (lane < 16 && (lane & 3) == 0) ? W_Q2 + (ks * 6144u + h1 * 192 + 96 * half + 16 * w + lane) * 4u : OOB);
        if (w == 0) TS(3);
    }

    if (w == 7) {
        wait_ge<false>(F_RDYA, 1, dead);
        u32x4 qv[4]; u32 spins = 0;
        for (;;) {
            CBAR();
            u32 mx = 0;
#pragma unroll
            for (int k = 0; k < 4; ++k) { qv[k] = ld128<SC1>(rWS, lane < 48 ? W_Q2 + (k * 6144u + r * 192u + 4 * lane) * 4u : OOB); mx = mx4(mx, qv[k]); }
            if (none_sent(mx)) break;
            POLL_STEP();
        }
        TS(4);
        u32x2 hi, lo; float qs[4];
#pragma unroll
        for (int e = 0; e < 4; ++e) qs[e] = (__uint_as_float(qv[0][e]) + __uint_as_float(qv[1][e])) + (__uint_as_float(qv[2][e]) + __uint_as_float(qv[3][e]));
        hi[0] = pk2(qs[0], qs[1]); hi[1] = pk2(qs[2], qs[3]);
        lo[0] = pk2(qs[0] - f_lo(hi[0]), qs[1] - f_hi(hi[0])); lo[1] = pk2(qs[2] - f_lo(hi[1]), qs[3] - f_hi(hi[1]));
        if (lane < 48) { lds_st<u32x2>(L_QH + lane * 8, hi); lds_st<u32x2>(L_QH + 384 + lane * 8, lo); }
        flag_st(F_QH, 1, lane);
    }
    if (w < 6) {
        wait_ge<true>(F_LAND, T_S2, dead);
        u32x4 bw[6];
        const u32 rowb = L_X + (16 * w + i16) * 384, sw = ((i16 >> 1) & 3) << 1;
#pragma unroll
        for (int t = 0; t < 6; ++t) bw[t] = lds_ld<u32x4>(rowb + (((4 * t + g) ^ sw) * 16));
        flag_add(F_S2DONE, lane);
        wait_ge<false>(F_QH, 1, dead);
        f32x4 a0 = {0.f, 0.f, 0.f, 0.f}, a1 = {0.f, 0.f, 0.f, 0.f};
#pragma unroll
        for (int t = 0; t < 6; t += 2) {
            a0 = mfma32(lds_ld<u32x4>(L_QH + (lane & 1) * 384 + 64 * t + 16 * g), bw[t], a0);
            a1 = mfma32(lds_ld<u32x4>(L_QH + (lane & 1) * 384 + 64 * (t + 1) + 16 * g), bw[t + 1], a1);
        }
        const float qa = ((a0[0] + a0[1]) + (a1[0] + a1[1])) * (0.07216878364870322f * 1.4426950408889634f);
        const u32 hi = bf1(qa), lo = bf1(qa - __uint_as_float(hi << 16));
        const u32x4 o = pack_row(g == 0 ? hi : lo);
        const u32 dst = W_QA + ((((r >> 4) * 8 + x) * 16 + (r & 15)) * 192u + (g & 1) * 96 + 16 * w + (lane & 8)) * 2u;
        st128<SC1>(o, rWS, (g < 2 && (lane & 7) == 0) ? dst : OOB);
        if (w == 0) TS(5);
    }

    const u32 jt = x + 8 * (r >> 1), g2 = r & 1;
    const u32 blk = L_A + BLK * w;
    {
        u32x4 bq[6];
        const u32 qoff = W_QA + (((g2 * 8 + w) * 16 + i16) * 192u + 8 * g) * 2u;
        wait_ge<false>(F_RDYA, 1, dead);
        CBAR();
#pragma unroll
        for (int k = 0; k < 6; ++k) bq[k] = ld128<SC1>(rWS, qoff + ((k / 3) * 96 + (k % 3) * 32) * 2);
        wait_ge<true>(F_LAND, T_TILE0 + 12 * (w + 1), dead);
        if (w == 0) TS(6);
        if (w == 7) TS(27);
        u32x4 av[4][3];
        const u32 ar = blk + i16 * 192, asw = ((i16 >> 2) & 1) << 1;
#pragma unroll
        for (int kb = 0; kb < 4; ++kb)
#pragma unroll
            for (int t = 0; t < 3; ++t) av[kb][t] = lds_ld<u32x4>(ar + kb * 16 * 192 + ((4 * t + g) ^ asw) * 16);
        u32 spins = 0;
        for (;;) {
            u32 mx = 0;
#pragma unroll
            for (int k = 0; k < 6; ++k) mx = mx4(mx, bq[k]);
            if (none_sent(mx)) break;
            POLL_STEP();
            CBAR();
#pragma unroll
            for (int k = 0; k < 6; ++k) bq[k] = ld128<SC1>(rWS, qoff + ((k / 3) * 96 + (k % 3) * 32) * 2);
        }
        if (w == 0) TS(7);
        if (w == 7) TS(26);
#pragma unroll
        for (int kb = 0; kb < 4; ++kb) {
            f32x4 acc = {0.f, 0.f, 0.f, 0.f};
#pragma unroll
            for (int t = 0; t < 3; ++t) { acc = mfma32(av[kb][t], bq[t], acc); acc = mfma32(av[kb][t], bq[3 + t], acc); }
#pragma unroll
            for (int e = 0; e < 4; ++e) lds_addf(L_SS + ((kb * 4 + e) * 64 + lane) * 4, acc[e]);
        }
        flag_add(F_SADD, lane);
        if (w == 0) TS(20);
    }
    {
        u32x4 vt[6][2];
        const u32 vb = blk + (4 * g + q4) * 192 + (p4 >> 1) * 16 + 8 * (p4 & 1);
#pragma unroll
        for (int u = 0; u < 6; ++u) {
            const u32 cu = vb + (u ^ (g & 1)) * 32;
#pragma unroll
            for (int t = 0; t < 2; ++t) vt[u][t] = cat(trd(cu + (32 * t) * 192), trd(cu + (32 * t + 16) * 192));
        }
        if (w == 0) TS(25);
        wait_ge<false>(F_SADD, 8, dead);
        if (w == 0) TS(21);
        float mloc = -INFINITY;
#pragma unroll
        for (int p = 0; p < 16; ++p) mloc = fmaxf(mloc, lds_ld<float>(L_SS + (p * 64 + lane) * 4));
        const float s0 = lds_ld<float>(L_SS + ((2 * w) * 64 + lane) * 4), s1 = lds_ld<float>(L_SS + ((2 * w + 1) * 64 + lane) * 4);
        mloc = xg_max(mloc);
        lds_st<u32>(L_PP + (w * 64 + lane) * 4, pk2(__builtin_amdgcn_exp2f(s0 - mloc), __builtin_amdgcn_exp2f(s1 - mloc)));
        flag_add(F_PCNT, lane);
        wait_ge<false>(F_PCNT, 8, dead);
        if (w == 0) TS(22);
        u32x4 pf[2];
#pragma unroll
        for (int t = 0; t < 2; ++t)
#pragma unroll
            for (int e = 0; e < 4; ++e) pf[t][e] = lds_ld<u32>(L_PP + ((4 * t + e) * 64 + lane) * 4);
        f32x4 oa[6];
#pragma unroll
        for (int u = 0; u < 6; ++u) { f32x4 z = {0.f, 0.f, 0.f, 0.f}; oa[u] = mfma32(vt[u][0], pf[0], z); oa[u] = mfma32(vt[u][1], pf[1], oa[u]); }
        flag_add(F_PVDONE, lane);
        if (w == 0) TS(23);
        wait_ge<false>(F_RDYB, 1, dead);
        const u32 hh = 16 * g2 + i16;
#pragma unroll
        for (int uu = 0; uu < 3; ++uu) {
            u32x4 o; o[0] = pk2(oa[2 * uu][0], oa[2 * uu][1]); o[1] = pk2(oa[2 * uu][2], oa[2 * uu][3]);
            o[2] = pk2(oa[2 * uu + 1][0], oa[2 * uu + 1][1]); o[3] = pk2(oa[2 * uu + 1][2], oa[2 * uu + 1][3]);
            st128<SC1>(san4(o), rWS, W_O + (((hh * 8 + w) * 128 + jt) * 96u + 32 * uu + 8 * g) * 2u);
        }
        if (w == 7) {
            u32x4 one; one[0] = 0x3F803F80u; one[1] = 0x3F803F80u; one[2] = 0x3F803F80u; one[3] = 0x3F803F80u;
            f32x4 z = {0.f, 0.f, 0.f, 0.f};
            z = mfma32(one, pf[0], z); z = mfma32(one, pf[1], z);
            u32x2 ml; ml[0] = san(__float_as_uint(mloc)); ml[1] = san(__float_as_uint(z[0]));
            st64<SC1>(ml, rWS, lane < 16 ? W_ML + (hh * 128 + jt) * 8u : OOB);
        }
        if (w == 0) TS(8);
    }

    {
        u32x4 ov[3], ma, mb0, mb1; u32 spins = 0;
        const u32 obase = W_O + ((r * 8 + x) * 128 + 16 * w) * 192u + lane * 16u;
        const u32 mlb = W_ML + r * 1024u;
        for (;;) {
            CBAR();
#pragma unroll
            for (int k = 0; k < 3; ++k) ov[k] = ld128<SC1>(rWS, obase + 1024 * k);
            ma = ld128<SC1>(rWS, mlb + lane * 16);
            mb0 = ld128<SC1>(rWS, mlb + (16 * w + 4 * g) * 8); mb1 = ld128<SC1>(rWS, mlb + (16 * w + 4 * g + 2) * 8);
            u32 mx = mx4(mx4(mx4(mx4(0, ov[0]), ov[1]), ov[2]), ma);
            mx = mx4(mx4(mx, mb0), mb1);
            if (none_sent(mx)) break;
            POLL_STEP();
        }
        if (w == 0) TS(9);
#pragma unroll
        for (int k = 0; k < 3; ++k) {
            const u32 ci = 64 * k + lane, row = ci / 12;
            lds_st<u32x4>(blk + ((ci ^ (((row >> 2) & 1) << 1)) * 16), ov[k]);
        }
        const float mA = __uint_as_float(ma[0]), lA = __uint_as_float(ma[1]), mB = __uint_as_float(ma[2]), lB = __uint_as_float(ma[3]);
        const float Mx = wave_max(fmaxf(mA, mB));
        float om[4];
        om[0] = __builtin_amdgcn_exp2f(__uint_as_float(mb0[0]) - Mx); om[1] = __builtin_amdgcn_exp2f(__uint_as_float(mb0[2]) - Mx);
        om[2] = __builtin_amdgcn_exp2f(__uint_as_float(mb1[0]) - Mx); om[3] = __builtin_amdgcn_exp2f(__uint_as_float(mb1[2]) - Mx);
        u32x2 af; { const u32 h0 = pk2(om[0], om[1]), h1b = pk2(om[2], om[3]);
            const u32 l0 = pk2(om[0] - f_lo(h0), om[1] - f_hi(h0)), l1 = pk2(om[2] - f_lo(h1b), om[3] - f_hi(h1b));
            af[0] = (lane & 1) ? l0 : h0; af[1] = (lane & 1) ? l1 : h1b; }
        const u32 tb = blk + (4 * g + q4) * 192 + (p4 >> 1) * 16 + 8 * (p4 & 1);
        const u32 amrow = 2 * w + (g & 1);
#pragma unroll
        for (int u = 0; u < 6; ++u) {
            f32x4 z = {0.f, 0.f, 0.f, 0.f};
            z = mfma16(af, trd(tb + (u ^ (g & 1)) * 32), z);
            const float v = z[0] + z[1];
            const u32 hi = bf1(v), lo = bf1(v - __uint_as_float(hi << 16));
            const u32 vi = 16 * u + i16;
            if (g < 2) lds_st<unsigned short>(L_AM + amrow * 192 + (((vi >> 3) ^ (((amrow >> 2) & 1) << 1)) * 16) + (vi & 7) * 2, (unsigned short)(g == 0 ? hi : lo));
        }
        flag_add(F_AM, lane);
        if (w < 6) {
            const float Lsum = wave_sum(__builtin_amdgcn_exp2f(mA - Mx) * lA + __builtin_amdgcn_exp2f(mB - Mx) * lB);
            const float rl = 1.f / Lsum;
            wait_ge<true>(F_LAND, T_UV, dead);
            u32x4 bw[2][3];
            const u32 sw = 2 * (g & 1) + (q4 >> 1);
#pragma unroll
            for (int d = 0; d < 2; ++d) {
                const u32 ub = L_X + (4 * g + q4) * 384 + (2 * ((2 * w + d) ^ sw) + (p4 >> 1)) * 16 + 8 * (p4 & 1);
#pragma unroll
                for (int t = 0; t < 3; ++t) bw[d][t] = cat(trd(ub + (32 * t) * 384), trd(ub + (32 * t + 16) * 384));
            }
            wait_ge<false>(F_AM, 8, dead);
            u32x4 aa[3];
#pragma unroll
            for (int t = 0; t < 3; ++t) aa[t] = lds_ld<u32x4>(L_AM + i16 * 192 + (((4 * t + g) ^ (((i16 >> 2) & 1) << 1)) * 16));
            float res[2];
#pragma unroll
            for (int d = 0; d < 2; ++d) {
                f32x4 z = {0.f, 0.f, 0.f, 0.f};
#pragma unroll
                for (int t = 0; t < 3; ++t) z = mfma32(aa[t], bw[d][t], z);
                res[d] = xg_sum((z[0] + z[1]) + (z[2] + z[3])) * rl;
            }
            const u32x4 o = pack_row(bf1(g == 0 ? res[0] : res[1]));
            const u32 dst = W_CTX + (x * 6144u + r * 192u + 16 * (2 * w + (g & 1)) + (lane & 8)) * 2u;
            st128<SC1>(o, rWS, (g < 2 && (lane & 7) == 0) ? dst : OOB);
            if (w == 0) TS(10);
        }
    }

    if (typeA) {
        wait_ge<true>(F_LAND, T_UV + 6 * (w + 1), dead);
        u32x4 bw[2][3];
        const u32 fbx = blk + B_W + (8 * g + q4) * 64 + (p4 >> 1) * 16 + 8 * (p4 & 1);
#pragma unroll
        for (int u = 0; u < 2; ++u) {
            const u32 ub = fbx + (u ^ (g & 1)) * 32;
#pragma unroll
            for (int t = 0; t < 3; ++t) bw[u][t] = cat(trd(ub + (32 * t) * 64), trd(ub + (32 * t + 4) * 64));
        }
        u32x4 ca[3]; u32 spins = 0;
        const u32 coff = i16 < 8 ? W_CTX + (i16 * 6144u + 768u * x + 96u * w + 8 * g) * 2u : OOB;
        for (;;) {
            CBAR();
            u32 mx = 0;
#pragma unroll
            for (int t = 0; t < 3; ++t) { ca[t] = ld128<SC1>(rWS, coff + 64 * t); mx = mx4(mx, ca[t]); }
            if (none_sent(mx)) break;
            POLL_STEP();
        }
        if (w == 0) TS(11);
        float res[2];
#pragma unroll
        for (int u = 0; u < 2; ++u) {
            f32x4 z = {0.f, 0.f, 0.f, 0.f};
#pragma unroll
            for (int t = 0; t < 3; ++t) z = mfma32(ca[t], bw[u][t], z);
            res[u] = xg_sum((z[0] + z[1]) + (z[2] + z[3]));
        }
        if (g < 2) lds_addf(L_FR + (16 * g + i16) * 4, g == 0 ? res[0] : res[1]);
        if (flag_add_ret(F_FRC, lane) == 7) {
            const float v = lds_ld<float>(L_FR + (lane & 31) * 4);
            const u32x4 o = pack_row(bf1(v));
            const u32 dst = W_AT + (x * 640u + 32u * r + 16 * (g & 1) + (lane & 8)) * 2u;
            st128<SC1>(o, rWS, (g < 2 && (lane & 7) == 0) ? dst : OOB);
            TS(12);
        }
    } else {
        const u32 fb = x * 12 + (r - 20);
        const u32 cA = 8 * (lane & 7) + (lane >> 3);
        const bool hasB = (lane & 7) < 2;
        const u32 colA = 80 * w + cA, colB = colA + 64;
        float ga[2], sb[2], cb1[2], gb[2], xv[2];
        {
            u32 mr[2][4][2]; u32 spins = 0;
            for (;;) {
                CBAR();
                u32 mx = 0;
#pragma unroll
                for (int kh = 0; kh < 2; ++kh)
#pragma unroll
                    for (int pm = 0; pm < 4; ++pm) {
                        mr[kh][pm][0] = ld32<SC1>(rWS, W_MR + (kh * 2560u + pm * 640u + colA) * 4u);
                        mr[kh][pm][1] = ld32<SC1>(rWS, hasB ? W_MR + (kh * 2560u + pm * 640u + colB) * 4u : OOB);
                        mx = umax(mx, umax(mr[kh][pm][0], mr[kh][pm][1]));
                    }
                if (none_sent(mx)) break;
                POLL_STEP();
            }
#pragma unroll
            for (int c = 0; c < 2; ++c) {
                ga[c] = __uint_as_float(mr[0][0][c]) + __uint_as_float(mr[1][0][c]);
                sb[c] = __uint_as_float(mr[0][1][c]) + __uint_as_float(mr[1][1][c]);
                cb1[c] = 1.f + (__uint_as_float(mr[0][2][c]) + __uint_as_float(mr[1][2][c]));
                gb[c] = __uint_as_float(mr[0][3][c]) + __uint_as_float(mr[1][3][c]);
            }
            xv[0] = __uint_as_float(ld16(rX, colA * 2) << 16); xv[1] = __uint_as_float(ld16(rX, hasB ? colB * 2 : OOB) << 16);
        }
        if (w == 0) TS(13);
        const u32 gab = blk + B_GA;
#pragma unroll
        for (int c = 0; c < 2; ++c) {
            if (c == 0 || hasB) {
                const u32 cc = cA + 64 * c;
                const u32 h = bf1(cb1[c]), l = bf1(cb1[c] - __uint_as_float(h << 16)), h2 = bf1(sb[c]), l2 = bf1(sb[c] - __uint_as_float(h2 << 16));
                lds_st<unsigned short>(gab + 2 * 160 + cc * 2, (unsigned short)h); lds_st<unsigned short>(gab + 3 * 160 + cc * 2, (unsigned short)l);
                lds_st<unsigned short>(gab + 4 * 160 + cc * 2, (unsigned short)h2); lds_st<unsigned short>(gab + 5 * 160 + cc * 2, (unsigned short)l2);
                lds_st<float>(blk + B_GB + cc * 4, gb[c]);
            }
        }
        wait_ge<true>(F_LAND, T_UV + 6 * (w + 1), dead);
        u32x2 b1[5], b2[5];
#pragma unroll
        for (int t = 0; t < 5; ++t) b1[t] = trd(blk + B_W + 512 * t + 8 * lane);
#pragma unroll
        for (int u = 0; u < 5; ++u) b2[u] = trd(blk + B_W2 + (4 * g + q4) * 160 + 32 * u + 8 * p4);
        u32x4 at0, at1; u32 spins = 0;
        const u32 aoff = W_AT + ((lane >> 3) * 640u + 80u * w + 8 * (lane & 7)) * 2u;
        for (;;) {
            CBAR();
            at0 = ld128<SC1>(rWS, aoff); at1 = ld128<SC1>(rWS, hasB ? aoff + 128 : OOB);
            if (none_sent(mx4(mx4(0, at0), at1))) break;
            POLL_STEP();
        }
        if (w == 0) TS(14);
        float x1[2];
#pragma unroll
        for (int c = 0; c < 2; ++c) {
            const u32x4 a = c ? at1 : at0;
            float v[8];
#pragma unroll
            for (int j = 0; j < 4; ++j) { v[2 * j] = f_lo(a[j]); v[2 * j + 1] = f_hi(a[j]); }
            float t4[4], t2[2];
#pragma unroll
            for (int j = 0; j < 4; ++j) t4[j] = sc32(v[j], v[j + 4]);
#pragma unroll
            for (int j = 0; j < 2; ++j) t2[j] = sc16(t4[j], t4[j + 2]);
            const bool b3 = (lane & 8) != 0;
            const float keep = b3 ? t2[1] : t2[0], give = b3 ? t2[0] : t2[1];
            const float attn = keep + dppf<ROR(8)>(give);
            x1[c] = xv[c] + ga[c] * attn;
        }
        if (!hasB) x1[1] = 0.f;
#pragma unroll
        for (int c = 0; c < 2; ++c) {
            if (c == 0 || hasB) {
                const u32 cc = cA + 64 * c;
                const float uv = x1[c] * cb1[c];
                const u32 h = bf1(uv), l = bf1(uv - __uint_as_float(h << 16));
                lds_st<unsigned short>(gab + cc * 2, (unsigned short)h); lds_st<unsigned short>(gab + 160 + cc * 2, (unsigned short)l);
                lds_st<float>(blk + B_X1 + cc * 4, x1[c]);
            }
        }
        CBAR();
        f32x4 z = {0.f, 0.f, 0.f, 0.f};
        const u32 arow = (lane & 7) < 6 ? (lane & 7) : 0;
#pragma unroll
        for (int t = 0; t < 5; ++t) z = mfma16(lds_ld<u32x2>(gab + arow * 160 + 32 * t + 8 * g), b1[t], z);
        const float s1 = wave_sum(x1[0] + x1[1]), s2 = wave_sum(x1[0] * x1[0] + x1[1] * x1[1]);
        if (g == 0) { lds_addf(L_GR + i16 * 4, z[0] + z[1]); lds_addf(L_GR + 64 + i16 * 4, z[2] + z[3]); }
        if (g == 1) lds_addf(L_GR + 128 + i16 * 4, z[0] + z[1]);
        if (lane == 32) { lds_addf(L_GR + 192, s1); lds_addf(L_GR + 196, s2); }
        flag_add(F_GRC, lane);
        wait_ge<false>(F_GRC, 8, dead);
        const f32x4 U = lds_ld<f32x4>(L_GR + 16 * g), V = lds_ld<f32x4>(L_GR + 64 + 16 * g), SB = lds_ld<f32x4>(L_GR + 128 + 16 * g);
        const f32x2 st = lds_ld<f32x2>(L_GR + 192);
        const float mu = st[0] * (1.f / 640.f);
        const float var = fmaxf(st[1] * (1.f / 640.f) - mu * mu, 0.f);
        const float rstd = 1.f / sqrtf(var + 1e-5f);
        float av[4];
#pragma unroll
        for (int e = 0; e < 4; ++e) av[e] = fmaxf(rstd * (U[e] - mu * V[e]) + SB[e], 0.f);
        u32x2 af; { const u32 h0 = pk2(av[0], av[1]), h1b = pk2(av[2], av[3]);
            const u32 l0 = pk2(av[0] - f_lo(h0), av[1] - f_hi(h0)), l1 = pk2(av[2] - f_lo(h1b), av[3] - f_hi(h1b));
            af[0] = (lane & 1) ? l0 : h0; af[1] = (lane & 1) ? l1 : h1b; }
        float fo[5];
#pragma unroll
        for (int u = 0; u < 5; ++u) { f32x4 y = {0.f, 0.f, 0.f, 0.f}; y = mfma16(af, b2[u], y); fo[u] = y[0] + y[1]; }
        {
            const float v = g == 0 ? fo[0] : (g == 1 ? fo[1] : (g == 2 ? fo[2] : fo[3]));
            const u32x4 o = pack_row(bf1(v)), o2 = pack_row(bf1(fo[4]));
            st128<SC1>(o, rWS, (lane & 7) == 0 ? W_FP + (((5 * w + g) * 96u + fb) * 16 + (lane & 8)) * 2u : OOB);
            st128<SC1>(o2, rWS, (lane & 55) == 0 ? W_FP + (((5 * w + 4) * 96u + fb) * 16 + (lane & 8)) * 2u : OOB);
        }
        if (w == 0) TS(15);
        const int ci = (r - 20) * 8 + x;
        if (ci < 40 && ci / 5 == w) {
            u32x4 fv[3]; u32 sp2 = 0;
            for (;;) {
                CBAR();
                u32 mx = 0;
#pragma unroll
                for (int k = 0; k < 3; ++k) { fv[k] = ld128<SC1>(rWS, W_FP + (u32)ci * 3072u + 1024 * k + lane * 16); mx = mx4(mx, fv[k]); }
                if (none_sent(mx)) break;
                if (++sp2 > CAP_G) { dead = true; break; }
                __builtin_amdgcn_s_sleep(1);
            }
            TS(16);
            float v[8];
#pragma unroll
            for (int j = 0; j < 4; ++j) { v[2 * j] = (f_lo(fv[0][j]) + f_lo(fv[1][j])) + f_lo(fv[2][j]); v[2 * j + 1] = (f_hi(fv[0][j]) + f_hi(fv[1][j])) + f_hi(fv[2][j]); }
            float t4[4], t2[2];
#pragma unroll
            for (int j = 0; j < 4; ++j) t4[j] = sc32(v[j], v[j + 4]);
#pragma unroll
            for (int j = 0; j < 2; ++j) t2[j] = sc16(t4[j], t4[j + 2]);
            const bool b3 = (lane & 8) != 0;
            const float keep = b3 ? t2[1] : t2[0], give = b3 ? t2[0] : t2[1];
            float f = keep + dppf<ROR(8)>(give);
            f += dppf<SHL(4)>(f);
            f += dppf<SHL(2)>(f);
            const u32 cc = 16 * (ci % 5) + 8 * (lane & 1) + (lane >> 3);
            const float o = lds_ld<float>(blk + B_X1 + cc * 4) + lds_ld<float>(blk + B_GB + cc * 4) * f;
            const rsrc_t rO = mkrs(p_out, M_ * 2);
            __builtin_amdgcn_raw_buffer_store_b16((unsigned short)bf1(o), rO, (lane & 6) == 0 ? (16 * ci + 8 * (lane & 1) + (lane >> 3)) * 2 : OOB, 0, 0);
            if (ci == 0 && lane == 0) { u32x2 e; e[0] = (u32)tag; e[1] = (u32)(tag >> 32); st64<SC1>(e, rWS, W_EPOCH); }
            TS(17);
        }
    }
}

extern "C" void kernel_launch(void* const* d_in, const int* in_sizes, int n_in, void* d_out, int out_size, void* d_ws, size_t ws_size, hipStream_t stream)
{
    (void)in_sizes; (void)n_in; (void)out_size; (void)ws_size;
    (void)hipFuncSetAttribute((const void*)adaln_mla_ffn_kernel, hipFuncAttributeMaxDynamicSharedMemorySize, (int)LDS_BYTES);
    hipLaunchKernelGGL(adaln_mla_ffn_kernel, dim3(256), dim3(640), LDS_BYTES, stream,
                       (const void*)d_in[0], (const void*)d_in[1], (const void*)d_in[2], (const void*)d_in[3], (const void*)d_in[4], (const void*)d_in[5],
                       (const void*)d_in[7], (const void*)d_in[8], (const void*)d_in[9], (const void*)d_in[10], d_out, d_ws);
}
```

```cpp
#include <hip/hip_runtime.h>
#include <hip/hip_bf16.h>
#include <stdint.h>

#define LAS __attribute__((address_space(3)))
#define DI __device__ __forceinline__
typedef unsigned int u32;
typedef unsigned long long u64;
typedef u32 u32x2 __attribute__((ext_vector_type(2)));
typedef u32 u32x3 __attribute__((ext_vector_type(3)));
typedef u32 u32x4 __attribute__((ext_vector_type(4)));
typedef float f32x2 __attribute__((ext_vector_type(2)));
typedef float f32x4 __attribute__((ext_vector_type(4)));
typedef short s16x4 __attribute__((ext_vector_type(4)));
typedef __bf16 bf16x2 __attribute__((ext_vector_type(2)));
typedef __bf16 bf16x8 __attribute__((ext_vector_type(8)));
typedef __amdgpu_buffer_rsrc_t rsrc_t;

#define WIN 18
#define WIN2 18
#define PAUSE 0
#define STG 6
#define STG2 3
#define NT_S0 0
#define NT_MR 2
#define NT_Q 1
#define NT_UK 1
#define NT_KV 1
#define NT_UV 1
#define NT_O 1
#define NT_W1 1
#define NT_W2 1
#define O_BASE 1048576u

constexpr int M_ = 640, H_ = 32, D_ = 192, DC = 768, SK = 8192, DFF = 1536;
constexpr u32 SENT = 0xFFFFFFFFu;
constexpr u32 OOB = 0x80000000u;

constexpr u32 W_EPOCH = 0;
constexpr u32 W_FA = 256;
constexpr u32 W_G1 = 4096;
constexpr u32 W_Q2 = 16384;
constexpr u32 W_SMALL = W_Q2 + 196608;
constexpr u32 W_QA = W_SMALL;
constexpr u32 W_CTX = W_QA + 98304;
constexpr u32 W_AT = W_CTX + 98304;
constexpr u32 W_FP = W_AT + 10240;
constexpr u32 W_MR = W_FP + 122880;
constexpr u32 W_SMALL_END = W_MR + 20480;
constexpr u32 SMALL_CH = (W_SMALL_END - W_SMALL) / 16;
constexpr u32 SMALL_PER = (SMALL_CH + 255) / 256;
constexpr u32 OROW = 208;
constexpr u32 W_O = O_BASE;
constexpr u32 O_BYTES = 32u * 8 * 128 * OROW;
constexpr u32 W_PROF = W_SMALL_END;
constexpr u32 W_END = W_O + O_BYTES;
static_assert(W_PROF + 256 * 64 * 8 <= W_O, "ws map");
static_assert(SMALL_PER <= 128, "prefill");
static_assert(W_END <= 268435456u, "ws map");

constexpr u32 L_A = 0;
constexpr u32 BLK = 12288;
constexpr u32 L_S1W = 5 * BLK;
constexpr u32 L_X = 98304;
constexpr u32 L_Y = 135168;
constexpr u32 L_FLG = L_Y;
constexpr u32 L_S0P = L_Y + 256;
constexpr u32 L_EML = L_Y + 448;
constexpr u32 L_FRP = L_Y + 512;
constexpr u32 L_GRP = L_Y + 1536;
constexpr u32 L_SC = L_Y + 3584;
constexpr u32 L_HX = L_Y + 7680;
constexpr u32 L_QH = L_Y + 8448;
constexpr u32 L_AM = L_Y + 9216;
constexpr u32 L_SIL = L_Y + 12288;
constexpr u32 LDS_BYTES = 163840;
constexpr u32 B_W = 4096, B_W2 = 7168, B_GA = 10240, B_X1 = 11200, B_GB = 11520;

enum { F_LAND = 0, F_RDYA, F_S0CNT, F_HX, F_S1DONE, F_QH, F_S2DONE, F_BLK, F_QAOK, F_SADD, F_SC, F_AM, F_FRC, F_GRC };

constexpr u32 CAP_G = 20000;
constexpr u32 CAP_L = 400000;

constexpr u32 T_S1 = 30, T_S2 = 66, T_TILE0 = 66, T_UV = 198;

DI rsrc_t mkrs(const void* p, u32 bytes) { return __builtin_amdgcn_make_buffer_rsrc((void*)p, (short)0, (int)bytes, 0x00020000); }
template <int AUX> DI u32x4 ld128(rsrc_t r, u32 off) { return __builtin_amdgcn_raw_buffer_load_b128(r, off, 0, AUX); }
template <int AUX> DI u32x3 ld96(rsrc_t r, u32 off) { return __builtin_amdgcn_raw_buffer_load_b96(r, off, 0, AUX); }
template <int AUX> DI u32x2 ld64(rsrc_t r, u32 off) { return __builtin_amdgcn_raw_buffer_load_b64(r, off, 0, AUX); }
template <int AUX> DI u32 ld32(rsrc_t r, u32 off) { return __builtin_amdgcn_raw_buffer_load_b32(r, off, 0, AUX); }
DI u32 ld16(rsrc_t r, u32 off) { return (u32)__builtin_amdgcn_raw_buffer_load_b16(r, off, 0, 0); }
template <int AUX> DI void st128(u32x4 v, rsrc_t r, u32 off) { __builtin_amdgcn_raw_buffer_store_b128(v, r, off, 0, AUX); }
template <int AUX> DI void st96(u32x3 v, rsrc_t r, u32 off) { __builtin_amdgcn_raw_buffer_store_b96(v, r, off, 0, AUX); }
template <int AUX> DI void st64(u32x2 v, rsrc_t r, u32 off) { __builtin_amdgcn_raw_buffer_store_b64(v, r, off, 0, AUX); }
template <int AUX> DI void st32(u32 v, rsrc_t r, u32 off) { __builtin_amdgcn_raw_buffer_store_b32(v, r, off, 0, AUX); }
constexpr int SC1 = 16, NT = 2;

DI float f_lo(u32 v) { return __uint_as_float(v << 16); }
DI float f_hi(u32 v) { return __uint_as_float(v & 0xFFFF0000u); }
DI u32 pk2(float a, float b) { bf16x2 v; v[0] = (__bf16)a; v[1] = (__bf16)b; return __builtin_bit_cast(u32, v); }
DI u32 bf1(float a) { return pk2(a, 0.f) & 0xFFFFu; }
DI u32 san(u32 v) { return v < 0xFFFFFFFEu ? v : 0xFFFFFFFEu; }
DI u32x4 san4(u32x4 v) { u32x4 r; r[0] = san(v[0]); r[1] = san(v[1]); r[2] = san(v[2]); r[3] = san(v[3]); return r; }
DI u32 umax(u32 a, u32 b) { return a > b ? a : b; }
DI u32 mx4(u32 acc, u32x4 v) { return umax(umax(acc, umax(v[0], v[1])), umax(v[2], v[3])); }

template <int CTRL> DI u32 dppu(u32 v) { return (u32)__builtin_amdgcn_update_dpp(0, (int)v, CTRL, 0xF, 0xF, true); }
template <int CTRL> DI float dppf(float v) { return __uint_as_float(dppu<CTRL>(__float_as_uint(v))); }
constexpr int ROR(int n) { return 0x120 + n; }
constexpr int SHL(int n) { return 0x100 + n; }
constexpr int NBC(int n) { return 0x150 + n; }
constexpr int QX1 = 0xB1, QX2 = 0x4E;
DI float row_sum(float v) { v += dppf<ROR(8)>(v); v += dppf<ROR(4)>(v); v += dppf<ROR(2)>(v); v += dppf<ROR(1)>(v); return v; }
DI u32 row_xor(u32 v) { v ^= dppu<ROR(8)>(v); v ^= dppu<ROR(4)>(v); v ^= dppu<ROR(2)>(v); v ^= dppu<ROR(1)>(v); return v; }
DI u32 row_add(u32 v) { v += dppu<ROR(8)>(v); v += dppu<ROR(4)>(v); v += dppu<ROR(2)>(v); v += dppu<ROR(1)>(v); return v; }
DI void swap16(float& a, float& b) { auto r = __builtin_amdgcn_permlane16_swap(__float_as_uint(a), __float_as_uint(b), false, false); a = __uint_as_float(r[0]); b = __uint_as_float(r[1]); }
DI void swap32(float& a, float& b) { auto r = __builtin_amdgcn_permlane32_swap(__float_as_uint(a), __float_as_uint(b), false, false); a = __uint_as_float(r[0]); b = __uint_as_float(r[1]); }
DI float xg_sum(float v) { float a = v, b = v; swap16(a, b); v = a + b; a = v; b = v; swap32(a, b); return a + b; }
DI float xg_max(float v) { float a = v, b = v; swap16(a, b); v = fmaxf(a, b); a = v; b = v; swap32(a, b); return fmaxf(a, b); }
DI float wave_sum(float v) { return xg_sum(row_sum(v)); }
DI float sc32(float a, float b) { swap32(a, b); return a + b; }
DI float sc16(float a, float b) { swap16(a, b); return a + b; }
DI u32 sel4(int e, u32 a, u32 b, u32 c, u32 d) { const u32 lo = (e & 1) ? b : a, hi = (e & 1) ? d : c; return (e & 2) ? hi : lo; }

DI f32x4 mfma32(u32x4 a, u32x4 b, f32x4 c) { return __builtin_amdgcn_mfma_f32_16x16x32_bf16(__builtin_bit_cast(bf16x8, a), __builtin_bit_cast(bf16x8, b), c, 0, 0, 0); }
DI f32x4 mfma16(u32x2 a, u32x2 b, f32x4 c) { return __builtin_amdgcn_mfma_f32_16x16x16bf16_1k(__builtin_bit_cast(s16x4, a), __builtin_bit_cast(s16x4, b), c, 0, 0, 0); }
DI u32x2 trd(u32 addr) { return __builtin_bit_cast(u32x2, __builtin_amdgcn_ds_read_tr16_b64_v4i16((LAS s16x4*)addr)); }
DI u32x4 cat(u32x2 a, u32x2 b) { u32x4 r; r[0] = a[0]; r[1] = a[1]; r[2] = b[0]; r[3] = b[1]; return r; }
template <class T> DI T lds_ld(u32 addr) { return *(LAS T*)addr; }
template <class T> DI void lds_st(u32 addr, T v) { *(LAS T*)addr = v; }
#define CBAR() asm volatile("" ::: "memory")

DI u32 flag_ld(int i) { return (u32)__builtin_amdgcn_readfirstlane((int)*(volatile LAS u32*)(L_FLG + 4 * i)); }
DI void flag_st(int i, u32 v, int lane) { CBAR(); if (lane == 0) *(volatile LAS u32*)(L_FLG + 4 * i) = v; CBAR(); }
DI void flag_add(int i, int lane) { CBAR(); if (lane == 0) __hip_atomic_fetch_add((LAS u32*)(L_FLG + 4 * i), 1u, __ATOMIC_RELAXED, __HIP_MEMORY_SCOPE_WORKGROUP); CBAR(); }
DI u32 flag_add_ret(int i, int lane) {
    CBAR(); u32 o = 0;
    if (lane == 0) o = __hip_atomic_fetch_add((LAS u32*)(L_FLG + 4 * i), 1u, __ATOMIC_RELAXED, __HIP_MEMORY_SCOPE_WORKGROUP);
    o = (u32)__builtin_amdgcn_readfirstlane((int)o); CBAR(); return o;
}
template <bool SLEEP> DI void wait_ge(int i, u32 target, bool& dead) {
    u32 n = 0;
    while (!dead && flag_ld(i) < target) { if (++n > CAP_L) dead = true; if (SLEEP) __builtin_amdgcn_s_sleep(1); }
    CBAR();
}
DI bool all_ok(bool ok) { return __builtin_amdgcn_ballot_w64(!ok) == 0ull; }
DI bool none_sent(u32 mx) { return __builtin_amdgcn_ballot_w64(mx == SENT) == 0ull; }
#define POLL_STEP() { if (++spins > CAP_G) { dead = true; break; } __builtin_amdgcn_s_sleep(1); }

template <int N> struct PSet { u32x4 v[N]; };
template <int N> DI void take(PSet<N>& res, const PSet<N>& s) {
#pragma unroll
    for (int k = 0; k < N; ++k) res.v[k] = s.v[k];
}
#define PIPE 0
template <int N, int STAGGER, class LD, class OK>
DI void ppoll(PSet<N>& res, PSet<N>& a, PSet<N>& b, PSet<N>& c, LD ld, OK ok, bool& dead) {
    (void)a; (void)b; (void)c;
    for (u32 spins = 0;;) {
        CBAR(); ld(res); CBAR();
        if (ok(res)) break;
        if (++spins > CAP_G) { dead = true; break; }
    }
    CBAR();
}
template <int N> DI void retire(PSet<N>&, PSet<N>&, PSet<N>&) {}

#define TS(i) do { } while (0)
#define TCAP(v) do { } while (0)
#define TSV(i, t_) do { } while (0)

DI u32x4 pack_row(u32 v16) {
    u32 pr = v16 | (dppu<SHL(1)>(v16) << 16);
    u32x4 r; r[0] = pr; r[1] = dppu<SHL(2)>(pr); r[2] = dppu<SHL(4)>(pr); r[3] = dppu<SHL(6)>(pr);
    return san4(r);
}

template <int NTL> DI void dma(u32 lds, u32 voff, rsrc_t rs, u32 soff) {
    if constexpr (NTL) asm volatile("s_nop 4\n\ts_mov_b32 m0, %0\n\ts_nop 0\n\tbuffer_load_dwordx4 %1, %2, %3 offen nt lds" ::"s"(lds), "v"(voff), "s"(rs), "s"(soff) : "memory");
    else asm volatile("s_nop 4\n\ts_mov_b32 m0, %0\n\ts_nop 0\n\tbuffer_load_dwordx4 %1, %2, %3 offen lds" ::"s"(lds), "v"(voff), "s"(rs), "s"(soff) : "memory");
}
template <int N> DI void waitvm() { asm volatile("s_waitcnt vmcnt(%0)" ::"i"(N) : "memory"); }
struct Ldr { int issued; int pub; int lane; bool go; bool dead; };
DI void publish(Ldr& s, int landed) { if (s.go && landed > s.pub) { s.pub = landed; flag_st(F_LAND, (u32)landed, s.lane); } }
template <int W> DI void step3(Ldr& s) { s.issued += 3; waitvm<W>(); publish(s, s.issued - W); }
template <int N> DI void drain(Ldr& s) { waitvm<N>(); publish(s, s.issued - N); if constexpr (N >= 3) drain<N - 3>(s); }
template <int W> DI void park(Ldr& s, int flag, u32 target) {
    if (flag_ld(flag) < target) { drain<W - 3>(s); wait_ge<true>(flag, target, s.dead); }
    CBAR();
}
template <int W, int NTL> DI void seg3(Ldr& s, u32 lds0, const u32 (&v)[3], rsrc_t rs, u32 soff0, u32 sstep, int ng) {
    for (int g = 0; g < ng; ++g) {
        const u32 so = soff0 + (u32)g * sstep, ld = lds0 + 3072u * (u32)g;
        dma<NTL>(ld, v[0], rs, so); dma<NTL>(ld + 1024, v[1], rs, so); dma<NTL>(ld + 2048, v[2], rs, so);
        step3<W>(s);
    }
}

extern __shared__ __attribute__((aligned(16))) char smem_dyn[];

__global__ void __launch_bounds__(640, 3)
adaln_mla_ffn_kernel(const void* __restrict__ p_x, const void* __restrict__ p_ckv, const void* __restrict__ p_wq, const void* __restrict__ p_wuk,
                     const void* __restrict__ p_wuv, const void* __restrict__ p_wo, const void* __restrict__ p_w1, const void* __restrict__ p_w2,
                     const void* __restrict__ p_c, const void* __restrict__ p_wmod, void* __restrict__ p_out, void* __restrict__ p_ws)
{
    const int tid = threadIdx.x, lane = tid & 63;
    const int wave = __builtin_amdgcn_readfirstlane(tid >> 6);
    const int b = blockIdx.x, x = b & 7, r = b >> 3;
    const bool typeA = r < 20;
    const int g = lane >> 4, i16 = lane & 15, q4 = i16 >> 2, p4 = lane & 3;
    bool dead = false;
    (void)smem_dyn;
    if (tid < 16) { u32x4 z; z[0] = 0; z[1] = 0; z[2] = 0; z[3] = 0; lds_st<u32x4>(L_FLG + tid * 16, z); }
    asm volatile("" ::"s"(p_x), "s"(p_ckv), "s"(p_wq), "s"(p_wuk), "s"(p_wuv), "s"(p_wo), "s"(p_w1), "s"(p_w2), "s"(p_c), "s"(p_wmod), "s"(p_out), "s"(p_ws));
    asm volatile("s_waitcnt lgkmcnt(0)" ::: "memory");
    __builtin_amdgcn_s_barrier();
    CBAR();
    TCAP(tbw);
    const rsrc_t rWS = mkrs(p_ws, W_END);

    if (wave == 8) {
        Ldr s; s.issued = 0; s.pub = 0; s.lane = lane; s.go = true; s.dead = false;
        u32 v1[3], vt[3], vu[3];
#pragma unroll
        for (int k = 0; k < 3; ++k) {
            const u32 p = 64 * k + lane;
            const u32 row = p / 12, ch = (p % 12) ^ (((row >> 2) & 1) << 1);
            v1[k] = row * 12288u + ch * 16; vt[k] = row * 1536u + ch * 16;
            const u32 r2 = p / 24, c2 = (p % 24) ^ (((r2 >> 1) & 3) << 1);
            vu[k] = r2 * 12288u + c2 * 16;
        }
        const rsrc_t rq = mkrs(p_wq, M_ * H_ * D_ * 2), ruk = mkrs(p_wuk, DC * H_ * D_ * 2), ruv = mkrs(p_wuv, DC * H_ * D_ * 2), rkv = mkrs(p_ckv, SK * DC * 2);
        const u32 ks = r >> 3, h1 = 4 * x + ((r >> 1) & 3), half = r & 1;
        const u32 so1 = 160u * ks * 12288u + (h1 * 192 + 96 * half) * 2;
        TCAP(tb8);
        seg3<WIN, NT_Q>(s, L_S1W, v1, rq, so1, 16u * 12288u, 10);
        TS(44); TSV(51, t_entry); TSV(54, tb8);
        const u32 so2 = 96u * x * 12288u + (u32)r * 384u;
        seg3<WIN, NT_UK>(s, L_X, vu, ruk, so2, 8u * 12288u, 12);
        TS(45);
        const u32 jt = x + 8 * (r & 15);
        const u32 sot = 64u * jt * 1536u;
        for (int w = 0; w < 5; ++w) seg3<WIN, NT_KV>(s, L_A + BLK * w, vt, rkv, sot + 192u * w, 16u * 1536u, 4);
        park<WIN>(s, F_S1DONE, 6);
        for (int w = 5; w < 8; ++w) seg3<WIN, NT_KV>(s, L_A + BLK * w, vt, rkv, sot + 192u * w, 16u * 1536u, 4);
        TS(46);
        if (PAUSE) { drain<WIN - 3>(s); TS(47); wait_ge<true>(F_QAOK, 8, s.dead); }
        park<WIN>(s, F_S2DONE, 6);
        seg3<WIN2, NT_UV>(s, L_X, vu, ruv, so2, 8u * 12288u, 12);
        TS(48);
        park<(WIN > WIN2 ? WIN : WIN2)>(s, F_BLK, 8);
        if (typeA) {
            const rsrc_t ro = mkrs(p_wo, H_ * D_ * M_ * 2);
            const u32 row = lane >> 2, ch = (lane & 3) ^ (((row >> 3) & 1) << 1);
            const u32 vf = row * 1280u + ch * 16;
            for (int w = 0; w < 8; ++w) {
                const u32 so = (768u * x + 96u * w) * 1280u + 64u * r, ld = L_A + BLK * w + B_W;
                for (int k = 0; k < 6; k += 3) {
                    dma<NT_O>(ld + 1024 * k, vf, ro, so + 16u * 1280u * k); dma<NT_O>(ld + 1024 * (k + 1), vf, ro, so + 16u * 1280u * (k + 1));
                    dma<NT_O>(ld + 1024 * (k + 2), vf, ro, so + 16u * 1280u * (k + 2));
                    step3<WIN2>(s);
                }
            }
        } else {
            const rsrc_t r1 = mkrs(p_w1, M_ * DFF * 2), r2 = mkrs(p_w2, DFF * M_ * 2);
            const u32 fb = x * 12 + (r - 20);
            const u32 va = (lane >> 1) * 3072u + (lane & 1) * 16, va2 = lane < 32 ? va : OOB;
            u32 vb[3];
#pragma unroll
            for (int k = 0; k < 3; ++k) { const u32 p = 64 * k + lane; vb[k] = p < 160 ? (p / 10) * 1280u + (p % 10) * 16 : OOB; }
            for (int w = 0; w < 8; ++w) {
                const u32 ld = L_A + BLK * w;
                const u32 sa = 80u * w * 3072u + fb * 32u, sb = 16u * fb * 1280u + 160u * w;
                dma<NT_W1>(ld + B_W, va, r1, sa); dma<NT_W1>(ld + B_W + 1024, va, r1, sa + 32u * 3072u); dma<NT_W1>(ld + B_W + 2048, va2, r1, sa + 64u * 3072u);
                step3<WIN2>(s);
                dma<NT_W2>(ld + B_W2, vb[0], r2, sb); dma<NT_W2>(ld + B_W2 + 1024, vb[1], r2, sb); dma<NT_W2>(ld + B_W2 + 2048, vb[2], r2, sb);
                step3<WIN2>(s);
            }
        }
        TS(49);
        drain<(WIN > WIN2 ? WIN : WIN2) - 3>(s);
        TS(50);
        return;
    }

    const u32x2 ep = ld64<SC1>(rWS, W_EPOCH);

    if (wave == 9) {
        u32x4 sv; sv[0] = SENT; sv[1] = SENT; sv[2] = SENT; sv[3] = SENT;
        {
            const u32 c0 = (u32)b * SMALL_PER + lane, c1 = c0 + 64;
            st128<SC1>(sv, rWS, c0 < SMALL_CH ? W_SMALL + c0 * 16 : OOB);
            st128<SC1>(sv, rWS, (lane + 64 < (int)SMALL_PER && c1 < SMALL_CH) ? W_SMALL + c1 * 16 : OOB);
        }
        const int it = 255 - b;
        const rsrc_t rc = mkrs(p_c, M_ * 2), rm = mkrs(p_wmod, M_ * 6 * M_ * 2);
        u32 cs[5];
        if (it < 160) {
#pragma unroll
            for (int k = 0; k < 5; ++k) cs[k] = ld16(rc, (320 * (it & 1) + 64 * k + lane) * 2);
        }
        TCAP(tb9);
        waitvm<0>();
        TSV(52, t_entry); TSV(55, tb9);
        const u64 tag = (((u64)ep[1] << 32) | ep[0]) + 1ull;
        u32x2 tg; tg[0] = (u32)tag; tg[1] = (u32)(tag >> 32);
        if (lane == 0) st64<SC1>(tg, rWS, W_FA + 8 * b);
        TS(39);
        {
            u32 spins = 0;
            for (;;) {
                CBAR();
                const u32x4 a = ld128<SC1>(rWS, W_FA + lane * 16), c = ld128<SC1>(rWS, W_FA + 1024 + lane * 16);
                const bool ok = a[0] == tg[0] && a[1] == tg[1] && a[2] == tg[0] && a[3] == tg[1] && c[0] == tg[0] && c[1] == tg[1] && c[2] == tg[0] && c[3] == tg[1];
                if (all_ok(ok)) break;
                POLL_STEP();
            }
            flag_st(F_RDYA, 1, lane);
            TS(40);
        }
        if (it < 160) {
            const int cb = it >> 1, kh = it & 1;
#pragma unroll
            for (int k = 0; k < 5; ++k) {
                const float cv = __uint_as_float(cs[k] << 16);
                lds_st<float>(L_SIL + (64 * k + lane) * 4, cv * __builtin_amdgcn_rcpf(1.f + __expf(-cv)));
            }
            wait_ge<true>(F_QAOK, 8, dead);
            TS(42);
            u32x4 wv[20];
            const u32 vo = (u32)(320 * kh + (lane >> 2)) * 7680u + (1280u + 32u * cb) * 2u + (lane & 3) * 16u;
#pragma unroll
            for (int k = 0; k < 20; ++k) wv[k] = ld128<NT_MR>(rm, vo + (u32)k * 16u * 7680u);
            float acc[8];
#pragma unroll
            for (int j = 0; j < 8; ++j) acc[j] = 0.f;
#pragma unroll
            for (int k = 0; k < 20; ++k) {
                const float sl = lds_ld<float>(L_SIL + (16 * k + (lane >> 2)) * 4);
#pragma unroll
                for (int j = 0; j < 4; ++j) { acc[2 * j] += sl * f_lo(wv[k][j]); acc[2 * j + 1] += sl * f_hi(wv[k][j]); }
            }
            float t4[4], t2[2];
#pragma unroll
            for (int j = 0; j < 4; ++j) t4[j] = sc32(acc[j], acc[j + 4]);
#pragma unroll
            for (int j = 0; j < 2; ++j) t2[j] = sc16(t4[j], t4[j + 2]);
#pragma unroll
            for (int j = 0; j < 2; ++j) { t2[j] += dppf<ROR(8)>(t2[j]); t2[j] += dppf<ROR(4)>(t2[j]); }
            const u32 pm = cb / 20;
#pragma unroll
            for (int j = 0; j < 2; ++j) {
                const u32 col = (cb % 20) * 32u + 8 * (lane & 3) + 4 * (lane >> 5) + 2 * ((lane >> 4) & 1) + j;
                const u32 cw = col / 80, cc = col % 80;
                const u32 pos = cc < 64 ? (cc & 7) * 8 + (cc >> 3) : 64 + (cc & 7) * 2 + ((cc >> 3) & 1);
                st32<SC1>(san(__float_as_uint(t2[j])), rWS, (lane & 12) == 0 ? W_MR + ((kh * 8u + cw) * 80u + pos) * 16u + pm * 4u : OOB);
            }
            TS(43);
        }
        return;
    }

    const int w = wave;
    const rsrc_t rX = mkrs(p_x, M_ * 2);
    u32x4 m0v, m1v; u32 c0v = 0, c1v = 0;
    const bool doS0 = typeA && w < 5;
    if (doS0) {
        const rsrc_t rc = mkrs(p_c, M_ * 2), rm = mkrs(p_wmod, M_ * 6 * M_ * 2);
        const u32 row0 = 128 * w + lane, colb = (x * 160 + 8 * r) * 2;
        m0v = ld128<NT_S0>(rm, row0 * 7680u + colb); m1v = ld128<NT_S0>(rm, (row0 + 64) * 7680u + colb);
        c0v = ld16(rc, row0 * 2); c1v = ld16(rc, (row0 + 64) * 2);
    }
    u32 xs[5]; u32 xr[2];
    const u32 ks = r >> 3;
    if (w >= 6) {
#pragma unroll
        for (int k = 0; k < 5; ++k) xs[k] = ld32<0>(rX, (64 * k + lane) * 4);
        xr[0] = ld16(rX, (160 * ks + 80 * (w - 6) + lane) * 2);
        xr[1] = ld16(rX, lane < 16 ? (160 * ks + 80 * (w - 6) + 64 + lane) * 2 : OOB);
    }
    if (w == 0) TSV(53, tbw);
    if (w == 5) TSV(56, tbw);
    if (w == 7) { TSV(57, tbw); TSV(58, t_entry); }
    if (w == 0) TS(0);
    const u64 tag = (((u64)ep[1] << 32) | ep[0]) + 1ull;
    const u32 T1 = (u32)tag, T2 = (u32)(tag >> 32);
    const u32 tagl = T1 & 0xFFu, tagh = (u32)(tag >> 8);

    if (doS0) {
        const float cA = __uint_as_float(c0v << 16), cB = __uint_as_float(c1v << 16);
        const float sA = cA * __builtin_amdgcn_rcpf(1.f + __expf(-cA)), sB = cB * __builtin_amdgcn_rcpf(1.f + __expf(-cB));
        float acc[8];
#pragma unroll
        for (int j = 0; j < 4; ++j) { acc[2 * j] = sA * f_lo(m0v[j]) + sB * f_lo(m1v[j]); acc[2 * j + 1] = sA * f_hi(m0v[j]) + sB * f_hi(m1v[j]); }
        if (w == 0) TS(31);
        float t4[4]; f32x2 t2;
#pragma unroll
        for (int j = 0; j < 4; ++j) t4[j] = sc32(acc[j], acc[j + 4]);
#pragma unroll
        for (int j = 0; j < 2; ++j) t2[j] = row_sum(sc16(t4[j], t4[j + 2]));
        if (i16 == 0) lds_st<f32x2>(L_S0P + (w * 8 + 4 * (g >> 1) + 2 * (g & 1)) * 4, t2);
        if (flag_add_ret(F_S0CNT, lane) == 4) {
            float v = 0.f;
#pragma unroll
            for (int p = 0; p < 5; ++p) v += lds_ld<float>(L_S0P + (p * 8 + (lane & 7)) * 4);
            const u32 v24 = (__float_as_uint(v) + 0x80u) >> 8;
            u32x2 gr; gr[0] = (tagl << 24) | v24; gr[1] = tagh;
            const u32 n = x * 160 + 8 * r + lane;
            const u32 off = W_G1 + (n < 640 ? n * 16 : (n - 640) * 16 + 8);
            st64<SC1>(gr, rWS, lane < 8 ? off : OOB);
            TS(1);
        }
    }

    const u32 h1 = 4 * x + ((r >> 1) & 3), half = r & 1;
    auto gr_ok = [&](const u32x4& v) { return (v[0] >> 24) == tagl && v[1] == tagh && (v[2] >> 24) == tagl && v[3] == tagh; };
    if (w >= 6) {
        float sum = 0.f;
#pragma unroll
        for (int k = 0; k < 5; ++k) sum += f_lo(xs[k]) + f_hi(xs[k]);
        const float mu = wave_sum(sum) * (1.f / 640.f);
        float sq = 0.f;
#pragma unroll
        for (int k = 0; k < 5; ++k) { const float a = f_lo(xs[k]) - mu, c = f_hi(xs[k]) - mu; sq += a * a + c * c; }
        const float rstd = 1.f / sqrtf(wave_sum(sq) * (1.f / 640.f) + 1e-5f);
        const u32 m0 = 80 * (w - 6) + lane;
        const bool has2 = lane < 16;
        const u32 o0 = W_G1 + (160 * ks + m0) * 16, o1 = has2 ? o0 + 1024 : OOB;
        PSet<2> gv, pa, pb, pc;
        ppoll<2, STG>(gv, pa, pb, pc,
            [&](PSet<2>& s) { s.v[0] = ld128<SC1>(rWS, o0); s.v[1] = ld128<SC1>(rWS, o1); },
            [&](const PSet<2>& s) { return all_ok(gr_ok(s.v[0]) && (!has2 || gr_ok(s.v[1]))); }, dead);
        if (w == 7) TS(2);
#pragma unroll
        for (int k = 0; k < 2; ++k) {
            const u32 m = m0 + 64 * k;
            const float sa = __uint_as_float(gv.v[k][0] << 8), ca = __uint_as_float(gv.v[k][2] << 8);
            const float hv = (__uint_as_float(xr[k] << 16) - mu) * rstd * (1.f + ca) + sa;
            const u32 hi = bf1(hv), lo = bf1(hv - __uint_as_float(hi << 16));
            const u32 pos = (m & ~31u) + 8 * ((m >> 2) & 3) + 4 * ((m >> 4) & 1) + (m & 3);
            if (k == 0 || has2) { lds_st<unsigned short>(L_HX + pos * 2, (unsigned short)hi); lds_st<unsigned short>(L_HX + 320 + pos * 2, (unsigned short)lo); }
        }
        flag_add(F_HX, lane);
        retire(pa, pb, pc);
    } else {
        wait_ge<true>(F_LAND, T_S1, dead);
        u32x4 bw[5];
        const u32 ab = L_S1W + (4 * g + q4) * 192 + (((2 * w + (p4 >> 1)) ^ ((g & 1) << 1)) * 16) + 8 * (p4 & 1);
#pragma unroll
        for (int t = 0; t < 5; ++t) bw[t] = cat(trd(ab + (32 * t) * 192), trd(ab + (32 * t + 16) * 192));
        flag_add(F_S1DONE, lane);
        if (w == 0) TS(32);
        wait_ge<false>(F_HX, 2, dead);
        f32x4 acc = {0.f, 0.f, 0.f, 0.f};
#pragma unroll
        for (int t = 0; t < 5; ++t) acc = mfma32(lds_ld<u32x4>(L_HX + (lane & 1) * 320 + 64 * t + 16 * g), bw[t], acc);
        const float qv = acc[0] + acc[1];
        u32x2 gr; gr[0] = (tagl << 24) | ((__float_as_uint(qv) + 0x80u) >> 8); gr[1] = tagh;
        st64<SC1>(gr, rWS, lane < 16 ? W_Q2 + ((h1 * 192 + 96 * half + 16 * w + lane) * 4u + ks) * 8u : OOB);
        if (w == 0) TS(3);

        wait_ge<true>(F_LAND, T_S2, dead);
        u32x4 bw2[6];
        const u32 rowb = L_X + (16 * w + i16) * 384, sw = ((i16 >> 1) & 3) << 1;
#pragma unroll
        for (int t = 0; t < 6; ++t) bw2[t] = lds_ld<u32x4>(rowb + (((4 * t + g) ^ sw) * 16));
        flag_add(F_S2DONE, lane);
        if (w == 0) TS(33);
        PSet<1> qv1, pa, pb, pc;
        const u32 qo = W_Q2 + (u32)r * 6144u + (64 * w + lane) * 16u;
        ppoll<1, STG>(qv1, pa, pb, pc,
            [&](PSet<1>& s) { s.v[0] = ld128<SC1>(rWS, qo); },
            [&](const PSet<1>& s) { return all_ok(gr_ok(s.v[0])); }, dead);
        if (w == 0) TS(4);
        {
            float q = __uint_as_float(qv1.v[0][0] << 8) + __uint_as_float(qv1.v[0][2] << 8);
            q += dppf<QX1>(q);
            const u32 hi = bf1(q), lo = bf1(q - __uint_as_float(hi << 16));
            lds_st<unsigned short>(L_QH + (lane & 1) * 384 + (32 * w + (lane >> 1)) * 2, (unsigned short)((lane & 1) ? lo : hi));
        }
        flag_add(F_QH, lane);
        wait_ge<false>(F_QH, 6, dead);
        f32x4 a0 = {0.f, 0.f, 0.f, 0.f}, a1 = {0.f, 0.f, 0.f, 0.f};
#pragma unroll
        for (int t = 0; t < 6; t += 2) {
            a0 = mfma32(lds_ld<u32x4>(L_QH + (lane & 1) * 384 + 64 * t + 16 * g), bw2[t], a0);
            a1 = mfma32(lds_ld<u32x4>(L_QH + (lane & 1) * 384 + 64 * (t + 1) + 16 * g), bw2[t + 1], a1);
        }
        const float qa = ((a0[0] + a0[1]) + (a1[0] + a1[1])) * (0.07216878364870322f * 1.4426950408889634f);
        const u32 hi = bf1(qa), lo = bf1(qa - __uint_as_float(hi << 16));
        const u32x4 o = pack_row(g == 0 ? hi : lo);
        const u32 ch = 2 * w + ((lane >> 3) & 1);
        const u32 dst = W_QA + ((((r >> 4) * 8 + x) * 6 + (ch >> 2) + 3 * (g & 1)) * 64u + (ch & 3) * 16 + (r & 15)) * 16u;
        wait_ge<false>(F_RDYA, 1, dead);
        st128<SC1>(o, rWS, (g < 2 && (lane & 7) == 0) ? dst : OOB);
        if (w == 0) TS(5);
        retire(pa, pb, pc);
    }

    const u32 g2 = r >> 4;
    const u32 blk = L_A + BLK * w;
    u32x4 vt[6][2];
    {
        u32x4 bq[6];
        const u32 qoff = W_QA + (g2 * 8 + w) * 6144u + lane * 16u;
        wait_ge<false>(F_RDYA, 1, dead);
#pragma unroll
        for (int k = 0; k < 6; ++k) bq[k] = ld128<SC1>(rWS, qoff + k * 1024);
        CBAR();
        wait_ge<true>(F_LAND, T_TILE0 + 12 * (w + 1), dead);
        if (w == 0) TS(6);
        if (w == 7) TS(27);
        u32x4 av[4][3];
        const u32 ar = blk + i16 * 192, asw = ((i16 >> 2) & 1) << 1;
#pragma unroll
        for (int kb = 0; kb < 4; ++kb)
#pragma unroll
            for (int t = 0; t < 3; ++t) av[kb][t] = lds_ld<u32x4>(ar + kb * 16 * 192 + ((4 * t + g) ^ asw) * 16);
        const u32 vb = blk + (4 * g + q4) * 192 + (p4 >> 1) * 16 + 8 * (p4 & 1);
#pragma unroll
        for (int u = 0; u < 6; ++u) {
            const u32 cu = vb + (u ^ (g & 1)) * 32;
#pragma unroll
            for (int t = 0; t < 2; ++t) vt[u][t] = cat(trd(cu + (32 * t) * 192), trd(cu + (32 * t + 16) * 192));
        }
        asm volatile("s_waitcnt lgkmcnt(0)" ::: "memory");
        flag_add(F_BLK, lane);
        if (w == 7) TS(28);
        {
            u32 mx = 0;
#pragma unroll
            for (int k = 0; k < 6; ++k) mx = mx4(mx, bq[k]);
            if (!none_sent(mx)) {
                PSet<1> q5, pa, pb, pc;
                ppoll<1, STG>(q5, pa, pb, pc,
                    [&](PSet<1>& s) { s.v[0] = ld128<SC1>(rWS, qoff + 5 * 1024); },
                    [&](const PSet<1>& s) { return none_sent(mx4(0, s.v[0])); }, dead);
                bq[5] = q5.v[0];
                if (w == 0) TS(34);
                u32 spins = 0;
                for (;;) {
                    CBAR();
#pragma unroll
                    for (int k = 0; k < 5; ++k) bq[k] = ld128<SC1>(rWS, qoff + k * 1024);
                    mx = 0;
#pragma unroll
                    for (int k = 0; k < 5; ++k) mx = mx4(mx, bq[k]);
                    if (none_sent(mx)) break;
                    POLL_STEP();
                }
                retire(pa, pb, pc);
            }
        }
        flag_add(F_QAOK, lane);
        if (w == 0) TS(7);
        if (w == 7) TS(26);
#pragma unroll
        for (int kb = 0; kb < 4; ++kb) {
            f32x4 acc = {0.f, 0.f, 0.f, 0.f};
#pragma unroll
            for (int t = 0; t < 3; ++t) { acc = mfma32(av[kb][t], bq[t], acc); acc = mfma32(av[kb][t], bq[3 + t], acc); }
            f32x2 lo2, hi2; lo2[0] = acc[0]; lo2[1] = acc[1]; hi2[0] = acc[2]; hi2[1] = acc[3];
            lds_st<f32x2>(blk + (kb * 2) * 512 + lane * 8, lo2);
            lds_st<f32x2>(blk + (kb * 2 + 1) * 512 + lane * 8, hi2);
        }
        flag_add(F_SADD, lane);
        if (w == 0) TS(20);
    }
    {
        wait_ge<false>(F_SADD, 8, dead);
        if (w == 0) TS(21);
        f32x2 s = {0.f, 0.f};
#pragma unroll
        for (int p = 0; p < 8; ++p) s += lds_ld<f32x2>(L_A + BLK * p + w * 512 + lane * 8);
        lds_st<f32x2>(L_SC + (w >> 1) * 1024 + lane * 16 + (w & 1) * 8, s);
        flag_add(F_SC, lane);
        wait_ge<false>(F_SC, 8, dead);
        if (w == 0) TS(22);
        f32x4 sc[4];
#pragma unroll
        for (int q = 0; q < 4; ++q) sc[q] = lds_ld<f32x4>(L_SC + q * 1024 + lane * 16);
        float mloc = fmaxf(fmaxf(fmaxf(sc[0][0], sc[0][1]), fmaxf(sc[0][2], sc[0][3])), fmaxf(fmaxf(sc[1][0], sc[1][1]), fmaxf(sc[1][2], sc[1][3])));
        mloc = fmaxf(mloc, fmaxf(fmaxf(fmaxf(sc[2][0], sc[2][1]), fmaxf(sc[2][2], sc[2][3])), fmaxf(fmaxf(sc[3][0], sc[3][1]), fmaxf(sc[3][2], sc[3][3]))));
        mloc = xg_max(mloc);
        u32x4 pf[2];
#pragma unroll
        for (int t = 0; t < 2; ++t)
#pragma unroll
            for (int e = 0; e < 4; ++e) {
                const f32x4 sq = sc[2 * t + (e >> 1)];
                pf[t][e] = pk2(__builtin_amdgcn_exp2f(sq[2 * (e & 1)] - mloc), __builtin_amdgcn_exp2f(sq[2 * (e & 1) + 1] - mloc));
            }
        f32x4 oa[6], lz;
#pragma unroll
        for (int u = 0; u < 6; ++u) { f32x4 z = {0.f, 0.f, 0.f, 0.f}; oa[u] = mfma32(pf[0], vt[u][0], z); oa[u] = mfma32(pf[1], vt[u][1], oa[u]); }
        {
            u32x4 one; one[0] = 0x3F803F80u; one[1] = 0x3F803F80u; one[2] = 0x3F803F80u; one[3] = 0x3F803F80u;
            f32x4 z = {0.f, 0.f, 0.f, 0.f};
            lz = mfma32(pf[0], one, z); lz = mfma32(pf[1], one, lz);
        }
        if (w == 0) TS(23);
        const u32 jj = x * 16 + (r & 15);
        const u32 orow = W_O + (((16 * g2 + 4 * g) * 8 + w) * 128 + jj) * OROW;
        u32 cx[4], cs[4];
#pragma unroll
        for (int e = 0; e < 4; ++e) {
            u32x3 d; d[0] = pk2(oa[0][e], oa[1][e]); d[1] = pk2(oa[2][e], oa[3][e]); d[2] = pk2(oa[4][e], oa[5][e]);
            st96<SC1>(d, rWS, orow + e * (8 * 128 * OROW) + i16 * 12);
            cx[e] = row_xor(d[0] ^ d[1] ^ d[2]); cs[e] = row_add(d[0] + d[1] + d[2]);
        }
        {
            const int e = i16 & 3;
            u32x4 pk;
            pk[0] = __float_as_uint(mloc);
            pk[1] = (sel4(e, __float_as_uint(lz[0]), __float_as_uint(lz[1]), __float_as_uint(lz[2]), __float_as_uint(lz[3])) & ~0xFFu) | tagl;
            pk[2] = sel4(e, cx[0], cx[1], cx[2], cx[3]) ^ pk[0] ^ pk[1] ^ T1;
            pk[3] = sel4(e, cs[0], cs[1], cs[2], cs[3]) + pk[0] + pk[1] + T2;
            st128<SC1>(pk, rWS, q4 == g ? orow + e * (8 * 128 * OROW) + 192 : OOB);
        }
        if (w == 0) TS(8);
    }

    {
        const u32 obase = W_O + ((r * 8 + x) * 128 + 16 * w) * OROW;
        const u32 poff = i16 < 4 ? obase + (4 * i16 + g) * OROW + 192 : OOB;
        PSet<1> pk, pa, pb, pc;
        ppoll<1, STG>(pk, pa, pb, pc,
            [&](PSet<1>& s) { s.v[0] = ld128<SC1>(rWS, poff); },
            [&](const PSet<1>& s) { return all_ok(i16 >= 4 || (s.v[0][1] & 0xFFu) == tagl); }, dead);
        if (w == 0) TS(35);
        u32x3 od[4];
        u32x4 bw[2][3];
        {
            u32 spins = 0;
            bool first = true;
            for (;;) {
                CBAR();
#pragma unroll
                for (int k = 0; k < 4; ++k) od[k] = ld96<SC1>(rWS, obase + (4 * k + g) * OROW + i16 * 12);
                if (!first) pk.v[0] = ld128<SC1>(rWS, poff);
                if (first && w < 6) {
                    wait_ge<true>(F_LAND, T_UV, dead);
                    const u32 sw = 2 * (g & 1) + (q4 >> 1);
#pragma unroll
                    for (int d = 0; d < 2; ++d) {
                        const u32 ub = L_X + (4 * g + q4) * 384 + (2 * ((2 * w + d) ^ sw) + (p4 >> 1)) * 16 + 8 * (p4 & 1);
#pragma unroll
                        for (int t = 0; t < 3; ++t) bw[d][t] = cat(trd(ub + (32 * t) * 384), trd(ub + (32 * t + 16) * 384));
                    }
                }
                first = false;
                u32 cx[4], cs[4];
#pragma unroll
                for (int k = 0; k < 4; ++k) { cx[k] = row_xor(od[k][0] ^ od[k][1] ^ od[k][2]); cs[k] = row_add(od[k][0] + od[k][1] + od[k][2]); }
                const u32x4 p = pk.v[0];
                const bool ok = i16 >= 4 || ((sel4(i16, cx[0], cx[1], cx[2], cx[3]) ^ p[0] ^ p[1] ^ T1) == p[2] &&
                                             (sel4(i16, cs[0], cs[1], cs[2], cs[3]) + p[0] + p[1] + T2) == p[3]);
                if (all_ok(ok)) break;
                POLL_STEP();
            }
        }
        if (w == 0) TS(9);
        const float mrow = i16 < 4 ? __uint_as_float(pk.v[0][0]) : -INFINITY;
        float Mw = fmaxf(mrow, dppf<QX1>(mrow)); Mw = fmaxf(Mw, dppf<QX2>(Mw)); Mw = xg_max(dppf<NBC(0)>(Mw));
        const float wt = __builtin_amdgcn_exp2f(mrow - Mw);
        float lw = i16 < 4 ? wt * __uint_as_float(pk.v[0][1] & ~0xFFu) : 0.f;
        lw += dppf<QX1>(lw); lw += dppf<QX2>(lw); lw = xg_sum(lw);
        float acc[6];
#pragma unroll
        for (int u = 0; u < 6; ++u) acc[u] = 0.f;
        {
            const float w0 = dppf<NBC(0)>(wt), w1 = dppf<NBC(1)>(wt), w2 = dppf<NBC(2)>(wt), w3 = dppf<NBC(3)>(wt);
#pragma unroll
            for (int j = 0; j < 3; ++j) {
                acc[2 * j] = (w0 * f_lo(od[0][j]) + w1 * f_lo(od[1][j])) + (w2 * f_lo(od[2][j]) + w3 * f_lo(od[3][j]));
                acc[2 * j + 1] = (w0 * f_hi(od[0][j]) + w1 * f_hi(od[1][j])) + (w2 * f_hi(od[2][j]) + w3 * f_hi(od[3][j]));
            }
        }
        const float y0 = sc16(sc32(acc[0], acc[3]), sc32(acc[1], acc[4]));
        const float t2 = sc32(acc[2], acc[5]);
        const float y1 = sc16(t2, t2);
        {
            const u32 u0 = 3 * (g >> 1) + (g & 1), u1 = 3 * (g >> 1) + 2;
            const u32 sw0 = ((w >> 1) & 1) << 1;
            const u32 a0 = L_AM + 2 * w * 192 + (((4 * (u0 >> 1) + q4) ^ sw0) * 16) + (4 * (u0 & 1) + p4) * 2;
            const u32 a1 = L_AM + (2 * w + (g & 1)) * 192 + (((4 * (u1 >> 1) + q4) ^ sw0) * 16) + (4 * (u1 & 1) + p4) * 2;
            const u32 h0 = bf1(y0), l0 = bf1(y0 - __uint_as_float(h0 << 16));
            const u32 h1b = bf1(y1), l1 = bf1(y1 - __uint_as_float(h1b << 16));
            lds_st<unsigned short>(a0, (unsigned short)h0); lds_st<unsigned short>(a0 + 192, (unsigned short)l0);
            lds_st<unsigned short>(a1, (unsigned short)((g & 1) ? l1 : h1b));
            f32x2 ml; ml[0] = Mw; ml[1] = lw;
            if (lane == 0) lds_st<f32x2>(L_EML + w * 8, ml);
        }
        flag_add(F_AM, lane);
        if (w < 6) {
            wait_ge<false>(F_AM, 8, dead);
            u32x4 aa[3];
#pragma unroll
            for (int t = 0; t < 3; ++t) aa[t] = lds_ld<u32x4>(L_AM + i16 * 192 + (((4 * t + g) ^ (((i16 >> 2) & 1) << 1)) * 16));
            const f32x4 em = lds_ld<f32x4>(L_EML + g * 16);
            const float Mx = xg_max(fmaxf(em[0], em[2]));
            const float fa = __builtin_amdgcn_exp2f(em[0] - Mx), fb = __builtin_amdgcn_exp2f(em[2] - Mx);
            const float rl = 1.f / xg_sum(fa * em[1] + fb * em[3]);
            float res[2];
#pragma unroll
            for (int d = 0; d < 2; ++d) {
                f32x4 z = {0.f, 0.f, 0.f, 0.f};
#pragma unroll
                for (int t = 0; t < 3; ++t) z = mfma32(aa[t], bw[d][t], z);
                res[d] = xg_sum(fa * (z[0] + z[1]) + fb * (z[2] + z[3])) * rl;
            }
            const u32x4 o = pack_row(bf1(g == 0 ? res[0] : res[1]));
            const u32 ch = 4 * w + 2 * (g & 1) + ((lane >> 3) & 1);
            const u32 wc = 2 * (r & 3) + (ch >= 12 ? 1 : 0), c12 = ch >= 12 ? ch - 12 : ch;
            const u32 dst = W_CTX + (((((r >> 2) * 8 + wc) * 3 + (c12 >> 2)) * 4 + (c12 & 3)) * 8 + x) * 16u;
            st128<SC1>(o, rWS, (g < 2 && (lane & 7) == 0) ? dst : OOB);
            if (w == 0) TS(10);
        }
        retire(pa, pb, pc);
    }

    if (typeA) {
        const u32 coff = i16 < 8 ? W_CTX + ((x * 8 + w) * 96u + g * 8 + i16) * 16u : OOB;
        wait_ge<true>(F_LAND, T_UV + 6 * (w + 1), dead);
        u32x4 bw[2][3];
        const u32 fbx = blk + B_W + (8 * g + q4) * 64 + (p4 >> 1) * 16 + 8 * (p4 & 1);
#pragma unroll
        for (int u = 0; u < 2; ++u) {
            const u32 ub = fbx + (u ^ (g & 1)) * 32;
#pragma unroll
            for (int t = 0; t < 3; ++t) bw[u][t] = cat(trd(ub + (32 * t) * 64), trd(ub + (32 * t + 4) * 64));
        }
        if (w == 0) TS(36);
        PSet<3> ca, pa, pb, pc;
        ppoll<3, STG2>(ca, pa, pb, pc,
            [&](PSet<3>& s) { s.v[0] = ld128<SC1>(rWS, coff); s.v[1] = ld128<SC1>(rWS, coff + 512); s.v[2] = ld128<SC1>(rWS, coff + 1024); },
            [&](const PSet<3>& s) { return none_sent(mx4(mx4(mx4(0, s.v[0]), s.v[1]), s.v[2])); }, dead);
        if (w == 0) TS(11);
        float res[2];
#pragma unroll
        for (int u = 0; u < 2; ++u) {
            f32x4 z = {0.f, 0.f, 0.f, 0.f};
#pragma unroll
            for (int t = 0; t < 3; ++t) z = mfma32(ca.v[t], bw[u][t], z);
            res[u] = xg_sum((z[0] + z[1]) + (z[2] + z[3]));
        }
        if (g < 2) lds_st<float>(L_FRP + (w * 32 + 16 * g + i16) * 4, g == 0 ? res[0] : res[1]);
        if (flag_add_ret(F_FRC, lane) == 7) {
            float v = 0.f;
#pragma unroll
            for (int p = 0; p < 8; ++p) v += lds_ld<float>(L_FRP + (p * 32 + (lane & 31)) * 4);
            const u32x4 o = pack_row(bf1(v));
            const u32 dst = W_AT + (x * 640u + 32u * r + 16 * (g & 1) + (lane & 8)) * 2u;
            st128<SC1>(o, rWS, (g < 2 && (lane & 7) == 0) ? dst : OOB);
            TS(12);
        }
        retire(pa, pb, pc);
    } else {
        const u32 fb = x * 12 + (r - 20);
        const u32 cA = 8 * (lane & 7) + (lane >> 3);
        const bool hasB = (lane & 7) < 2;
        const u32 colA = 80 * w + cA, colB = colA + 64;
        float ga[2], sb[2], cb1[2], gb[2], xv[2];
        {
            xv[0] = __uint_as_float(ld16(rX, colA * 2) << 16); xv[1] = __uint_as_float(ld16(rX, hasB ? colB * 2 : OOB) << 16);
            u32x4 mr[2][2]; u32 spins = 0;
            const u32 posB = 64 + (lane >> 3) * 2 + (lane & 1);
            for (;;) {
                CBAR();
                u32 mx = 0;
#pragma unroll
                for (int kh = 0; kh < 2; ++kh) {
                    mr[kh][0] = ld128<SC1>(rWS, W_MR + ((kh * 8u + w) * 80u + lane) * 16u);
                    mr[kh][1] = ld128<SC1>(rWS, hasB ? W_MR + ((kh * 8u + w) * 80u + posB) * 16u : OOB);
                    mx = mx4(mx4(mx, mr[kh][0]), mr[kh][1]);
                }
                if (none_sent(mx)) break;
                POLL_STEP();
            }
#pragma unroll
            for (int c = 0; c < 2; ++c) {
                ga[c] = __uint_as_float(mr[0][c][0]) + __uint_as_float(mr[1][c][0]);
                sb[c] = __uint_as_float(mr[0][c][1]) + __uint_as_float(mr[1][c][1]);
                cb1[c] = 1.f + (__uint_as_float(mr[0][c][2]) + __uint_as_float(mr[1][c][2]));
                gb[c] = __uint_as_float(mr[0][c][3]) + __uint_as_float(mr[1][c][3]);
            }
        }
        if (w == 0) TS(13);
        const u32 gab = blk + B_GA;
#pragma unroll
        for (int c = 0; c < 2; ++c) {
            if (c == 0 || hasB) {
                const u32 cc = cA + 64 * c;
                const u32 h = bf1(cb1[c]), l = bf1(cb1[c] - __uint_as_float(h << 16)), h2 = bf1(sb[c]), l2 = bf1(sb[c] - __uint_as_float(h2 << 16));
                lds_st<unsigned short>(gab + 2 * 160 + cc * 2, (unsigned short)h); lds_st<unsigned short>(gab + 3 * 160 + cc * 2, (unsigned short)l);
                lds_st<unsigned short>(gab + 4 * 160 + cc * 2, (unsigned short)h2); lds_st<unsigned short>(gab + 5 * 160 + cc * 2, (unsigned short)l2);
                lds_st<float>(blk + B_GB + cc * 4, gb[c]);
            }
        }
        wait_ge<true>(F_LAND, T_UV + 6 * (w + 1), dead);
        u32x2 b1[5], b2[5];
#pragma unroll
        for (int t = 0; t < 5; ++t) b1[t] = trd(blk + B_W + 512 * t + 8 * lane);
#pragma unroll
        for (int u = 0; u < 5; ++u) b2[u] = trd(blk + B_W2 + (4 * g + q4) * 160 + 32 * u + 8 * p4);
        if (w == 0) TS(36);
        const u32 aoff = W_AT + ((lane >> 3) * 640u + 80u * w + 8 * (lane & 7)) * 2u, aoff2 = hasB ? aoff + 128 : OOB;
        PSet<2> at, pa, pb, pc;
        ppoll<2, STG2>(at, pa, pb, pc,
            [&](PSet<2>& s) { s.v[0] = ld128<SC1>(rWS, aoff); s.v[1] = ld128<SC1>(rWS, aoff2); },
            [&](const PSet<2>& s) { return none_sent(mx4(mx4(0, s.v[0]), s.v[1])); }, dead);
        if (w == 0) TS(14);
        float x1[2];
#pragma unroll
        for (int c = 0; c < 2; ++c) {
            const u32x4 a = at.v[c];
            float v[8];
#pragma unroll
            for (int j = 0; j < 4; ++j) { v[2 * j] = f_lo(a[j]); v[2 * j + 1] = f_hi(a[j]); }
            float t4[4], t2[2];
#pragma unroll
            for (int j = 0; j < 4; ++j) t4[j] = sc32(v[j], v[j + 4]);
#pragma unroll
            for (int j = 0; j < 2; ++j) t2[j] = sc16(t4[j], t4[j + 2]);
            const bool b3 = (lane & 8) != 0;
            const float keep = b3 ? t2[1] : t2[0], give = b3 ? t2[0] : t2[1];
            const float attn = keep + dppf<ROR(8)>(give);
            x1[c] = xv[c] + ga[c] * attn;
        }
        if (!hasB) x1[1] = 0.f;
#pragma unroll
        for (int c = 0; c < 2; ++c) {
            if (c == 0 || hasB) {
                const u32 cc = cA + 64 * c;
                const float uv = x1[c] * cb1[c];
                const u32 h = bf1(uv), l = bf1(uv - __uint_as_float(h << 16));
                lds_st<unsigned short>(gab + cc * 2, (unsigned short)h); lds_st<unsigned short>(gab + 160 + cc * 2, (unsigned short)l);
                lds_st<float>(blk + B_X1 + cc * 4, x1[c]);
            }
        }
        CBAR();
        f32x4 z = {0.f, 0.f, 0.f, 0.f};
        const u32 arow = (q4 < 2 ? q4 : 2) * 2 + (lane & 1);
#pragma unroll
        for (int t = 0; t < 5; ++t) z = mfma16(lds_ld<u32x2>(gab + arow * 160 + 32 * t + 8 * g), b1[t], z);
        const float s1 = wave_sum(x1[0] + x1[1]), s2 = wave_sum(x1[0] * x1[0] + x1[1] * x1[1]);
        lds_st<float>(L_GRP + w * 256 + lane * 4, g < 3 ? z[0] + z[1] : (p4 == 0 ? s1 : (p4 == 1 ? s2 : 0.f)));
        flag_add(F_GRC, lane);
        wait_ge<false>(F_GRC, 8, dead);
        f32x4 gs = lds_ld<f32x4>(L_GRP + (2 * p4) * 256 + q4 * 64 + g * 16) + lds_ld<f32x4>(L_GRP + (2 * p4 + 1) * 256 + q4 * 64 + g * 16);
#pragma unroll
        for (int e = 0; e < 4; ++e) { gs[e] += dppf<QX1>(gs[e]); gs[e] += dppf<QX2>(gs[e]); }
        const float mu = dppf<NBC(12)>(gs[0]) * (1.f / 640.f);
        const float var = fmaxf(dppf<NBC(12)>(gs[1]) * (1.f / 640.f) - mu * mu, 0.f);
        const float rstd = 1.f / sqrtf(var + 1e-5f);
        float av[4];
#pragma unroll
        for (int e = 0; e < 4; ++e) av[e] = fmaxf(rstd * (dppf<NBC(0)>(gs[e]) - mu * dppf<NBC(4)>(gs[e])) + dppf<NBC(8)>(gs[e]), 0.f);
        u32x2 af; { const u32 h0 = pk2(av[0], av[1]), h1b = pk2(av[2], av[3]);
            const u32 l0 = pk2(av[0] - f_lo(h0), av[1] - f_hi(h0)), l1 = pk2(av[2] - f_lo(h1b), av[3] - f_hi(h1b));
            af[0] = (lane & 1) ? l0 : h0; af[1] = (lane & 1) ? l1 : h1b; }
        float fo[5];
#pragma unroll
        for (int u = 0; u < 5; ++u) { f32x4 y = {0.f, 0.f, 0.f, 0.f}; y = mfma16(af, b2[u], y); fo[u] = y[0] + y[1]; }
        {
            const float v = g == 0 ? fo[0] : (g == 1 ? fo[1] : (g == 2 ? fo[2] : fo[3]));
            const u32x4 o = pack_row(bf1(v)), o2 = pack_row(bf1(fo[4]));
            st128<SC1>(o, rWS, (lane & 7) == 0 ? W_FP + (((5 * w + g) * 96u + fb) * 16 + (lane & 8)) * 2u : OOB);
            st128<SC1>(o2, rWS, (lane & 55) == 0 ? W_FP + (((5 * w + 4) * 96u + fb) * 16 + (lane & 8)) * 2u : OOB);
        }
        if (w == 0) TS(15);
        const int ci = (r - 20) * 8 + x;
        if (ci < 40 && ci / 5 == w) {
            const u32 fo0 = W_FP + (u32)ci * 3072u + lane * 16;
            PSet<3> fv, qa, qb, qc;
            ppoll<3, STG2>(fv, qa, qb, qc,
                [&](PSet<3>& s) { s.v[0] = ld128<SC1>(rWS, fo0); s.v[1] = ld128<SC1>(rWS, fo0 + 1024); s.v[2] = ld128<SC1>(rWS, fo0 + 2048); },
                [&](const PSet<3>& s) { return none_sent(mx4(mx4(mx4(0, s.v[0]), s.v[1]), s.v[2])); }, dead);
            TS(16);
            float v[8];
#pragma unroll
            for (int j = 0; j < 4; ++j) { v[2 * j] = (f_lo(fv.v[0][j]) + f_lo(fv.v[1][j])) + f_lo(fv.v[2][j]); v[2 * j + 1] = (f_hi(fv.v[0][j]) + f_hi(fv.v[1][j])) + f_hi(fv.v[2][j]); }
            float t4[4], t2[2];
#pragma unroll
            for (int j = 0; j < 4; ++j) t4[j] = sc32(v[j], v[j + 4]);
#pragma unroll
            for (int j = 0; j < 2; ++j) t2[j] = sc16(t4[j], t4[j + 2]);
            const bool b3 = (lane & 8) != 0;
            const float keep = b3 ? t2[1] : t2[0], give = b3 ? t2[0] : t2[1];
            float f = keep + dppf<ROR(8)>(give);
            f += dppf<SHL(4)>(f);
            f += dppf<SHL(2)>(f);
            const u32 cc = 16 * (ci % 5) + 8 * (lane & 1) + (lane >> 3);
            const float o = lds_ld<float>(blk + B_X1 + cc * 4) + lds_ld<float>(blk + B_GB + cc * 4) * f;
            const rsrc_t rO = mkrs(p_out, M_ * 2);
            __builtin_amdgcn_raw_buffer_store_b16((unsigned short)bf1(o), rO, (lane & 6) == 0 ? (16 * ci + 8 * (lane & 1) + (lane >> 3)) * 2 : OOB, 0, 0);
            if (ci == 0 && lane == 0) { u32x2 e; e[0] = T1; e[1] = T2; st64<SC1>(e, rWS, W_EPOCH); }
            TS(17);
        }
    }
}

extern "C" void kernel_launch(void* const* d_in, const int* in_sizes, int n_in, void* d_out, int out_size, void* d_ws, size_t ws_size, hipStream_t stream)
{
    (void)in_sizes; (void)n_in; (void)out_size; (void)ws_size;
    (void)hipFuncSetAttribute((const void*)adaln_mla_ffn_kernel, hipFuncAttributeMaxDynamicSharedMemorySize, (int)LDS_BYTES);
    hipLaunchKernelGGL(adaln_mla_ffn_kernel, dim3(256), dim3(640), LDS_BYTES, stream,
                       (const void*)d_in[0], (const void*)d_in[1], (const void*)d_in[2], (const void*)d_in[3], (const void*)d_in[4], (const void*)d_in[5],
                       (const void*)d_in[7], (const void*)d_in[8], (const void*)d_in[9], (const void*)d_in[10], d_out, d_ws);
}
```

```cpp
#include <hip/hip_runtime.h>
#include <hip/hip_bf16.h>
#include <stdint.h>

#define LAS __attribute__((address_space(3)))
#define DI __device__ __forceinline__
typedef unsigned int u32;
typedef unsigned long long u64;
typedef u32 u32x2 __attribute__((ext_vector_type(2)));
typedef u32 u32x3 __attribute__((ext_vector_type(3)));
typedef u32 u32x4 __attribute__((ext_vector_type(4)));
typedef float f32x2 __attribute__((ext_vector_type(2)));
typedef float f32x4 __attribute__((ext_vector_type(4)));
typedef short s16x4 __attribute__((ext_vector_type(4)));
typedef __bf16 bf16x2 __attribute__((ext_vector_type(2)));
typedef __bf16 bf16x8 __attribute__((ext_vector_type(8)));
typedef __amdgpu_buffer_rsrc_t rsrc_t;

#define WIN 18
#define WIN2 18
#define PAUSE 0
#define STG 6
#define STG2 3
#define NT_S0 0
#define NT_MR 2
#define NT_Q 1
#define NT_UK 1
#define NT_KV 1
#define NT_UV 1
#define NT_O 1
#define NT_W1 1
#define NT_W2 1
#define O_BASE 1048576u

constexpr int M_ = 640, H_ = 32, D_ = 192, DC = 768, SK = 8192, DFF = 1536;
constexpr u32 SENT = 0xFFFFFFFFu;
constexpr u32 OOB = 0x80000000u;

constexpr u32 W_EPOCH = 0;
constexpr u32 W_FA = 256;
constexpr u32 W_G1 = 4096;
constexpr u32 W_Q2 = 32768;
constexpr u32 W_SMALL = W_Q2 + 98304;
constexpr u32 W_QA = W_SMALL;
constexpr u32 W_CTX = W_QA + 49152;
constexpr u32 W_AT = W_CTX + 98304;
constexpr u32 W_FP = W_AT + 10240;
constexpr u32 W_MR = W_FP + 122880;
constexpr u32 W_SMALL_END = W_MR + 20480;
constexpr u32 SMALL_CH = (W_SMALL_END - W_SMALL) / 16;
constexpr u32 SMALL_PER = (SMALL_CH + 255) / 256;
constexpr u32 OROW = 208;
constexpr u32 W_O = O_BASE;
constexpr u32 O_BYTES = 32u * 8 * 128 * OROW;
constexpr u32 W_PROF = W_SMALL_END;
constexpr u32 W_END = W_O + O_BYTES;
static_assert(W_PROF + 256 * 64 * 8 <= W_O, "ws map");
static_assert(SMALL_PER <= 128, "prefill");
static_assert(W_END <= 268435456u, "ws map");

constexpr u32 L_A = 0;
constexpr u32 BLK = 12288;
constexpr u32 L_S1W = 5 * BLK;
constexpr u32 L_X = 98304;
constexpr u32 L_Y = 135168;
constexpr u32 L_FLG = L_Y;
constexpr u32 L_S0P = L_Y + 13568;
constexpr u32 L_EML = L_Y + 448;
constexpr u32 L_FRP = L_Y + 512;
constexpr u32 L_GRP = L_Y + 1536;
constexpr u32 L_SC = L_Y + 3584;
constexpr u32 L_HX = L_Y + 7680;
constexpr u32 L_QH = L_Y + 8448;
constexpr u32 L_AM = L_Y + 9216;
constexpr u32 L_SIL = L_Y + 12288;
constexpr u32 LDS_BYTES = 163840;
constexpr u32 B_W = 4096, B_W2 = 7168, B_GA = 10240, B_X1 = 11200, B_GB = 11520;

enum { F_LAND = 0, F_RDYA, F_S0CNT, F_HX, F_S1DONE, F_QH, F_S2DONE, F_BLK, F_QAOK, F_SADD, F_SC, F_AM, F_FRC, F_GRC };

constexpr u32 CAP_G = 20000;
constexpr u32 CAP_L = 400000;

constexpr u32 T_S1 = 30, T_S2 = 66, T_TILE0 = 66, T_UV = 198;

DI rsrc_t mkrs(const void* p, u32 bytes) { return __builtin_amdgcn_make_buffer_rsrc((void*)p, (short)0, (int)bytes, 0x00020000); }
template <int AUX> DI u32x4 ld128(rsrc_t r, u32 off) { return __builtin_amdgcn_raw_buffer_load_b128(r, off, 0, AUX); }
template <int AUX> DI u32x3 ld96(rsrc_t r, u32 off) { return __builtin_amdgcn_raw_buffer_load_b96(r, off, 0, AUX); }
template <int AUX> DI u32x2 ld64(rsrc_t r, u32 off) { return __builtin_amdgcn_raw_buffer_load_b64(r, off, 0, AUX); }
template <int AUX> DI u32 ld32(rsrc_t r, u32 off) { return __builtin_amdgcn_raw_buffer_load_b32(r, off, 0, AUX); }
DI u32 ld16(rsrc_t r, u32 off) { return (u32)__builtin_amdgcn_raw_buffer_load_b16(r, off, 0, 0); }
template <int AUX> DI void st128(u32x4 v, rsrc_t r, u32 off) { __builtin_amdgcn_raw_buffer_store_b128(v, r, off, 0, AUX); }
template <int AUX> DI void st96(u32x3 v, rsrc_t r, u32 off) { __builtin_amdgcn_raw_buffer_store_b96(v, r, off, 0, AUX); }
template <int AUX> DI void st64(u32x2 v, rsrc_t r, u32 off) { __builtin_amdgcn_raw_buffer_store_b64(v, r, off, 0, AUX); }
template <int AUX> DI void st32(u32 v, rsrc_t r, u32 off) { __builtin_amdgcn_raw_buffer_store_b32(v, r, off, 0, AUX); }
constexpr int SC1 = 16, NT = 2;

DI float f_lo(u32 v) { return __uint_as_float(v << 16); }
DI float f_hi(u32 v) { return __uint_as_float(v & 0xFFFF0000u); }
DI u32 pk2(float a, float b) { bf16x2 v; v[0] = (__bf16)a; v[1] = (__bf16)b; return __builtin_bit_cast(u32, v); }
DI u32 bf1(float a) { return pk2(a, 0.f) & 0xFFFFu; }
DI u32 san(u32 v) { return v < 0xFFFFFFFEu ? v : 0xFFFFFFFEu; }
DI u32x4 san4(u32x4 v) { u32x4 r; r[0] = san(v[0]); r[1] = san(v[1]); r[2] = san(v[2]); r[3] = san(v[3]); return r; }
DI u32 umax(u32 a, u32 b) { return a > b ? a : b; }
DI u32 mx4(u32 acc, u32x4 v) { return umax(umax(acc, umax(v[0], v[1])), umax(v[2], v[3])); }

DI u32 tagw(float v, u32 tg) { return ((__float_as_uint(v) + 0x800u) & 0xFFFFF000u) | tg; }
DI float untag(u32 v) { return __uint_as_float(v & 0xFFFFF000u); }
DI bool tag_ok(u32x4 v, u32 tg) { return ((((v[0] ^ tg) | (v[1] ^ tg)) | ((v[2] ^ tg) | (v[3] ^ tg))) & 0xFFFu) == 0u; }
DI float dot2(u32 a, u32 b, float c) { return __builtin_amdgcn_fdot2_f32_bf16(__builtin_bit_cast(bf16x2, a), __builtin_bit_cast(bf16x2, b), c, false); }
template <int CTRL> DI u32 dppu(u32 v) { return (u32)__builtin_amdgcn_update_dpp(0, (int)v, CTRL, 0xF, 0xF, true); }
template <int CTRL> DI float dppf(float v) { return __uint_as_float(dppu<CTRL>(__float_as_uint(v))); }
constexpr int ROR(int n) { return 0x120 + n; }
constexpr int SHL(int n) { return 0x100 + n; }
constexpr int NBC(int n) { return 0x150 + n; }
constexpr int QX1 = 0xB1, QX2 = 0x4E;
DI float row_sum(float v) { v += dppf<ROR(8)>(v); v += dppf<ROR(4)>(v); v += dppf<ROR(2)>(v); v += dppf<ROR(1)>(v); return v; }
DI u32 row_xor(u32 v) { v ^= dppu<ROR(8)>(v); v ^= dppu<ROR(4)>(v); v ^= dppu<ROR(2)>(v); v ^= dppu<ROR(1)>(v); return v; }
DI u32 row_add(u32 v) { v += dppu<ROR(8)>(v); v += dppu<ROR(4)>(v); v += dppu<ROR(2)>(v); v += dppu<ROR(1)>(v); return v; }
DI void swap16(float& a, float& b) { auto r = __builtin_amdgcn_permlane16_swap(__float_as_uint(a), __float_as_uint(b), false, false); a = __uint_as_float(r[0]); b = __uint_as_float(r[1]); }
DI void swap32(float& a, float& b) { auto r = __builtin_amdgcn_permlane32_swap(__float_as_uint(a), __float_as_uint(b), false, false); a = __uint_as_float(r[0]); b = __uint_as_float(r[1]); }
DI float xg_sum(float v) { float a = v, b = v; swap16(a, b); v = a + b; a = v; b = v; swap32(a, b); return a + b; }
DI float xg_max(float v) { float a = v, b = v; swap16(a, b); v = fmaxf(a, b); a = v; b = v; swap32(a, b); return fmaxf(a, b); }
DI float wave_sum(float v) { return xg_sum(row_sum(v)); }
DI float sc32(float a, float b) { swap32(a, b); return a + b; }
DI float sc16(float a, float b) { swap16(a, b); return a + b; }
DI u32 sel4(int e, u32 a, u32 b, u32 c, u32 d) { const u32 lo = (e & 1) ? b : a, hi = (e & 1) ? d : c; return (e & 2) ? hi : lo; }

DI f32x4 mfma32(u32x4 a, u32x4 b, f32x4 c) { return __builtin_amdgcn_mfma_f32_16x16x32_bf16(__builtin_bit_cast(bf16x8, a), __builtin_bit_cast(bf16x8, b), c, 0, 0, 0); }
DI f32x4 mfma16(u32x2 a, u32x2 b, f32x4 c) { return __builtin_amdgcn_mfma_f32_16x16x16bf16_1k(__builtin_bit_cast(s16x4, a), __builtin_bit_cast(s16x4, b), c, 0, 0, 0); }
DI u32x2 trd(u32 addr) { return __builtin_bit_cast(u32x2, __builtin_amdgcn_ds_read_tr16_b64_v4i16((LAS s16x4*)addr)); }
DI u32x4 cat(u32x2 a, u32x2 b) { u32x4 r; r[0] = a[0]; r[1] = a[1]; r[2] = b[0]; r[3] = b[1]; return r; }
template <class T> DI T lds_ld(u32 addr) { return *(LAS T*)addr; }
template <class T> DI void lds_st(u32 addr, T v) { *(LAS T*)addr = v; }
#define CBAR() asm volatile("" ::: "memory")

DI u32 flag_ld(int i) { return (u32)__builtin_amdgcn_readfirstlane((int)*(volatile LAS u32*)(L_FLG + 4 * i)); }
DI void flag_st(int i, u32 v, int lane) { CBAR(); if (lane == 0) *(volatile LAS u32*)(L_FLG + 4 * i) = v; CBAR(); }
DI void flag_add(int i, int lane) { CBAR(); if (lane == 0) __hip_atomic_fetch_add((LAS u32*)(L_FLG + 4 * i), 1u, __ATOMIC_RELAXED, __HIP_MEMORY_SCOPE_WORKGROUP); CBAR(); }
DI u32 flag_add_ret(int i, int lane) {
    CBAR(); u32 o = 0;
    if (lane == 0) o = __hip_atomic_fetch_add((LAS u32*)(L_FLG + 4 * i), 1u, __ATOMIC_RELAXED, __HIP_MEMORY_SCOPE_WORKGROUP);
    o = (u32)__builtin_amdgcn_readfirstlane((int)o); CBAR(); return o;
}
template <bool SLEEP> DI void wait_ge(int i, u32 target, bool& dead) {
    u32 n = 0;
    while (!dead && flag_ld(i) < target) { if (++n > CAP_L) dead = true; if (SLEEP) __builtin_amdgcn_s_sleep(1); }
    CBAR();
}
DI bool all_ok(bool ok) { return __builtin_amdgcn_ballot_w64(!ok) == 0ull; }
DI bool none_sent(u32 mx) { return __builtin_amdgcn_ballot_w64(mx == SENT) == 0ull; }
#define POLL_STEP() { if (++spins > CAP_G) { dead = true; break; } __builtin_amdgcn_s_sleep(1); }

template <int N> struct PSet { u32x4 v[N]; };
template <int N> DI void take(PSet<N>& res, const PSet<N>& s) {
#pragma unroll
    for (int k = 0; k < N; ++k) res.v[k] = s.v[k];
}
#define PIPE 0
template <int N, int STAGGER, class LD, class OK>
DI void ppoll(PSet<N>& res, PSet<N>& a, PSet<N>& b, PSet<N>& c, LD ld, OK ok, bool& dead) {
    (void)a; (void)b; (void)c;
    for (u32 spins = 0;;) {
        CBAR(); ld(res); CBAR();
        if (ok(res)) break;
        if (++spins > CAP_G) { dead = true; break; }
    }
    CBAR();
}
template <int N> DI void retire(PSet<N>&, PSet<N>&, PSet<N>&) {}

#define TS(i) do { } while (0)
#define TCAP(v) do { } while (0)
#define TSV(i, t_) do { } while (0)

DI u32x4 pack_row(u32 v16) {
    u32 pr = v16 | (dppu<SHL(1)>(v16) << 16);
    u32x4 r; r[0] = pr; r[1] = dppu<SHL(2)>(pr); r[2] = dppu<SHL(4)>(pr); r[3] = dppu<SHL(6)>(pr);
    return san4(r);
}

template <int NTL> DI void dma(u32 lds, u32 voff, rsrc_t rs, u32 soff) {
    if constexpr (NTL) asm volatile("s_nop 4\n\ts_mov_b32 m0, %0\n\ts_nop 0\n\tbuffer_load_dwordx4 %1, %2, %3 offen nt lds" ::"s"(lds), "v"(voff), "s"(rs), "s"(soff) : "memory");
    else asm volatile("s_nop 4\n\ts_mov_b32 m0, %0\n\ts_nop 0\n\tbuffer_load_dwordx4 %1, %2, %3 offen lds" ::"s"(lds), "v"(voff), "s"(rs), "s"(soff) : "memory");
}
template <int N> DI void waitvm() { asm volatile("s_waitcnt vmcnt(%0)" ::"i"(N) : "memory"); }
struct Ldr { int issued; int pub; int lane; bool go; bool dead; };
DI void publish(Ldr& s, int landed) { if (s.go && landed > s.pub) { s.pub = landed; flag_st(F_LAND, (u32)landed, s.lane); } }
template <int W> DI void step3(Ldr& s) { s.issued += 3; waitvm<W>(); publish(s, s.issued - W); }
template <int N> DI void drain(Ldr& s) { waitvm<N>(); publish(s, s.issued - N); if constexpr (N >= 3) drain<N - 3>(s); }
template <int W> DI void park(Ldr& s, int flag, u32 target) {
    if (flag_ld(flag) < target) { drain<W - 3>(s); wait_ge<true>(flag, target, s.dead); }
    CBAR();
}
template <int W, int NTL> DI void seg3(Ldr& s, u32 lds0, const u32 (&v)[3], rsrc_t rs, u32 soff0, u32 sstep, int ng) {
    for (int g = 0; g < ng; ++g) {
        const u32 so = soff0 + (u32)g * sstep, ld = lds0 + 3072u * (u32)g;
        dma<NTL>(ld, v[0], rs, so); dma<NTL>(ld + 1024, v[1], rs, so); dma<NTL>(ld + 2048, v[2], rs, so);
        step3<W>(s);
    }
}

extern __shared__ __attribute__((aligned(16))) char smem_dyn[];

__global__ void __launch_bounds__(640, 3)
adaln_mla_ffn_kernel(const void* __restrict__ p_x, const void* __restrict__ p_ckv, const void* __restrict__ p_wq, const void* __restrict__ p_wuk,
                     const void* __restrict__ p_wuv, const void* __restrict__ p_wo, const void* __restrict__ p_w1, const void* __restrict__ p_w2,
                     const void* __restrict__ p_c, const void* __restrict__ p_wmod, void* __restrict__ p_out, void* __restrict__ p_ws)
{
    const int tid = threadIdx.x, lane = tid & 63;
    const int wave = __builtin_amdgcn_readfirstlane(tid >> 6);
    const int b = blockIdx.x, x = b & 7, r = b >> 3;
    const bool typeA = r < 20;
    const int g = lane >> 4, i16 = lane & 15, q4 = i16 >> 2, p4 = lane & 3;
    bool dead = false;
    (void)smem_dyn;
    if (tid < 16) { u32x4 z; z[0] = 0; z[1] = 0; z[2] = 0; z[3] = 0; lds_st<u32x4>(L_FLG + tid * 16, z); }
    asm volatile("" ::"s"(p_x), "s"(p_ckv), "s"(p_wq), "s"(p_wuk), "s"(p_wuv), "s"(p_wo), "s"(p_w1), "s"(p_w2), "s"(p_c), "s"(p_wmod), "s"(p_out), "s"(p_ws));
    asm volatile("s_waitcnt lgkmcnt(0)" ::: "memory");
    __builtin_amdgcn_s_barrier();
    CBAR();
    TCAP(tbw);
    const rsrc_t rWS = mkrs(p_ws, W_END);

    if (wave == 8) {
        Ldr s; s.issued = 0; s.pub = 0; s.lane = lane; s.go = true; s.dead = false;
        u32 v1[3], vt[3], vu[3];
#pragma unroll
        for (int k = 0; k < 3; ++k) {
            const u32 p = 64 * k + lane;
            const u32 row = p / 12, ch = (p % 12) ^ (((row >> 2) & 1) << 1);
            v1[k] = row * 12288u + ch * 16; vt[k] = row * 1536u + ch * 16;
            const u32 r2 = p / 24, c2 = (p % 24) ^ (((r2 >> 1) & 3) << 1);
            vu[k] = r2 * 12288u + c2 * 16;
        }
        const rsrc_t rq = mkrs(p_wq, M_ * H_ * D_ * 2), ruk = mkrs(p_wuk, DC * H_ * D_ * 2), ruv = mkrs(p_wuv, DC * H_ * D_ * 2), rkv = mkrs(p_ckv, SK * DC * 2);
        const u32 ks = r >> 3, h1 = 4 * x + ((r >> 1) & 3), half = r & 1;
        const u32 so1 = 160u * ks * 12288u + (h1 * 192 + 96 * half) * 2;
        TCAP(tb8);
        seg3<WIN, NT_Q>(s, L_S1W, v1, rq, so1, 16u * 12288u, 10);
        TS(44); TSV(51, t_entry); TSV(54, tb8);
        const u32 so2 = 96u * x * 12288u + (u32)r * 384u;
        seg3<WIN, NT_UK>(s, L_X, vu, ruk, so2, 8u * 12288u, 12);
        TS(45);
        const u32 jt = x + 8 * (r & 15);
        const u32 sot = 64u * jt * 1536u;
        for (int w = 0; w < 5; ++w) seg3<WIN, NT_KV>(s, L_A + BLK * w, vt, rkv, sot + 192u * w, 16u * 1536u, 4);
        park<WIN>(s, F_S1DONE, 6);
        for (int w = 5; w < 8; ++w) seg3<WIN, NT_KV>(s, L_A + BLK * w, vt, rkv, sot + 192u * w, 16u * 1536u, 4);
        TS(46);
        if (PAUSE) { drain<WIN - 3>(s); TS(47); wait_ge<true>(F_QAOK, 8, s.dead); }
        park<WIN>(s, F_S2DONE, 6);
        seg3<WIN2, NT_UV>(s, L_X, vu, ruv, so2, 8u * 12288u, 12);
        TS(48);
        park<(WIN > WIN2 ? WIN : WIN2)>(s, F_BLK, 8);
        if (typeA) {
            const rsrc_t ro = mkrs(p_wo, H_ * D_ * M_ * 2);
            const u32 row = lane >> 2, ch = (lane & 3) ^ (((row >> 3) & 1) << 1);
            const u32 vf = row * 1280u + ch * 16;
            for (int w = 0; w < 8; ++w) {
                const u32 so = (768u * x + 96u * w) * 1280u + 64u * r, ld = L_A + BLK * w + B_W;
                for (int k = 0; k < 6; k += 3) {
                    dma<NT_O>(ld + 1024 * k, vf, ro, so + 16u * 1280u * k); dma<NT_O>(ld + 1024 * (k + 1), vf, ro, so + 16u * 1280u * (k + 1));
                    dma<NT_O>(ld + 1024 * (k + 2), vf, ro, so + 16u * 1280u * (k + 2));
                    step3<WIN2>(s);
                }
            }
        } else {
            const rsrc_t r1 = mkrs(p_w1, M_ * DFF * 2), r2 = mkrs(p_w2, DFF * M_ * 2);
            const u32 fb = x * 12 + (r - 20);
            const u32 va = (lane >> 1) * 3072u + (lane & 1) * 16, va2 = lane < 32 ? va : OOB;
            u32 vb[3];
#pragma unroll
            for (int k = 0; k < 3; ++k) { const u32 p = 64 * k + lane; vb[k] = p < 160 ? (p / 10) * 1280u + (p % 10) * 16 : OOB; }
            for (int w = 0; w < 8; ++w) {
                const u32 ld = L_A + BLK * w;
                const u32 sa = 80u * w * 3072u + fb * 32u, sb = 16u * fb * 1280u + 160u * w;
                dma<NT_W1>(ld + B_W, va, r1, sa); dma<NT_W1>(ld + B_W + 1024, va, r1, sa + 32u * 3072u); dma<NT_W1>(ld + B_W + 2048, va2, r1, sa + 64u * 3072u);
                step3<WIN2>(s);
                dma<NT_W2>(ld + B_W2, vb[0], r2, sb); dma<NT_W2>(ld + B_W2 + 1024, vb[1], r2, sb); dma<NT_W2>(ld + B_W2 + 2048, vb[2], r2, sb);
                step3<WIN2>(s);
            }
        }
        TS(49);
        drain<(WIN > WIN2 ? WIN : WIN2) - 3>(s);
        TS(50);
        return;
    }

    if (wave == 9) {
        const u32x2 ep = ld64<SC1>(rWS, W_EPOCH);
        u32x4 sv; sv[0] = SENT; sv[1] = SENT; sv[2] = SENT; sv[3] = SENT;
        {
            const u32 c0 = (u32)b * SMALL_PER + lane, c1 = c0 + 64;
            st128<SC1>(sv, rWS, c0 < SMALL_CH ? W_SMALL + c0 * 16 : OOB);
            st128<SC1>(sv, rWS, (lane + 64 < (int)SMALL_PER && c1 < SMALL_CH) ? W_SMALL + c1 * 16 : OOB);
        }
        const int it = 255 - b;
        const rsrc_t rc = mkrs(p_c, M_ * 2), rm = mkrs(p_wmod, M_ * 6 * M_ * 2);
        u32 cs[5];
        if (it < 160) {
#pragma unroll
            for (int k = 0; k < 5; ++k) cs[k] = ld16(rc, (320 * (it & 1) + 64 * k + lane) * 2);
        }
        TCAP(tb9);
        waitvm<0>();
        TSV(52, t_entry); TSV(55, tb9);
        const u64 tag = (((u64)ep[1] << 32) | ep[0]) + 1ull;
        u32x2 tg; tg[0] = (u32)tag; tg[1] = (u32)(tag >> 32);
        if (lane == 0) st64<SC1>(tg, rWS, W_FA + 8 * b);
        TS(39);
        wait_ge<true>(F_HX, 5, dead);
        {
            u32 spins = 0;
            for (;;) {
                CBAR();
                const u32x4 a = ld128<SC1>(rWS, W_FA + lane * 16), c = ld128<SC1>(rWS, W_FA + 1024 + lane * 16);
                const bool ok = a[0] == tg[0] && a[1] == tg[1] && a[2] == tg[0] && a[3] == tg[1] && c[0] == tg[0] && c[1] == tg[1] && c[2] == tg[0] && c[3] == tg[1];
                if (all_ok(ok)) break;
                POLL_STEP();
            }
            flag_st(F_RDYA, 1, lane);
            TS(40);
        }
        if (it < 160) {
            const int cb = it >> 1, kh = it & 1;
#pragma unroll
            for (int k = 0; k < 5; ++k) {
                const float cv = __uint_as_float(cs[k] << 16);
                lds_st<float>(L_SIL + (64 * k + lane) * 4, cv * __builtin_amdgcn_rcpf(1.f + __expf(-cv)));
            }
            wait_ge<true>(F_QAOK, 8, dead);
            TS(42);
            u32x4 wv[20];
            const u32 vo = (u32)(320 * kh + (lane >> 2)) * 7680u + (1280u + 32u * cb) * 2u + (lane & 3) * 16u;
#pragma unroll
            for (int k = 0; k < 20; ++k) wv[k] = ld128<NT_MR>(rm, vo + (u32)k * 16u * 7680u);
            float acc[8];
#pragma unroll
            for (int j = 0; j < 8; ++j) acc[j] = 0.f;
#pragma unroll
            for (int k = 0; k < 20; ++k) {
                const float sl = lds_ld<float>(L_SIL + (16 * k + (lane >> 2)) * 4);
#pragma unroll
                for (int j = 0; j < 4; ++j) { acc[2 * j] += sl * f_lo(wv[k][j]); acc[2 * j + 1] += sl * f_hi(wv[k][j]); }
            }
            float t4[4], t2[2];
#pragma unroll
            for (int j = 0; j < 4; ++j) t4[j] = sc32(acc[j], acc[j + 4]);
#pragma unroll
            for (int j = 0; j < 2; ++j) t2[j] = sc16(t4[j], t4[j + 2]);
#pragma unroll
            for (int j = 0; j < 2; ++j) { t2[j] += dppf<ROR(8)>(t2[j]); t2[j] += dppf<ROR(4)>(t2[j]); }
            const u32 pm = cb / 20;
#pragma unroll
            for (int j = 0; j < 2; ++j) {
                const u32 col = (cb % 20) * 32u + 8 * (lane & 3) + 4 * (lane >> 5) + 2 * ((lane >> 4) & 1) + j;
                const u32 cw = col / 80, cc = col % 80;
                const u32 pos = cc < 64 ? (cc & 7) * 8 + (cc >> 3) : 64 + (cc & 7) * 2 + ((cc >> 3) & 1);
                st32<SC1>(san(__float_as_uint(t2[j])), rWS, (lane & 12) == 0 ? W_MR + ((kh * 8u + cw) * 80u + pos) * 16u + pm * 4u : OOB);
            }
            TS(43);
        }
        return;
    }

    const int w = wave;
    const rsrc_t rX = mkrs(p_x, M_ * 2);
    const u32 ks = r >> 3;
    const bool front = w < 5;
    const bool doS0 = typeA && front;
    const u32 cg = (x & 1) * 20 + r, kp = x >> 1;
    u32x4 m0v, m1v, xa; u32 cv = 0, xb = 0; u32x2 xo;
    if (doS0) {
        const rsrc_t rc = mkrs(p_c, M_ * 2), rm = mkrs(p_wmod, M_ * 6 * M_ * 2);
        const u32 row0 = 160 * kp + 32 * w + 2 * (lane >> 2);
        const u32 off = row0 * 7680u + cg * 64u + (lane & 3) * 16u;
        m0v = ld128<NT_S0>(rm, off); m1v = ld128<NT_S0>(rm, off + 7680u);
        cv = ld32<0>(rc, row0 * 2);
    }
    const u32x2 ep = ld64<SC1>(rWS, W_EPOCH);
    if (front) {
        xa = ld128<0>(rX, lane * 16); xb = ld32<0>(rX, 1024 + lane * 4);
        xo = ld64<0>(rX, (160 * ks + 32 * w + 4 * (lane & 7)) * 2);
    }
    if (w == 0) TSV(53, tbw);
    if (w == 5) TSV(56, tbw);
    if (w == 7) { TSV(57, tbw); TSV(58, t_entry); }
    if (w == 0) TS(0);
    const u64 tag = (((u64)(u32)__builtin_amdgcn_readfirstlane((int)ep[1]) << 32) | (u32)__builtin_amdgcn_readfirstlane((int)ep[0])) + 1ull;
    const u32 T1 = (u32)tag, T2 = (u32)(tag >> 32);
    const u32 tagl = T1 & 0xFFu, tg12 = T1 & 0xFFFu;

    if (doS0) {
        const float cA = f_lo(cv), cB = f_hi(cv);
        const u32 sp = pk2(cA * __builtin_amdgcn_rcpf(1.f + __expf(-cA)), cB * __builtin_amdgcn_rcpf(1.f + __expf(-cB)));
        float acc[8];
#pragma unroll
        for (int j = 0; j < 4; ++j) {
            acc[2 * j] = dot2(__builtin_amdgcn_perm(m1v[j], m0v[j], 0x05040100u), sp, 0.f);
            acc[2 * j + 1] = dot2(__builtin_amdgcn_perm(m1v[j], m0v[j], 0x07060302u), sp, 0.f);
        }
        if (w == 0) TS(31);
        float t4[4]; f32x2 t2;
#pragma unroll
        for (int j = 0; j < 4; ++j) t4[j] = sc32(acc[j], acc[j + 4]);
#pragma unroll
        for (int j = 0; j < 2; ++j) { float t = sc16(t4[j], t4[j + 2]); t += dppf<ROR(8)>(t); t += dppf<ROR(4)>(t); t2[j] = t; }
        if ((lane & 12) == 0) lds_st<f32x2>(L_S0P + (w * 32 + 8 * (lane & 3) + 4 * (lane >> 5) + 2 * ((lane >> 4) & 1)) * 4, t2);
        if (flag_add_ret(F_S0CNT, lane) == 4) {
            f32x4 v = lds_ld<f32x4>(L_S0P + (lane & 7) * 16);
#pragma unroll
            for (int p = 1; p < 5; ++p) v += lds_ld<f32x4>(L_S0P + p * 128 + (lane & 7) * 16);
            u32x4 o;
#pragma unroll
            for (int j = 0; j < 4; ++j) o[j] = tagw(v[j], tg12);
            const u32 wh = cg >= 20 ? 1u : 0u, m0 = 32 * (cg - 20 * wh);
            st128<SC1>(o, rWS, lane < 8 ? W_G1 + ((wh * 4 + kp) * 640u + m0 + 4 * lane) * 4u : OOB);
            TS(1);
        }
    }

    const u32 h1 = 4 * x + ((r >> 1) & 3), half = r & 1;
    u32x4 bw[5];
    auto read_bw = [&]() {
        wait_ge<true>(F_LAND, T_S1, dead);
        const u32 ab = L_S1W + (4 * g + q4) * 192 + (((2 * w + (p4 >> 1)) ^ ((g & 1) << 1)) * 16) + 8 * (p4 & 1);
#pragma unroll
        for (int t = 0; t < 5; ++t) bw[t] = cat(trd(ab + (32 * t) * 192), trd(ab + (32 * t + 16) * 192));
        asm volatile("s_waitcnt lgkmcnt(0)" ::: "memory");
        flag_add(F_S1DONE, lane);
        if (w == 0) TS(32);
    };
    if (w == 5) read_bw();
    if (front) {
        float xv[10];
#pragma unroll
        for (int k = 0; k < 4; ++k) { xv[2 * k] = f_lo(xa[k]); xv[2 * k + 1] = f_hi(xa[k]); }
        xv[8] = f_lo(xb); xv[9] = f_hi(xb);
        float sum = 0.f;
#pragma unroll
        for (int k = 0; k < 10; ++k) sum += xv[k];
        const float mu = wave_sum(sum) * (1.f / 640.f);
        float sq = 0.f;
#pragma unroll
        for (int k = 0; k < 10; ++k) { const float a = xv[k] - mu; sq += a * a; }
        const float rstd = 1.f / sqrtf(wave_sum(sq) * (1.f / 640.f) + 1e-5f);
        float aj[4], bj[4];
#pragma unroll
        for (int j = 0; j < 4; ++j) {
            const float xn = (((j & 1) ? f_hi(xo[j >> 1]) : f_lo(xo[j >> 1])) - mu) * rstd;
            aj[j] = lane >= 32 ? xn : 1.f; bj[j] = lane < 8 ? xn : 0.f;
        }
        const u32 goff = W_G1 + ((u32)(lane >> 3) * 640u + 160 * ks + 32 * w + 4 * (lane & 7)) * 4u;
        const u32 hadr = L_HX + ((lane >> 3) & 1) * 320 + (32 * w + 8 * (lane & 3) + 4 * ((lane >> 2) & 1) + 2 * (lane >> 5) + ((lane >> 4) & 1)) * 2;
        read_bw();
        PSet<1> gv, pa, pb, pc;
        ppoll<1, STG>(gv, pa, pb, pc,
            [&](PSet<1>& s) { s.v[0] = ld128<SC1>(rWS, goff); },
            [&](const PSet<1>& s) { return all_ok(tag_ok(s.v[0], tg12)); }, dead);
        if (w == 4) TS(2);
        float t[4];
#pragma unroll
        for (int j = 0; j < 4; ++j) t[j] = aj[j] * untag(gv.v[0][j]) + bj[j];
        float hv = sc16(sc32(t[0], t[2]), sc32(t[1], t[3]));
        hv += dppf<ROR(8)>(hv);
        const u32 hi = bf1(hv), lo = bf1(hv - __uint_as_float(hi << 16));
        lds_st<unsigned short>(hadr, (unsigned short)((lane & 8) ? lo : hi));
        flag_add(F_HX, lane);
        retire(pa, pb, pc);
    }
    u32x4 bw2[6];
    if (w < 6) {
        wait_ge<false>(F_HX, 5, dead);
        f32x4 acc = {0.f, 0.f, 0.f, 0.f};
#pragma unroll
        for (int t = 0; t < 5; ++t) acc = mfma32(lds_ld<u32x4>(L_HX + (lane & 1) * 320 + 64 * t + 16 * g), bw[t], acc);
        const u32 qb = tagw(acc[0] + acc[1], tg12);
        u32x4 o; o[0] = qb; o[1] = dppu<SHL(1)>(qb); o[2] = dppu<SHL(2)>(qb); o[3] = dppu<SHL(3)>(qb);
        st128<SC1>(o, rWS, (lane & 51) == 0 ? W_Q2 + (ks * 6144u + h1 * 192 + 96 * half + 16 * w + lane) * 4u : OOB);
        if (w == 0) TS(3);
        wait_ge<true>(F_LAND, T_S2, dead);
        const u32 rowb = L_X + (16 * w + i16) * 384, sw = ((i16 >> 1) & 3) << 1;
#pragma unroll
        for (int t = 0; t < 6; ++t) bw2[t] = lds_ld<u32x4>(rowb + (((4 * t + g) ^ sw) * 16));
        flag_add(F_S2DONE, lane);
        if (w == 0) TS(33);
    }
    if (w >= 5) {
        if (w != 5) wait_ge<true>(F_HX, 5, dead);
        const u32 qo = W_Q2 + ((u32)g * 6144u + (u32)r * 192u + 64 * (w - 5) + 4 * i16) * 4u;
        const u32 qadr = L_QH + (64 * (w - 5) + 4 * i16 + 2 * (lane >> 5) + ((lane >> 4) & 1)) * 2;
        PSet<1> qv1, pa, pb, pc;
        ppoll<1, STG>(qv1, pa, pb, pc,
            [&](PSet<1>& s) { s.v[0] = ld128<SC1>(rWS, qo); },
            [&](const PSet<1>& s) { return all_ok(tag_ok(s.v[0], tg12)); }, dead);
        if (w == 7) TS(4);
        const float q = sc16(sc32(untag(qv1.v[0][0]), untag(qv1.v[0][2])), sc32(untag(qv1.v[0][1]), untag(qv1.v[0][3])));
        const u32 hi = bf1(q), lo = bf1(q - __uint_as_float(hi << 16));
        lds_st<unsigned short>(qadr, (unsigned short)hi); lds_st<unsigned short>(qadr + 384, (unsigned short)lo);
        flag_add(F_QH, lane);
        retire(pa, pb, pc);
    }
    if (w < 6) {
        wait_ge<false>(F_QH, 3, dead);
        f32x4 a0 = {0.f, 0.f, 0.f, 0.f}, a1 = {0.f, 0.f, 0.f, 0.f};
#pragma unroll
        for (int t = 0; t < 6; t += 2) {
            a0 = mfma32(lds_ld<u32x4>(L_QH + (lane & 1) * 384 + 64 * t + 16 * g), bw2[t], a0);
            a1 = mfma32(lds_ld<u32x4>(L_QH + (lane & 1) * 384 + 64 * (t + 1) + 16 * g), bw2[t + 1], a1);
        }
        const float qa = ((a0[0] + a0[1]) + (a1[0] + a1[1])) * (0.07216878364870322f * 1.4426950408889634f);
        const u32x4 o = pack_row(bf1(qa));
        const u32 ch = 2 * w + ((lane >> 3) & 1);
        const u32 dst = W_QA + ((((r >> 4) * 8 + x) * 3 + (ch >> 2)) * 64u + (ch & 3) * 16 + (r & 15)) * 16u;
        wait_ge<false>(F_RDYA, 1, dead);
        st128<SC1>(o, rWS, (lane & 55) == 0 ? dst : OOB);
        if (w == 0) TS(5);
    }

    const u32 g2 = r >> 4;
    const u32 blk = L_A + BLK * w;
    u32x4 vt[6][2];
    {
        u32x4 bq[3];
        const u32 qoff = W_QA + (g2 * 8 + w) * 3072u + lane * 16u;
        wait_ge<false>(F_RDYA, 1, dead);
#pragma unroll
        for (int k = 0; k < 3; ++k) bq[k] = ld128<SC1>(rWS, qoff + k * 1024);
        CBAR();
        wait_ge<true>(F_LAND, T_TILE0 + 12 * (w + 1), dead);
        if (w == 0) TS(6);
        if (w == 7) TS(27);
        u32x4 av[4][3];
        const u32 ar = blk + i16 * 192, asw = ((i16 >> 2) & 1) << 1;
#pragma unroll
        for (int kb = 0; kb < 4; ++kb)
#pragma unroll
            for (int t = 0; t < 3; ++t) av[kb][t] = lds_ld<u32x4>(ar + kb * 16 * 192 + ((4 * t + g) ^ asw) * 16);
        const u32 vb = blk + (4 * g + q4) * 192 + (p4 >> 1) * 16 + 8 * (p4 & 1);
#pragma unroll
        for (int u = 0; u < 6; ++u) {
            const u32 cu = vb + (u ^ (g & 1)) * 32;
#pragma unroll
            for (int t = 0; t < 2; ++t) vt[u][t] = cat(trd(cu + (32 * t) * 192), trd(cu + (32 * t + 16) * 192));
        }
        asm volatile("s_waitcnt lgkmcnt(0)" ::: "memory");
        flag_add(F_BLK, lane);
        if (w == 7) TS(28);
        {
            u32 spins = 0;
            for (;;) {
                u32 mx = 0;
#pragma unroll
                for (int k = 0; k < 3; ++k) mx = mx4(mx, bq[k]);
                if (none_sent(mx)) break;
                POLL_STEP();
                CBAR();
#pragma unroll
                for (int k = 0; k < 3; ++k) bq[k] = ld128<SC1>(rWS, qoff + k * 1024);
            }
        }
        flag_add(F_QAOK, lane);
        if (w == 0) TS(7);
        if (w == 7) TS(26);
#pragma unroll
        for (int kb = 0; kb < 4; ++kb) {
            f32x4 acc = {0.f, 0.f, 0.f, 0.f};
#pragma unroll
            for (int t = 0; t < 3; ++t) acc = mfma32(av[kb][t], bq[t], acc);
            f32x2 lo2, hi2; lo2[0] = acc[0]; lo2[1] = acc[1]; hi2[0] = acc[2]; hi2[1] = acc[3];
            lds_st<f32x2>(blk + (kb * 2) * 512 + lane * 8, lo2);
            lds_st<f32x2>(blk + (kb * 2 + 1) * 512 + lane * 8, hi2);
        }
        flag_add(F_SADD, lane);
        if (w == 0) TS(20);
    }
    {
        wait_ge<false>(F_SADD, 8, dead);
        if (w == 0) TS(21);
        f32x2 s = {0.f, 0.f};
#pragma unroll
        for (int p = 0; p < 8; ++p) s += lds_ld<f32x2>(L_A + BLK * p + w * 512 + lane * 8);
        lds_st<f32x2>(L_SC + (w >> 1) * 1024 + lane * 16 + (w & 1) * 8, s);
        flag_add(F_SC, lane);
        wait_ge<false>(F_SC, 8, dead);
        if (w == 0) TS(22);
        f32x4 sc[4];
#pragma unroll
        for (int q = 0; q < 4; ++q) sc[q] = lds_ld<f32x4>(L_SC + q * 1024 + lane * 16);
        float mloc = fmaxf(fmaxf(fmaxf(sc[0][0], sc[0][1]), fmaxf(sc[0][2], sc[0][3])), fmaxf(fmaxf(sc[1][0], sc[1][1]), fmaxf(sc[1][2], sc[1][3])));
        mloc = fmaxf(mloc, fmaxf(fmaxf(fmaxf(sc[2][0], sc[2][1]), fmaxf(sc[2][2], sc[2][3])), fmaxf(fmaxf(sc[3][0], sc[3][1]), fmaxf(sc[3][2], sc[3][3]))));
        mloc = xg_max(mloc);
        u32x4 pf[2];
#pragma unroll
        for (int t = 0; t < 2; ++t)
#pragma unroll
            for (int e = 0; e < 4; ++e) {
                const f32x4 sq = sc[2 * t + (e >> 1)];
                pf[t][e] = pk2(__builtin_amdgcn_exp2f(sq[2 * (e & 1)] - mloc), __builtin_amdgcn_exp2f(sq[2 * (e & 1) + 1] - mloc));
            }
        f32x4 oa[6], lz;
#pragma unroll
        for (int u = 0; u < 6; ++u) { f32x4 z = {0.f, 0.f, 0.f, 0.f}; oa[u] = mfma32(pf[0], vt[u][0], z); oa[u] = mfma32(pf[1], vt[u][1], oa[u]); }
        {
            u32x4 one; one[0] = 0x3F803F80u; one[1] = 0x3F803F80u; one[2] = 0x3F803F80u; one[3] = 0x3F803F80u;
            f32x4 z = {0.f, 0.f, 0.f, 0.f};
            lz = mfma32(pf[0], one, z); lz = mfma32(pf[1], one, lz);
        }
        if (w == 0) TS(23);
        const u32 jj = x * 16 + (r & 15);
        const u32 orow = W_O + (((16 * g2 + 4 * g) * 8 + w) * 128 + jj) * OROW;
        u32 cx[4], cs[4];
#pragma unroll
        for (int e = 0; e < 4; ++e) {
            u32x3 d; d[0] = pk2(oa[0][e], oa[1][e]); d[1] = pk2(oa[2][e], oa[3][e]); d[2] = pk2(oa[4][e], oa[5][e]);
            st96<SC1>(d, rWS, orow + e * (8 * 128 * OROW) + i16 * 12);
            cx[e] = row_xor(d[0] ^ d[1] ^ d[2]); cs[e] = row_add(d[0] + d[1] + d[2]);
        }
        {
            const int e = i16 & 3;
            u32x4 pk;
            pk[0] = __float_as_uint(mloc);
            pk[1] = (sel4(e, __float_as_uint(lz[0]), __float_as_uint(lz[1]), __float_as_uint(lz[2]), __float_as_uint(lz[3])) & ~0xFFu) | tagl;
            pk[2] = sel4(e, cx[0], cx[1], cx[2], cx[3]) ^ pk[0] ^ pk[1] ^ T1;
            pk[3] = sel4(e, cs[0], cs[1], cs[2], cs[3]) + pk[0] + pk[1] + T2;
            st128<SC1>(pk, rWS, q4 == g ? orow + e * (8 * 128 * OROW) + 192 : OOB);
        }
        if (w == 0) TS(8);
    }

    {
        const u32 obase = W_O + ((r * 8 + x) * 128 + 16 * w) * OROW;
        const u32 poff = i16 < 4 ? obase + (4 * i16 + g) * OROW + 192 : OOB;
        PSet<1> pk, pa, pb, pc;
        ppoll<1, STG>(pk, pa, pb, pc,
            [&](PSet<1>& s) { s.v[0] = ld128<SC1>(rWS, poff); },
            [&](const PSet<1>& s) { return all_ok(i16 >= 4 || (s.v[0][1] & 0xFFu) == tagl); }, dead);
        if (w == 0) TS(35);
        u32x3 od[4];
        u32x4 bw[2][3];
        {
            u32 spins = 0;
            bool first = true;
            for (;;) {
                CBAR();
#pragma unroll
                for (int k = 0; k < 4; ++k) od[k] = ld96<SC1>(rWS, obase + (4 * k + g) * OROW + i16 * 12);
                if (!first) pk.v[0] = ld128<SC1>(rWS, poff);
                if (first && w < 6) {
                    wait_ge<true>(F_LAND, T_UV, dead);
                    const u32 sw = 2 * (g & 1) + (q4 >> 1);
#pragma unroll
                    for (int d = 0; d < 2; ++d) {
                        const u32 ub = L_X + (4 * g + q4) * 384 + (2 * ((2 * w + d) ^ sw) + (p4 >> 1)) * 16 + 8 * (p4 & 1);
#pragma unroll
                        for (int t = 0; t < 3; ++t) bw[d][t] = cat(trd(ub + (32 * t) * 384), trd(ub + (32 * t + 16) * 384));
                    }
                }
                first = false;
                u32 cx[4], cs[4];
#pragma unroll
                for (int k = 0; k < 4; ++k) { cx[k] = row_xor(od[k][0] ^ od[k][1] ^ od[k][2]); cs[k] = row_add(od[k][0] + od[k][1] + od[k][2]); }
                const u32x4 p = pk.v[0];
                const bool ok = i16 >= 4 || ((sel4(i16, cx[0], cx[1], cx[2], cx[3]) ^ p[0] ^ p[1] ^ T1) == p[2] &&
                                             (sel4(i16, cs[0], cs[1], cs[2], cs[3]) + p[0] + p[1] + T2) == p[3]);
                if (all_ok(ok)) break;
                POLL_STEP();
            }
        }
        if (w == 0) TS(9);
        const float mrow = i16 < 4 ? __uint_as_float(pk.v[0][0]) : -INFINITY;
        float Mw = fmaxf(mrow, dppf<QX1>(mrow)); Mw = fmaxf(Mw, dppf<QX2>(Mw)); Mw = xg_max(dppf<NBC(0)>(Mw));
        const float wt = __builtin_amdgcn_exp2f(mrow - Mw);
        float lw = i16 < 4 ? wt * __uint_as_float(pk.v[0][1] & ~0xFFu) : 0.f;
        lw += dppf<QX1>(lw); lw += dppf<QX2>(lw); lw = xg_sum(lw);
        float acc[6];
#pragma unroll
        for (int u = 0; u < 6; ++u) acc[u] = 0.f;
        {
            const float w0 = dppf<NBC(0)>(wt), w1 = dppf<NBC(1)>(wt), w2 = dppf<NBC(2)>(wt), w3 = dppf<NBC(3)>(wt);
#pragma unroll
            for (int j = 0; j < 3; ++j) {
                acc[2 * j] = (w0 * f_lo(od[0][j]) + w1 * f_lo(od[1][j])) + (w2 * f_lo(od[2][j]) + w3 * f_lo(od[3][j]));
                acc[2 * j + 1] = (w0 * f_hi(od[0][j]) + w1 * f_hi(od[1][j])) + (w2 * f_hi(od[2][j]) + w3 * f_hi(od[3][j]));
            }
        }
        const float y0 = sc16(sc32(acc[0], acc[3]), sc32(acc[1], acc[4]));
        const float t2 = sc32(acc[2], acc[5]);
        const float y1 = sc16(t2, t2);
        {
            const u32 u0 = 3 * (g >> 1) + (g & 1), u1 = 3 * (g >> 1) + 2;
            const u32 sw0 = ((w >> 1) & 1) << 1;
            const u32 a0 = L_AM + 2 * w * 192 + (((4 * (u0 >> 1) + q4) ^ sw0) * 16) + (4 * (u0 & 1) + p4) * 2;
            const u32 a1 = L_AM + (2 * w + (g & 1)) * 192 + (((4 * (u1 >> 1) + q4) ^ sw0) * 16) + (4 * (u1 & 1) + p4) * 2;
            const u32 h0 = bf1(y0), l0 = bf1(y0 - __uint_as_float(h0 << 16));
            const u32 h1b = bf1(y1), l1 = bf1(y1 - __uint_as_float(h1b << 16));
            lds_st<unsigned short>(a0, (unsigned short)h0); lds_st<unsigned short>(a0 + 192, (unsigned short)l0);
            lds_st<unsigned short>(a1, (unsigned short)((g & 1) ? l1 : h1b));
            f32x2 ml; ml[0] = Mw; ml[1] = lw;
            if (lane == 0) lds_st<f32x2>(L_EML + w * 8, ml);
        }
        flag_add(F_AM, lane);
        if (w < 6) {
            wait_ge<false>(F_AM, 8, dead);
            u32x4 aa[3];
#pragma unroll
            for (int t = 0; t < 3; ++t) aa[t] = lds_ld<u32x4>(L_AM + i16 * 192 + (((4 * t + g) ^ (((i16 >> 2) & 1) << 1)) * 16));
            const f32x4 em = lds_ld<f32x4>(L_EML + g * 16);
            const float Mx = xg_max(fmaxf(em[0], em[2]));
            const float fa = __builtin_amdgcn_exp2f(em[0] - Mx), fb = __builtin_amdgcn_exp2f(em[2] - Mx);
            const float rl = 1.f / xg_sum(fa * em[1] + fb * em[3]);
            float res[2];
#pragma unroll
            for (int d = 0; d < 2; ++d) {
                f32x4 z = {0.f, 0.f, 0.f, 0.f};
#pragma unroll
                for (int t = 0; t < 3; ++t) z = mfma32(aa[t], bw[d][t], z);
                res[d] = xg_sum(fa * (z[0] + z[1]) + fb * (z[2] + z[3])) * rl;
            }
            const u32x4 o = pack_row(bf1(g == 0 ? res[0] : res[1]));
            const u32 ch = 4 * w + 2 * (g & 1) + ((lane >> 3) & 1);
            const u32 wc = 2 * (r & 3) + (ch >= 12 ? 1 : 0), c12 = ch >= 12 ? ch - 12 : ch;
            const u32 dst = W_CTX + (((((r >> 2) * 8 + wc) * 3 + (c12 >> 2)) * 4 + (c12 & 3)) * 8 + x) * 16u;
            st128<SC1>(o, rWS, (g < 2 && (lane & 7) == 0) ? dst : OOB);
            if (w == 0) TS(10);
        }
        retire(pa, pb, pc);
    }

    if (typeA) {
        const u32 coff = i16 < 8 ? W_CTX + ((x * 8 + w) * 96u + g * 8 + i16) * 16u : OOB;
        wait_ge<true>(F_LAND, T_UV + 6 * (w + 1), dead);
        u32x4 bw[2][3];
        const u32 fbx = blk + B_W + (8 * g + q4) * 64 + (p4 >> 1) * 16 + 8 * (p4 & 1);
#pragma unroll
        for (int u = 0; u < 2; ++u) {
            const u32 ub = fbx + (u ^ (g & 1)) * 32;
#pragma unroll
            for (int t = 0; t < 3; ++t) bw[u][t] = cat(trd(ub + (32 * t) * 64), trd(ub + (32 * t + 4) * 64));
        }
        if (w == 0) TS(36);
        PSet<3> ca, pa, pb, pc;
        ppoll<3, STG2>(ca, pa, pb, pc,
            [&](PSet<3>& s) { s.v[0] = ld128<SC1>(rWS, coff); s.v[1] = ld128<SC1>(rWS, coff + 512); s.v[2] = ld128<SC1>(rWS, coff + 1024); },
            [&](const PSet<3>& s) { return none_sent(mx4(mx4(mx4(0, s.v[0]), s.v[1]), s.v[2])); }, dead);
        if (w == 0) TS(11);
        float res[2];
#pragma unroll
        for (int u = 0; u < 2; ++u) {
            f32x4 z = {0.f, 0.f, 0.f, 0.f};
#pragma unroll
            for (int t = 0; t < 3; ++t) z = mfma32(ca.v[t], bw[u][t], z);
            res[u] = xg_sum((z[0] + z[1]) + (z[2] + z[3]));
        }
        if (g < 2) lds_st<float>(L_FRP + (w * 32 + 16 * g + i16) * 4, g == 0 ? res[0] : res[1]);
        if (flag_add_ret(F_FRC, lane) == 7) {
            float v = 0.f;
#pragma unroll
            for (int p = 0; p < 8; ++p) v += lds_ld<float>(L_FRP + (p * 32 + (lane & 31)) * 4);
            const u32x4 o = pack_row(bf1(v));
            const u32 dst = W_AT + (x * 640u + 32u * r + 16 * (g & 1) + (lane & 8)) * 2u;
            st128<SC1>(o, rWS, (g < 2 && (lane & 7) == 0) ? dst : OOB);
            TS(12);
        }
        retire(pa, pb, pc);
    } else {
        const u32 fb = x * 12 + (r - 20);
        const u32 cA = 8 * (lane & 7) + (lane >> 3);
        const bool hasB = (lane & 7) < 2;
        const u32 colA = 80 * w + cA, colB = colA + 64;
        float ga[2], sb[2], cb1[2], gb[2], xv[2];
        {
            xv[0] = __uint_as_float(ld16(rX, colA * 2) << 16); xv[1] = __uint_as_float(ld16(rX, hasB ? colB * 2 : OOB) << 16);
            u32x4 mr[2][2]; u32 spins = 0;
            const u32 posB = 64 + (lane >> 3) * 2 + (lane & 1);
            for (;;) {
                CBAR();
                u32 mx = 0;
#pragma unroll
                for (int kh = 0; kh < 2; ++kh) {
                    mr[kh][0] = ld128<SC1>(rWS, W_MR + ((kh * 8u + w) * 80u + lane) * 16u);
                    mr[kh][1] = ld128<SC1>(rWS, hasB ? W_MR + ((kh * 8u + w) * 80u + posB) * 16u : OOB);
                    mx = mx4(mx4(mx, mr[kh][0]), mr[kh][1]);
                }
                if (none_sent(mx)) break;
                POLL_STEP();
            }
#pragma unroll
            for (int c = 0; c < 2; ++c) {
                ga[c] = __uint_as_float(mr[0][c][0]) + __uint_as_float(mr[1][c][0]);
                sb[c] = __uint_as_float(mr[0][c][1]) + __uint_as_float(mr[1][c][1]);
                cb1[c] = 1.f + (__uint_as_float(mr[0][c][2]) + __uint_as_float(mr[1][c][2]));
                gb[c] = __uint_as_float(mr[0][c][3]) + __uint_as_float(mr[1][c][3]);
            }
        }
        if (w == 0) TS(13);
        const u32 gab = blk + B_GA;
#pragma unroll
        for (int c = 0; c < 2; ++c) {
            if (c == 0 || hasB) {
                const u32 cc = cA + 64 * c;
                const u32 h = bf1(cb1[c]), l = bf1(cb1[c] - __uint_as_float(h << 16)), h2 = bf1(sb[c]), l2 = bf1(sb[c] - __uint_as_float(h2 << 16));
                lds_st<unsigned short>(gab + 2 * 160 + cc * 2, (unsigned short)h); lds_st<unsigned short>(gab + 3 * 160 + cc * 2, (unsigned short)l);
                lds_st<unsigned short>(gab + 4 * 160 + cc * 2, (unsigned short)h2); lds_st<unsigned short>(gab + 5 * 160 + cc * 2, (unsigned short)l2);
                lds_st<float>(blk + B_GB + cc * 4, gb[c]);
            }
        }
        wait_ge<true>(F_LAND, T_UV + 6 * (w + 1), dead);
        u32x2 b1[5], b2[5];
#pragma unroll
        for (int t = 0; t < 5; ++t) b1[t] = trd(blk + B_W + 512 * t + 8 * lane);
#pragma unroll
        for (int u = 0; u < 5; ++u) b2[u] = trd(blk + B_W2 + (4 * g + q4) * 160 + 32 * u + 8 * p4);
        if (w == 0) TS(36);
        const u32 aoff = W_AT + ((lane >> 3) * 640u + 80u * w + 8 * (lane & 7)) * 2u, aoff2 = hasB ? aoff + 128 : OOB;
        PSet<2> at, pa, pb, pc;
        ppoll<2, STG2>(at, pa, pb, pc,
            [&](PSet<2>& s) { s.v[0] = ld128<SC1>(rWS, aoff); s.v[1] = ld128<SC1>(rWS, aoff2); },
            [&](const PSet<2>& s) { return none_sent(mx4(mx4(0, s.v[0]), s.v[1])); }, dead);
        if (w == 0) TS(14);
        float x1[2];
#pragma unroll
        for (int c = 0; c < 2; ++c) {
            const u32x4 a = at.v[c];
            float v[8];
#pragma unroll
            for (int j = 0; j < 4; ++j) { v[2 * j] = f_lo(a[j]); v[2 * j + 1] = f_hi(a[j]); }
            float t4[4], t2[2];
#pragma unroll
            for (int j = 0; j < 4; ++j) t4[j] = sc32(v[j], v[j + 4]);
#pragma unroll
            for (int j = 0; j < 2; ++j) t2[j] = sc16(t4[j], t4[j + 2]);
            const bool b3 = (lane & 8) != 0;
            const float keep = b3 ? t2[1] : t2[0], give = b3 ? t2[0] : t2[1];
            const float attn = keep + dppf<ROR(8)>(give);
            x1[c] = xv[c] + ga[c] * attn;
        }
        if (!hasB) x1[1] = 0.f;
#pragma unroll
        for (int c = 0; c < 2; ++c) {
            if (c == 0 || hasB) {
                const u32 cc = cA + 64 * c;
                const float uv = x1[c] * cb1[c];
                const u32 h = bf1(uv), l = bf1(uv - __uint_as_float(h << 16));
                lds_st<unsigned short>(gab + cc * 2, (unsigned short)h); lds_st<unsigned short>(gab + 160 + cc * 2, (unsigned short)l);
                lds_st<float>(blk + B_X1 + cc * 4, x1[c]);
            }
        }
        CBAR();
        f32x4 z = {0.f, 0.f, 0.f, 0.f};
        const u32 arow = (q4 < 2 ? q4 : 2) * 2 + (lane & 1);
#pragma unroll
        for (int t = 0; t < 5; ++t) z = mfma16(lds_ld<u32x2>(gab + arow * 160 + 32 * t + 8 * g), b1[t], z);
        const float s1 = wave_sum(x1[0] + x1[1]), s2 = wave_sum(x1[0] * x1[0] + x1[1] * x1[1]);
        lds_st<float>(L_GRP + w * 256 + lane * 4, g < 3 ? z[0] + z[1] : (p4 == 0 ? s1 : (p4 == 1 ? s2 : 0.f)));
        flag_add(F_GRC, lane);
        wait_ge<false>(F_GRC, 8, dead);
        f32x4 gs = lds_ld<f32x4>(L_GRP + (2 * p4) * 256 + q4 * 64 + g * 16) + lds_ld<f32x4>(L_GRP + (2 * p4 + 1) * 256 + q4 * 64 + g * 16);
#pragma unroll
        for (int e = 0; e < 4; ++e) { gs[e] += dppf<QX1>(gs[e]); gs[e] += dppf<QX2>(gs[e]); }
        const float mu = dppf<NBC(12)>(gs[0]) * (1.f / 640.f);
        const float var = fmaxf(dppf<NBC(12)>(gs[1]) * (1.f / 640.f) - mu * mu, 0.f);
        const float rstd = 1.f / sqrtf(var + 1e-5f);
        float av[4];
#pragma unroll
        for (int e = 0; e < 4; ++e) av[e] = fmaxf(rstd * (dppf<NBC(0)>(gs[e]) - mu * dppf<NBC(4)>(gs[e])) + dppf<NBC(8)>(gs[e]), 0.f);
        u32x2 af; { const u32 h0 = pk2(av[0], av[1]), h1b = pk2(av[2], av[3]);
            const u32 l0 = pk2(av[0] - f_lo(h0), av[1] - f_hi(h0)), l1 = pk2(av[2] - f_lo(h1b), av[3] - f_hi(h1b));
            af[0] = (lane & 1) ? l0 : h0; af[1] = (lane & 1) ? l1 : h1b; }
        float fo[5];
#pragma unroll
        for (int u = 0; u < 5; ++u) { f32x4 y = {0.f, 0.f, 0.f, 0.f}; y = mfma16(af, b2[u], y); fo[u] = y[0] + y[1]; }
        {
            const float v = g == 0 ? fo[0] : (g == 1 ? fo[1] : (g == 2 ? fo[2] : fo[3]));
            const u32x4 o = pack_row(bf1(v)), o2 = pack_row(bf1(fo[4]));
            st128<SC1>(o, rWS, (lane & 7) == 0 ? W_FP + (((5 * w + g) * 96u + fb) * 16 + (lane & 8)) * 2u : OOB);
            st128<SC1>(o2, rWS, (lane & 55) == 0 ? W_FP + (((5 * w + 4) * 96u + fb) * 16 + (lane & 8)) * 2u : OOB);
        }
        if (w == 0) TS(15);
        const int ci = (r - 20) * 8 + x;
        if (ci < 40 && ci / 5 == w) {
            const u32 fo0 = W_FP + (u32)ci * 3072u + lane * 16;
            PSet<3> fv, qa, qb, qc;
            ppoll<3, STG2>(fv, qa, qb, qc,
                [&](PSet<3>& s) { s.v[0] = ld128<SC1>(rWS, fo0); s.v[1] = ld128<SC1>(rWS, fo0 + 1024); s.v[2] = ld128<SC1>(rWS, fo0 + 2048); },
                [&](const PSet<3>& s) { return none_sent(mx4(mx4(mx4(0, s.v[0]), s.v[1]), s.v[2])); }, dead);
            TS(16);
            float v[8];
#pragma unroll
            for (int j = 0; j < 4; ++j) { v[2 * j] = (f_lo(fv.v[0][j]) + f_lo(fv.v[1][j])) + f_lo(fv.v[2][j]); v[2 * j + 1] = (f_hi(fv.v[0][j]) + f_hi(fv.v[1][j])) + f_hi(fv.v[2][j]); }
            float t4[4], t2[2];
#pragma unroll
            for (int j = 0; j < 4; ++j) t4[j] = sc32(v[j], v[j + 4]);
#pragma unroll
            for (int j = 0; j < 2; ++j) t2[j] = sc16(t4[j], t4[j + 2]);
            const bool b3 = (lane & 8) != 0;
            const float keep = b3 ? t2[1] : t2[0], give = b3 ? t2[0] : t2[1];
            float f = keep + dppf<ROR(8)>(give);
            f += dppf<SHL(4)>(f);
            f += dppf<SHL(2)>(f);
            const u32 cc = 16 * (ci % 5) + 8 * (lane & 1) + (lane >> 3);
            const float o = lds_ld<float>(blk + B_X1 + cc * 4) + lds_ld<float>(blk + B_GB + cc * 4) * f;
            const rsrc_t rO = mkrs(p_out, M_ * 2);
            __builtin_amdgcn_raw_buffer_store_b16((unsigned short)bf1(o), rO, (lane & 6) == 0 ? (16 * ci + 8 * (lane & 1) + (lane >> 3)) * 2 : OOB, 0, 0);
            if (ci == 0 && lane == 0) { u32x2 e; e[0] = T1; e[1] = T2; st64<SC1>(e, rWS, W_EPOCH); }
            TS(17);
        }
    }
}

extern "C" void kernel_launch(void* const* d_in, const int* in_sizes, int n_in, void* d_out, int out_size, void* d_ws, size_t ws_size, hipStream_t stream)
{
    (void)in_sizes; (void)n_in; (void)out_size; (void)ws_size;
    (void)hipFuncSetAttribute((const void*)adaln_mla_ffn_kernel, hipFuncAttributeMaxDynamicSharedMemorySize, (int)LDS_BYTES);
    hipLaunchKernelGGL(adaln_mla_ffn_kernel, dim3(256), dim3(640), LDS_BYTES, stream,
                       (const void*)d_in[0], (const void*)d_in[1], (const void*)d_in[2], (const void*)d_in[3], (const void*)d_in[4], (const void*)d_in[5],
                       (const void*)d_in[7], (const void*)d_in[8], (const void*)d_in[9], (const void*)d_in[10], d_out, d_ws);
}
```

```cpp
#include <hip/hip_runtime.h>
#include <hip/hip_bf16.h>
#include <stdint.h>

#define LAS __attribute__((address_space(3)))
#define DI __device__ __forceinline__
typedef unsigned int u32;
typedef unsigned long long u64;
typedef u32 u32x2 __attribute__((ext_vector_type(2)));
typedef u32 u32x3 __attribute__((ext_vector_type(3)));
typedef u32 u32x4 __attribute__((ext_vector_type(4)));
typedef float f32x2 __attribute__((ext_vector_type(2)));
typedef float f32x4 __attribute__((ext_vector_type(4)));
typedef short s16x4 __attribute__((ext_vector_type(4)));
typedef __bf16 bf16x2 __attribute__((ext_vector_type(2)));
typedef __bf16 bf16x8 __attribute__((ext_vector_type(8)));
typedef __amdgpu_buffer_rsrc_t rsrc_t;

#define WIN 18
#define WIN2 18
#define PAUSE 0
#define STG 6
#define STG2 3
#define NT_S0 0
#define NT_MR 2
#define NT_Q 1
#define NT_UK 1
#define NT_KV 1
#define NT_UV 1
#define NT_O 1
#define NT_W1 1
#define NT_W2 1
#define LPRIO 3
#define CPF 1
#define FEND "adaln_code_end"
#define KNAME "adaln_mla_ffn_kernel"
#define QDLY 40
#define ODLY 0
#define ELIGHT 1
#define MRAT 1
#define O_BASE 1048576u

constexpr int M_ = 640, H_ = 32, D_ = 192, DC = 768, SK = 8192, DFF = 1536;
constexpr u32 SENT = 0xFFFFFFFFu;
constexpr u32 OOB = 0x80000000u;

constexpr u32 W_EPOCH = 0;
constexpr u32 W_FA = 256;
constexpr u32 W_G1 = 4096;
constexpr u32 W_Q2 = 32768;
constexpr u32 W_SMALL = W_Q2 + 98304;
constexpr u32 W_QA = W_SMALL;
constexpr u32 W_CTX = W_QA + 98304;
constexpr u32 W_AT = W_CTX + 98304;
constexpr u32 W_FP = W_AT + 10240;
constexpr u32 W_MR = W_FP + 122880;
constexpr u32 W_SMALL_END = W_MR + 20480;
constexpr u32 SMALL_CH = (W_SMALL_END - W_SMALL) / 16;
constexpr u32 SMALL_PER = (SMALL_CH + 255) / 256;
constexpr u32 W_O = O_BASE;
constexpr u32 W_PK = W_O + 8u * 128 * 2 * 3072;
constexpr u32 O_BYTES = 8u * 128 * 2 * 3072 + 8u * 128 * 32 * 16;
constexpr u32 W_PROF = W_SMALL_END;
constexpr u32 W_END = W_O + O_BYTES;
static_assert(W_PROF + 256 * 64 * 8 <= W_O, "ws map");
static_assert(SMALL_PER <= 128, "prefill");
static_assert(W_END <= 268435456u, "ws map");

constexpr u32 L_A = 0;
constexpr u32 BLK = 12288;
constexpr u32 L_S1W = 5 * BLK;
constexpr u32 L_X = 98304;
constexpr u32 L_Y = 135168;
constexpr u32 L_FLG = L_Y;
constexpr u32 L_S0P = L_Y + 13568;
constexpr u32 L_LAND = L_Y + 14336;
constexpr u32 L_EML = L_Y + 448;
constexpr u32 L_FRP = L_Y + 512;
constexpr u32 L_GRP = L_Y + 1536;
constexpr u32 L_SC = L_Y + 3584;
constexpr u32 L_HX = L_Y + 7680;
constexpr u32 L_QH = L_Y + 8448;
constexpr u32 L_AM = L_Y + 9216;
constexpr u32 L_SIL = L_Y + 12288;
constexpr u32 LDS_BYTES = 163840;
constexpr u32 B_W = 4096, B_W2 = 7168, B_GA = 10240, B_X1 = 11200, B_GB = 11520;

enum { F_LAND = 0, F_RDYA, F_S0CNT, F_HX, F_S1DONE, F_QH, F_S2DONE, F_BLK, F_QAOK, F_SADD, F_SC, F_AM, F_FRC, F_GRC, F_QAST };

constexpr u32 CAP_G = 20000;
constexpr u32 CAP_L = 400000;

constexpr u32 T_S1 = 30, T_S2 = 66, T_TILE0 = 66, T_UV = 198;

DI rsrc_t mkrs(const void* p, u32 bytes) { return __builtin_amdgcn_make_buffer_rsrc((void*)p, (short)0, (int)bytes, 0x00020000); }
template <int AUX> DI u32x4 ld128(rsrc_t r, u32 off) { return __builtin_amdgcn_raw_buffer_load_b128(r, off, 0, AUX); }
template <int AUX> DI u32x3 ld96(rsrc_t r, u32 off) { return __builtin_amdgcn_raw_buffer_load_b96(r, off, 0, AUX); }
template <int AUX> DI u32x2 ld64(rsrc_t r, u32 off) { return __builtin_amdgcn_raw_buffer_load_b64(r, off, 0, AUX); }
template <int AUX> DI u32 ld32(rsrc_t r, u32 off) { return __builtin_amdgcn_raw_buffer_load_b32(r, off, 0, AUX); }
DI u32 ld16(rsrc_t r, u32 off) { return (u32)__builtin_amdgcn_raw_buffer_load_b16(r, off, 0, 0); }
template <int AUX> DI void st128(u32x4 v, rsrc_t r, u32 off) { __builtin_amdgcn_raw_buffer_store_b128(v, r, off, 0, AUX); }
template <int AUX> DI void st96(u32x3 v, rsrc_t r, u32 off) { __builtin_amdgcn_raw_buffer_store_b96(v, r, off, 0, AUX); }
template <int AUX> DI void st64(u32x2 v, rsrc_t r, u32 off) { __builtin_amdgcn_raw_buffer_store_b64(v, r, off, 0, AUX); }
template <int AUX> DI void st32(u32 v, rsrc_t r, u32 off) { __builtin_amdgcn_raw_buffer_store_b32(v, r, off, 0, AUX); }
constexpr int SC1 = 16, NT = 2;

DI float f_lo(u32 v) { return __uint_as_float(v << 16); }
DI float f_hi(u32 v) { return __uint_as_float(v & 0xFFFF0000u); }
DI u32 pk2(float a, float b) { bf16x2 v; v[0] = (__bf16)a; v[1] = (__bf16)b; return __builtin_bit_cast(u32, v); }
DI u32 bf1(float a) { return pk2(a, 0.f) & 0xFFFFu; }
DI u32 san(u32 v) { return v < 0xFFFFFFFEu ? v : 0xFFFFFFFEu; }
DI u32x4 san4(u32x4 v) { u32x4 r; r[0] = san(v[0]); r[1] = san(v[1]); r[2] = san(v[2]); r[3] = san(v[3]); return r; }
DI u32 umax(u32 a, u32 b) { return a > b ? a : b; }
DI u32 mx4(u32 acc, u32x4 v) { return umax(umax(acc, umax(v[0], v[1])), umax(v[2], v[3])); }

DI u32 tagw(float v, u32 tg) { return ((__float_as_uint(v) + 0x800u) & 0xFFFFF000u) | tg; }
DI float untag(u32 v) { return __uint_as_float(v & 0xFFFFF000u); }
DI bool tag_ok(u32x4 v, u32 tg) { return ((((v[0] ^ tg) | (v[1] ^ tg)) | ((v[2] ^ tg) | (v[3] ^ tg))) & 0xFFFu) == 0u; }
DI float dot2(u32 a, u32 b, float c) { return __builtin_amdgcn_fdot2_f32_bf16(__builtin_bit_cast(bf16x2, a), __builtin_bit_cast(bf16x2, b), c, false); }
template <int CTRL> DI u32 dppu(u32 v) { return (u32)__builtin_amdgcn_update_dpp(0, (int)v, CTRL, 0xF, 0xF, true); }
template <int CTRL> DI float dppf(float v) { return __uint_as_float(dppu<CTRL>(__float_as_uint(v))); }
constexpr int ROR(int n) { return 0x120 + n; }
constexpr int SHL(int n) { return 0x100 + n; }
constexpr int NBC(int n) { return 0x150 + n; }
constexpr int QX1 = 0xB1, QX2 = 0x4E;
DI float row_sum(float v) { v += dppf<ROR(8)>(v); v += dppf<ROR(4)>(v); v += dppf<ROR(2)>(v); v += dppf<ROR(1)>(v); return v; }
DI u32 row_xor(u32 v) { v ^= dppu<ROR(8)>(v); v ^= dppu<ROR(4)>(v); v ^= dppu<ROR(2)>(v); v ^= dppu<ROR(1)>(v); return v; }
DI u32 row_add(u32 v) { v += dppu<ROR(8)>(v); v += dppu<ROR(4)>(v); v += dppu<ROR(2)>(v); v += dppu<ROR(1)>(v); return v; }
DI void swap16(float& a, float& b) { auto r = __builtin_amdgcn_permlane16_swap(__float_as_uint(a), __float_as_uint(b), false, false); a = __uint_as_float(r[0]); b = __uint_as_float(r[1]); }
DI void swap32(float& a, float& b) { auto r = __builtin_amdgcn_permlane32_swap(__float_as_uint(a), __float_as_uint(b), false, false); a = __uint_as_float(r[0]); b = __uint_as_float(r[1]); }
DI float xg_sum(float v) { float a = v, b = v; swap16(a, b); v = a + b; a = v; b = v; swap32(a, b); return a + b; }
DI float xg_max(float v) { float a = v, b = v; swap16(a, b); v = fmaxf(a, b); a = v; b = v; swap32(a, b); return fmaxf(a, b); }
DI u32 xg_xor(u32 v) {
    auto r = __builtin_amdgcn_permlane16_swap(v, v, false, false); v = r[0] ^ r[1];
    auto q = __builtin_amdgcn_permlane32_swap(v, v, false, false); return q[0] ^ q[1];
}
DI float wave_sum(float v) { return xg_sum(row_sum(v)); }
DI float sc32(float a, float b) { swap32(a, b); return a + b; }
DI float sc16(float a, float b) { swap16(a, b); return a + b; }
DI u32 sel4(int e, u32 a, u32 b, u32 c, u32 d) { const u32 lo = (e & 1) ? b : a, hi = (e & 1) ? d : c; return (e & 2) ? hi : lo; }

DI f32x4 mfma32(u32x4 a, u32x4 b, f32x4 c) { return __builtin_amdgcn_mfma_f32_16x16x32_bf16(__builtin_bit_cast(bf16x8, a), __builtin_bit_cast(bf16x8, b), c, 0, 0, 0); }
DI f32x4 mfma16(u32x2 a, u32x2 b, f32x4 c) { return __builtin_amdgcn_mfma_f32_16x16x16bf16_1k(__builtin_bit_cast(s16x4, a), __builtin_bit_cast(s16x4, b), c, 0, 0, 0); }
DI u32x2 trd(u32 addr) { return __builtin_bit_cast(u32x2, __builtin_amdgcn_ds_read_tr16_b64_v4i16((LAS s16x4*)addr)); }
DI u32x4 cat(u32x2 a, u32x2 b) { u32x4 r; r[0] = a[0]; r[1] = a[1]; r[2] = b[0]; r[3] = b[1]; return r; }
template <class T> DI T lds_ld(u32 addr) { return *(LAS T*)addr; }
template <class T> DI void lds_st(u32 addr, T v) { *(LAS T*)addr = v; }
#define CBAR() asm volatile("" ::: "memory")

DI u32 flag_ld(int i) { return (u32)__builtin_amdgcn_readfirstlane((int)*(volatile LAS u32*)(i == F_LAND ? L_LAND : L_FLG + 4 * i)); }
DI void flag_st(int i, u32 v, int lane) { CBAR(); if (lane == 0) *(volatile LAS u32*)(L_FLG + 4 * i) = v; CBAR(); }
DI void flag_add(int i, int lane) { CBAR(); if (lane == 0) __hip_atomic_fetch_add((LAS u32*)(L_FLG + 4 * i), 1u, __ATOMIC_RELAXED, __HIP_MEMORY_SCOPE_WORKGROUP); CBAR(); }
DI u32 flag_add_ret(int i, int lane) {
    CBAR(); u32 o = 0;
    if (lane == 0) o = __hip_atomic_fetch_add((LAS u32*)(L_FLG + 4 * i), 1u, __ATOMIC_RELAXED, __HIP_MEMORY_SCOPE_WORKGROUP);
    o = (u32)__builtin_amdgcn_readfirstlane((int)o); CBAR(); return o;
}
template <bool SLEEP> DI void wait_ge(int i, u32 target, bool& dead) {
    u32 n = 0;
    while (!dead && flag_ld(i) < target) { if (++n > CAP_L) dead = true; if (SLEEP) __builtin_amdgcn_s_sleep(1); }
    CBAR();
}
DI bool all_ok(bool ok) { return __builtin_amdgcn_ballot_w64(!ok) == 0ull; }
DI bool none_sent(u32 mx) { return __builtin_amdgcn_ballot_w64(mx == SENT) == 0ull; }
#define POLL_STEP() { if (++spins > CAP_G) { dead = true; break; } __builtin_amdgcn_s_sleep(1); }

template <int N> struct PSet { u32x4 v[N]; };
template <int N> DI void take(PSet<N>& res, const PSet<N>& s) {
#pragma unroll
    for (int k = 0; k < N; ++k) res.v[k] = s.v[k];
}
#define PIPE 0
template <int N, int STAGGER, class LD, class OK>
DI void ppoll(PSet<N>& res, PSet<N>& a, PSet<N>& b, PSet<N>& c, LD ld, OK ok, bool& dead) {
    (void)a; (void)b; (void)c;
    for (u32 spins = 0;;) {
        CBAR(); ld(res); CBAR();
        if (ok(res)) break;
        if (++spins > CAP_G) { dead = true; break; }
    }
    CBAR();
}
template <int N> DI void retire(PSet<N>&, PSet<N>&, PSet<N>&) {}

#define TS(i) do { } while (0)
#define TCAP(v) do { } while (0)
#define TSV(i, t_) do { } while (0)
#define TSDUMP() do { } while (0)

DI u32x4 pack_row(u32 v16) {
    u32 pr = v16 | (dppu<SHL(1)>(v16) << 16);
    u32x4 r; r[0] = pr; r[1] = dppu<SHL(2)>(pr); r[2] = dppu<SHL(4)>(pr); r[3] = dppu<SHL(6)>(pr);
    return san4(r);
}

template <int NTL> DI void dma3(u32 lds, u32 v0, u32 v1, u32 v2, rsrc_t rs, u32 soff) {
    if constexpr (NTL) asm volatile("s_mov_b32 m0, %0\n\ts_nop 3\n\tbuffer_load_dwordx4 %1, %4, %5 offen nt lds\n\tbuffer_load_dwordx4 %2, %4, %5 offen offset:1024 nt lds\n\t"
                                    "buffer_load_dwordx4 %3, %4, %5 offen offset:2048 nt lds" ::"s"(lds), "v"(v0), "v"(v1), "v"(v2), "s"(rs), "s"(soff) : "memory");
    else asm volatile("s_mov_b32 m0, %0\n\ts_nop 3\n\tbuffer_load_dwordx4 %1, %4, %5 offen lds\n\tbuffer_load_dwordx4 %2, %4, %5 offen offset:1024 lds\n\t"
                      "buffer_load_dwordx4 %3, %4, %5 offen offset:2048 lds" ::"s"(lds), "v"(v0), "v"(v1), "v"(v2), "s"(rs), "s"(soff) : "memory");
}
template <int N> DI void waitvm() { asm volatile("s_waitcnt vmcnt(%0)" ::"i"(N) : "memory"); }
struct Ldr { int issued; int pub; u32 ladr; bool dead; };
DI void publish(Ldr& s, int landed) { s.pub = landed > s.pub ? landed : s.pub; CBAR(); *(volatile LAS u32*)s.ladr = (u32)s.pub; CBAR(); }
template <int W> DI void step3(Ldr& s) { s.issued += 3; waitvm<W>(); publish(s, s.issued - W); }
template <int N> DI void drain(Ldr& s) { waitvm<N>(); publish(s, s.issued - N); if constexpr (N >= 3) drain<N - 3>(s); }
template <int W> DI void park(Ldr& s, int flag, u32 target) {
    if (flag_ld(flag) < target) { drain<W - 3>(s); wait_ge<true>(flag, target, s.dead); }
    CBAR();
}
template <int W, int NTL> DI void seg3(Ldr& s, u32 lds0, const u32 (&v)[3], rsrc_t rs, u32 soff0, u32 sstep, int ng) {
    u32 so = soff0, ld = lds0;
#pragma unroll 1
    for (int g = 0; g < ng; ++g) {
        dma3<NTL>(ld, v[0], v[1], v[2], rs, so);
        so += sstep; ld += 3072u;
        step3<W>(s);
    }
}

extern __shared__ __attribute__((aligned(16))) char smem_dyn[];

extern "C" __global__ void __launch_bounds__(640, 3)
adaln_mla_ffn_kernel(const void* __restrict__ p_x, const void* __restrict__ p_ckv, const void* __restrict__ p_wq, const void* __restrict__ p_wuk,
                     const void* __restrict__ p_wuv, const void* __restrict__ p_wo, const void* __restrict__ p_w1, const void* __restrict__ p_w2,
                     const void* __restrict__ p_c, const void* __restrict__ p_wmod, void* __restrict__ p_out, void* __restrict__ p_ws)
{
    const int tid = threadIdx.x, lane = tid & 63;
    const int wave = __builtin_amdgcn_readfirstlane(tid >> 6);
    const int b = blockIdx.x, x = b & 7, r = b >> 3;
    const bool typeA = r < 20;
    const int g = lane >> 4, i16 = lane & 15, q4 = i16 >> 2, p4 = lane & 3;
    bool dead = false;
    (void)smem_dyn;
    if (tid < 17) { u32x4 z; z[0] = 0; z[1] = 0; z[2] = 0; z[3] = 0; lds_st<u32x4>(tid < 16 ? L_FLG + tid * 16 : L_LAND, z); }
    asm volatile("" ::"s"(p_x), "s"(p_ckv), "s"(p_wq), "s"(p_wuk), "s"(p_wuv), "s"(p_wo), "s"(p_w1), "s"(p_w2), "s"(p_c), "s"(p_wmod), "s"(p_out), "s"(p_ws));
    asm volatile("s_waitcnt lgkmcnt(0)" ::: "memory");
    __builtin_amdgcn_s_barrier();
    CBAR();
    TCAP(tbw);
    const rsrc_t rWS = mkrs(p_ws, W_END);

    if (wave == 8) {
        Ldr s; s.issued = 0; s.pub = 0; s.ladr = L_LAND + 4 * lane; s.dead = false;
        __builtin_amdgcn_s_setprio(LPRIO);
        u32 v1[3], vt[3], vu[3];
#pragma unroll
        for (int k = 0; k < 3; ++k) {
            const u32 p = 64 * k + lane;
            const u32 row = p / 12, ch = (p % 12) ^ (((row >> 2) & 1) << 1);
            v1[k] = row * 12288u + ch * 16 - 1024u * k; vt[k] = row * 1536u + ch * 16 - 1024u * k;
            const u32 r2 = p / 24, c2 = (p % 24) ^ (((r2 >> 1) & 3) << 1);
            vu[k] = r2 * 12288u + c2 * 16 - 1024u * k;
        }
        const rsrc_t rq = mkrs(p_wq, M_ * H_ * D_ * 2), ruk = mkrs(p_wuk, DC * H_ * D_ * 2), ruv = mkrs(p_wuv, DC * H_ * D_ * 2), rkv = mkrs(p_ckv, SK * DC * 2);
        const u32 ks = r >> 3, h1 = 4 * x + ((r >> 1) & 3), half = r & 1;
        const u32 so1 = 160u * ks * 12288u + (h1 * 192 + 96 * half) * 2;
        TCAP(tb8);
        seg3<WIN, NT_Q>(s, L_S1W, v1, rq, so1, 16u * 12288u, 10);
        TS(44); TSV(51, t_entry); TSV(54, tb8);
        const u32 so2 = 96u * x * 12288u + (u32)r * 384u;
        seg3<WIN, NT_UK>(s, L_X, vu, ruk, so2, 8u * 12288u, 12);
        TS(45);
        const u32 jt = x + 8 * (r & 15);
        const u32 sot = 64u * jt * 1536u;
#pragma unroll 1
        for (int w = 0; w < 8; ++w) {
            if (w == 5) park<WIN>(s, F_S1DONE, 6);
            seg3<WIN, NT_KV>(s, L_A + BLK * w, vt, rkv, sot + 192u * w, 16u * 1536u, 4);
        }
        TS(46);
        if (PAUSE) { drain<WIN - 3>(s); TS(47); wait_ge<true>(F_QAOK, 8, s.dead); }
        park<WIN>(s, F_S2DONE, 6);
        seg3<WIN2, NT_UV>(s, L_X, vu, ruv, so2, 8u * 12288u, 12);
        TS(48);
        park<(WIN > WIN2 ? WIN : WIN2)>(s, F_BLK, 8);
        if (typeA) {
            const rsrc_t ro = mkrs(p_wo, H_ * D_ * M_ * 2);
            const u32 row = lane >> 2, ch = (lane & 3) ^ (((row >> 3) & 1) << 1);
            const u32 vf = row * 1280u + ch * 16;
            const u32 vo[3] = {vf, vf + 16u * 1280u - 1024u, vf + 32u * 1280u - 2048u};
#pragma unroll 1
            for (int w = 0; w < 8; ++w)
                seg3<WIN2, NT_O>(s, L_A + BLK * w + B_W, vo, ro, (768u * x + 96u * w) * 1280u + 64u * r, 48u * 1280u, 2);
        } else {
            const rsrc_t r1 = mkrs(p_w1, M_ * DFF * 2), r2 = mkrs(p_w2, DFF * M_ * 2);
            const u32 fb = x * 12 + (r - 20);
            const u32 va = (lane >> 1) * 3072u + (lane & 1) * 16;
            const u32 v1a[3] = {va, va + 32u * 3072u - 1024u, lane < 32 ? va + 64u * 3072u - 2048u : OOB};
            u32 vb[3];
#pragma unroll
            for (int k = 0; k < 3; ++k) { const u32 p = 64 * k + lane; vb[k] = p < 160 ? (p / 10) * 1280u + (p % 10) * 16 - 1024u * k : OOB; }
#pragma unroll 1
            for (int w = 0; w < 8; ++w) {
                const u32 ld = L_A + BLK * w;
                dma3<NT_W1>(ld + B_W, v1a[0], v1a[1], v1a[2], r1, 80u * w * 3072u + fb * 32u);
                step3<WIN2>(s);
                dma3<NT_W2>(ld + B_W2, vb[0], vb[1], vb[2], r2, 16u * fb * 1280u + 160u * w);
                step3<WIN2>(s);
            }
        }
        TS(49);
        drain<(WIN > WIN2 ? WIN : WIN2) - 3>(s);
        TS(50);
        TSDUMP();
        return;
    }

    if (wave == 9) {
        const u32x2 ep = ld64<SC1>(rWS, W_EPOCH);
        u32x4 sv; sv[0] = SENT; sv[1] = SENT; sv[2] = SENT; sv[3] = SENT;
        {
            const u32 c0 = (u32)b * SMALL_PER + lane, c1 = c0 + 64;
            st128<SC1>(sv, rWS, c0 < SMALL_CH ? W_SMALL + c0 * 16 : OOB);
            st128<SC1>(sv, rWS, (lane + 64 < (int)SMALL_PER && c1 < SMALL_CH) ? W_SMALL + c1 * 16 : OOB);
        }
        {
            u64 pc; u32 before, len;
            asm volatile("s_getpc_b64 %0\n.Lpf%=:\n\ts_mov_b32 %1, .Lpf%=-" KNAME "\n\ts_mov_b32 %2, " FEND "-" KNAME : "=s"(pc), "=s"(before), "=s"(len));
            const rsrc_t rcode = mkrs((const void*)(pc - before), len < 65536u ? len : 0u);
            const u32x4 c0 = ld128<0>(rcode, (u32)r * 2048u + lane * 16u), c1 = ld128<0>(rcode, (u32)r * 2048u + 1024u + lane * 16u);
            asm volatile("" ::"v"(c0), "v"(c1));
        }
        const int it = 255 - b;
        const rsrc_t rc = mkrs(p_c, M_ * 2), rm = mkrs(p_wmod, M_ * 6 * M_ * 2);
        u32 cs[5];
        if (it < 160) {
#pragma unroll
            for (int k = 0; k < 5; ++k) cs[k] = ld16(rc, (320 * (it & 1) + 64 * k + lane) * 2);
        }
        TCAP(tb9);
        waitvm<0>();
        TSV(52, t_entry); TSV(55, tb9);
        const u64 tag = (((u64)ep[1] << 32) | ep[0]) + 1ull;
        u32x2 tg; tg[0] = (u32)tag; tg[1] = (u32)(tag >> 32);
        if (lane == 0) st64<SC1>(tg, rWS, W_FA + 8 * b);
        TS(39);
        wait_ge<true>(F_HX, 5, dead);
        {
            u32 spins = 0;
            for (;;) {
                CBAR();
                const u32x4 a = ld128<SC1>(rWS, W_FA + lane * 16), c = ld128<SC1>(rWS, W_FA + 1024 + lane * 16);
                const bool ok = a[0] == tg[0] && a[1] == tg[1] && a[2] == tg[0] && a[3] == tg[1] && c[0] == tg[0] && c[1] == tg[1] && c[2] == tg[0] && c[3] == tg[1];
                if (all_ok(ok)) break;
                POLL_STEP();
            }
            flag_st(F_RDYA, 1, lane);
            TS(40);
        }
        if (it < 160) {
            const int cb = it >> 1, kh = it & 1;
#pragma unroll
            for (int k = 0; k < 5; ++k) {
                const float cv = __uint_as_float(cs[k] << 16);
                lds_st<float>(L_SIL + (64 * k + lane) * 4, cv * __builtin_amdgcn_rcpf(1.f + __expf(-cv)));
            }
            if (MRAT) wait_ge<true>(F_LAND, T_TILE0 + 96, dead); else wait_ge<true>(F_QAOK, 8, dead);
            TS(42);
            u32x4 wv[20];
            const u32 vo = (u32)(320 * kh + (lane >> 2)) * 7680u + (1280u + 32u * cb) * 2u + (lane & 3) * 16u;
#pragma unroll
            for (int k = 0; k < 20; ++k) wv[k] = ld128<NT_MR>(rm, vo + (u32)k * 16u * 7680u);
            float acc[8];
#pragma unroll
            for (int j = 0; j < 8; ++j) acc[j] = 0.f;
#pragma unroll
            for (int k = 0; k < 20; ++k) {
                const float sl = lds_ld<float>(L_SIL + (16 * k + (lane >> 2)) * 4);
#pragma unroll
                for (int j = 0; j < 4; ++j) { acc[2 * j] += sl * f_lo(wv[k][j]); acc[2 * j + 1] += sl * f_hi(wv[k][j]); }
            }
            float t4[4], t2[2];
#pragma unroll
            for (int j = 0; j < 4; ++j) t4[j] = sc32(acc[j], acc[j + 4]);
#pragma unroll
            for (int j = 0; j < 2; ++j) t2[j] = sc16(t4[j], t4[j + 2]);
#pragma unroll
            for (int j = 0; j < 2; ++j) { t2[j] += dppf<ROR(8)>(t2[j]); t2[j] += dppf<ROR(4)>(t2[j]); }
            const u32 pm = cb / 20;
#pragma unroll
            for (int j = 0; j < 2; ++j) {
                const u32 col = (cb % 20) * 32u + 8 * (lane & 3) + 4 * (lane >> 5) + 2 * ((lane >> 4) & 1) + j;
                const u32 cw = col / 80, cc = col % 80;
                const u32 pos = cc < 64 ? (cc & 7) * 8 + (cc >> 3) : 64 + (cc & 7) * 2 + ((cc >> 3) & 1);
                st32<SC1>(san(__float_as_uint(t2[j])), rWS, (lane & 12) == 0 ? W_MR + ((kh * 8u + cw) * 80u + pos) * 16u + pm * 4u : OOB);
            }
            TS(43);
        }
        TSDUMP();
        return;
    }

    const int w = wave;
    const rsrc_t rX = mkrs(p_x, M_ * 2);
    const u32 ks = r >> 3;
    const bool front = w < 5;
    const bool doS0 = typeA && front;
    const u32 cg = (x & 1) * 20 + r, kp = x >> 1;
    u32x4 m0v, m1v, xa; u32 cv = 0, xb = 0; u32x2 xo;
    if (doS0) {
        const rsrc_t rc = mkrs(p_c, M_ * 2), rm = mkrs(p_wmod, M_ * 6 * M_ * 2);
        const u32 row0 = 160 * kp + 32 * w + 2 * (lane >> 2);
        const u32 off = row0 * 7680u + cg * 64u + (lane & 3) * 16u;
        m0v = ld128<NT_S0>(rm, off); m1v = ld128<NT_S0>(rm, off + 7680u);
        cv = ld32<0>(rc, row0 * 2);
    }
    const u32x2 ep = ld64<SC1>(rWS, W_EPOCH);
    if (front) {
        xa = ld128<0>(rX, lane * 16); xb = ld32<0>(rX, 1024 + lane * 4);
        xo = ld64<0>(rX, (160 * ks + 32 * w + 4 * (lane & 7)) * 2);
    }
    if (w == 0) TSV(53, tbw);
    if (w == 5) TSV(56, tbw);
    if (w == 7) { TSV(57, tbw); TSV(58, t_entry); }
    if (w == 0) TSV(30, t_entry);
    if (w == 0) TS(0);
    const u64 tag = (((u64)(u32)__builtin_amdgcn_readfirstlane((int)ep[1]) << 32) | (u32)__builtin_amdgcn_readfirstlane((int)ep[0])) + 1ull;
    const u32 T1 = (u32)tag, T2 = (u32)(tag >> 32);
    const u32 tagl = T1 & 0xFFu, tg12 = T1 & 0xFFFu;

    if (doS0) {
        const float cA = f_lo(cv), cB = f_hi(cv);
        const float sA = cA * __builtin_amdgcn_rcpf(1.f + __expf(-cA)), sB = cB * __builtin_amdgcn_rcpf(1.f + __expf(-cB));
        const u32 sph = pk2(sA, sB), spl = pk2(sA - f_lo(sph), sB - f_hi(sph));
        float acc[8];
#pragma unroll
        for (int j = 0; j < 4; ++j) {
            const u32 pa = __builtin_amdgcn_perm(m1v[j], m0v[j], 0x05040100u), pb = __builtin_amdgcn_perm(m1v[j], m0v[j], 0x07060302u);
            acc[2 * j] = dot2(pa, spl, dot2(pa, sph, 0.f));
            acc[2 * j + 1] = dot2(pb, spl, dot2(pb, sph, 0.f));
        }
        if (w == 0) TS(31);
        float t4[4]; f32x2 t2;
#pragma unroll
        for (int j = 0; j < 4; ++j) t4[j] = sc32(acc[j], acc[j + 4]);
#pragma unroll
        for (int j = 0; j < 2; ++j) { float t = sc16(t4[j], t4[j + 2]); t += dppf<ROR(8)>(t); t += dppf<ROR(4)>(t); t2[j] = t; }
        if ((lane & 12) == 0) lds_st<f32x2>(L_S0P + (w * 32 + 8 * (lane & 3) + 4 * (lane >> 5) + 2 * ((lane >> 4) & 1)) * 4, t2);
        if (flag_add_ret(F_S0CNT, lane) == 4) {
            f32x4 v = lds_ld<f32x4>(L_S0P + (lane & 7) * 16);
#pragma unroll
            for (int p = 1; p < 5; ++p) v += lds_ld<f32x4>(L_S0P + p * 128 + (lane & 7) * 16);
            u32x4 o;
#pragma unroll
            for (int j = 0; j < 4; ++j) o[j] = tagw(v[j], tg12);
            const u32 wh = cg >= 20 ? 1u : 0u, m0 = 32 * (cg - 20 * wh);
            st128<SC1>(o, rWS, lane < 8 ? W_G1 + ((wh * 4 + kp) * 640u + m0 + 4 * lane) * 4u : OOB);
            TS(1);
        }
    }

    const u32 h1 = 4 * x + ((r >> 1) & 3), half = r & 1;
    u32x4 bw[5];
    auto read_bw = [&]() {
        wait_ge<true>(F_LAND, T_S1, dead);
        const u32 ab = L_S1W + (4 * g + q4) * 192 + (((2 * w + (p4 >> 1)) ^ ((g & 1) << 1)) * 16) + 8 * (p4 & 1);
#pragma unroll
        for (int t = 0; t < 5; ++t) bw[t] = cat(trd(ab + (32 * t) * 192), trd(ab + (32 * t + 16) * 192));
        asm volatile("s_waitcnt lgkmcnt(0)" ::: "memory");
        flag_add(F_S1DONE, lane);
        if (w == 0) TS(32);
    };
    if (w == 5) read_bw();
    if (front) {
        float xv[10];
#pragma unroll
        for (int k = 0; k < 4; ++k) { xv[2 * k] = f_lo(xa[k]); xv[2 * k + 1] = f_hi(xa[k]); }
        xv[8] = f_lo(xb); xv[9] = f_hi(xb);
        float sum = 0.f;
#pragma unroll
        for (int k = 0; k < 10; ++k) sum += xv[k];
        const float mu = wave_sum(sum) * (1.f / 640.f);
        float sq = 0.f;
#pragma unroll
        for (int k = 0; k < 10; ++k) { const float a = xv[k] - mu; sq += a * a; }
        const float rstd = 1.f / sqrtf(wave_sum(sq) * (1.f / 640.f) + 1e-5f);
        float aj[4], bj[4];
#pragma unroll
        for (int j = 0; j < 4; ++j) {
            const float xn = (((j & 1) ? f_hi(xo[j >> 1]) : f_lo(xo[j >> 1])) - mu) * rstd;
            aj[j] = lane >= 32 ? xn : 1.f; bj[j] = lane < 8 ? xn : 0.f;
        }
        const u32 goff = W_G1 + ((u32)(lane >> 3) * 640u + 160 * ks + 32 * w + 4 * (lane & 7)) * 4u;
        const u32 hadr = L_HX + ((lane >> 3) & 1) * 320 + (32 * w + 8 * (lane & 3) + 4 * ((lane >> 2) & 1) + 2 * (lane >> 5) + ((lane >> 4) & 1)) * 2;
        read_bw();
        PSet<1> gv, pa, pb, pc;
        ppoll<1, STG>(gv, pa, pb, pc,
            [&](PSet<1>& s) { s.v[0] = ld128<SC1>(rWS, goff); },
            [&](const PSet<1>& s) { return all_ok(tag_ok(s.v[0], tg12)); }, dead);
        if (w == 4) TS(2);
        float t[4];
#pragma unroll
        for (int j = 0; j < 4; ++j) t[j] = aj[j] * untag(gv.v[0][j]) + bj[j];
        float hv = sc16(sc32(t[0], t[2]), sc32(t[1], t[3]));
        hv += dppf<ROR(8)>(hv);
        const u32 hi = bf1(hv), lo = bf1(hv - __uint_as_float(hi << 16));
        lds_st<unsigned short>(hadr, (unsigned short)((lane & 8) ? lo : hi));
        flag_add(F_HX, lane);
        retire(pa, pb, pc);
    }
    u32x4 bw2[6];
    if (w < 6) {
        wait_ge<false>(F_HX, 5, dead);
        f32x4 acc = {0.f, 0.f, 0.f, 0.f};
#pragma unroll
        for (int t = 0; t < 5; ++t) acc = mfma32(lds_ld<u32x4>(L_HX + (lane & 1) * 320 + 64 * t + 16 * g), bw[t], acc);
        const u32 qb = tagw(acc[0] + acc[1], tg12);
        u32x4 o; o[0] = qb; o[1] = dppu<SHL(1)>(qb); o[2] = dppu<SHL(2)>(qb); o[3] = dppu<SHL(3)>(qb);
        st128<SC1>(o, rWS, (lane & 51) == 0 ? W_Q2 + (ks * 6144u + h1 * 192 + 96 * half + 16 * w + lane) * 4u : OOB);
        if (w == 0) TS(3);
        wait_ge<true>(F_LAND, T_S2, dead);
        const u32 rowb = L_X + (16 * w + i16) * 384, sw = ((i16 >> 1) & 3) << 1;
#pragma unroll
        for (int t = 0; t < 6; ++t) bw2[t] = lds_ld<u32x4>(rowb + (((4 * t + g) ^ sw) * 16));
        flag_add(F_S2DONE, lane);
        if (w == 0) TS(33);
    }
    if (w >= 5) {
        if (w != 5) wait_ge<true>(F_HX, 5, dead);
        const u32 qo = W_Q2 + ((u32)g * 6144u + (u32)r * 192u + 64 * (w - 5) + 4 * i16) * 4u;
        const u32 qadr = L_QH + (64 * (w - 5) + 4 * i16 + 2 * (lane >> 5) + ((lane >> 4) & 1)) * 2;
        PSet<1> qv1, pa, pb, pc;
        ppoll<1, STG>(qv1, pa, pb, pc,
            [&](PSet<1>& s) { s.v[0] = ld128<SC1>(rWS, qo); },
            [&](const PSet<1>& s) { return all_ok(tag_ok(s.v[0], tg12)); }, dead);
        if (w == 7) TS(4);
        const float q = sc16(sc32(untag(qv1.v[0][0]), untag(qv1.v[0][2])), sc32(untag(qv1.v[0][1]), untag(qv1.v[0][3])));
        const u32 hi = bf1(q), lo = bf1(q - __uint_as_float(hi << 16));
        lds_st<unsigned short>(qadr, (unsigned short)hi); lds_st<unsigned short>(qadr + 384, (unsigned short)lo);
        flag_add(F_QH, lane);
        retire(pa, pb, pc);
    }
    if (w < 6) {
        wait_ge<false>(F_QH, 3, dead);
        f32x4 a0 = {0.f, 0.f, 0.f, 0.f}, a1 = {0.f, 0.f, 0.f, 0.f};
#pragma unroll
        for (int t = 0; t < 6; t += 2) {
            a0 = mfma32(lds_ld<u32x4>(L_QH + (lane & 1) * 384 + 64 * t + 16 * g), bw2[t], a0);
            a1 = mfma32(lds_ld<u32x4>(L_QH + (lane & 1) * 384 + 64 * (t + 1) + 16 * g), bw2[t + 1], a1);
        }
        const float qa = ((a0[0] + a0[1]) + (a1[0] + a1[1])) * (0.07216878364870322f * 1.4426950408889634f);
        const u32 qh = bf1(qa), ql = bf1(qa - __uint_as_float(qh << 16));
        const u32x4 o = pack_row((g & 1) ? ql : qh);
        const u32 dst = W_QA + ((u32)r * 8 + x) * 384u + (w >> 1) * 128 + g * 64 + (w & 1) * 32 + ((lane >> 3) & 1) * 16;
        wait_ge<false>(F_RDYA, 1, dead);
        st128<SC1>(o, rWS, (lane & 39) == 0 ? dst : OOB);
        flag_add(F_QAST, lane);
        if (w == 0) TS(5);
    }

    const u32 g2 = r >> 4;
    const u32 blk = L_A + BLK * w;
    u32x4 vt[6][2];
    {
        u32x4 bq[6];
        const u32 qoff = W_QA + ((16 * g2 + i16) * 8 + w) * 384u + g * 16u;
        auto issue_q = [&]() {
            CBAR();
#pragma unroll
            for (int k = 0; k < 6; ++k) bq[k] = ld128<SC1>(rWS, qoff + k * 64);
            CBAR();
        };
        bool seen = false, gate = false, landed = false; u32 tq = 0;
        auto gate_step = [&]() {
            if (!seen) { if (flag_ld(F_QAST) >= 6) { seen = true; tq = (u32)__builtin_amdgcn_s_memtime(); } }
            else gate = (u32)__builtin_amdgcn_s_memtime() - tq >= QDLY * 64u;
        };
        wait_ge<false>(F_RDYA, 1, dead);
        for (u32 n = 0; !dead;) {
            landed = flag_ld(F_LAND) >= T_TILE0 + 12 * (w + 1);
            if (landed) break;
            gate_step();
            if (gate) break;
            if (++n > CAP_L) dead = true;
            __builtin_amdgcn_s_sleep(1);
        }
        if (!landed) { issue_q(); wait_ge<true>(F_LAND, T_TILE0 + 12 * (w + 1), dead); }
        if (w == 0) TS(6);
        if (w == 7) TS(27);
        u32x4 av[4][3];
        const u32 ar = blk + i16 * 192, asw = ((i16 >> 2) & 1) << 1;
#pragma unroll
        for (int kb = 0; kb < 4; ++kb)
#pragma unroll
            for (int t = 0; t < 3; ++t) av[kb][t] = lds_ld<u32x4>(ar + kb * 16 * 192 + ((4 * t + g) ^ asw) * 16);
        const u32 vb = blk + (4 * g + q4) * 192 + (p4 >> 1) * 16 + 8 * (p4 & 1);
#pragma unroll
        for (int u = 0; u < 6; ++u) {
            const u32 cu = vb + (u ^ (g & 1)) * 32;
#pragma unroll
            for (int t = 0; t < 2; ++t) vt[u][t] = cat(trd(cu + (32 * t) * 192), trd(cu + (32 * t + 16) * 192));
        }
        asm volatile("s_waitcnt lgkmcnt(0)" ::: "memory");
        flag_add(F_BLK, lane);
        if (w == 7) TS(28);
        if (landed) {
            for (u32 n = 0; !dead && !gate;) { gate_step(); if (++n > CAP_L) dead = true; if (!gate) __builtin_amdgcn_s_sleep(1); }
            issue_q();
        }
        {
            u32 spins = 0;
            for (;;) {
                u32 mx = 0;
#pragma unroll
                for (int k = 0; k < 6; ++k) mx = mx4(mx, bq[k]);
                if (none_sent(mx)) break;
                POLL_STEP();
                issue_q();
            }
        }
        flag_add(F_QAOK, lane);
        if (w == 0) TS(7);
        if (w == 7) TS(26);
#pragma unroll
        for (int kb = 0; kb < 4; ++kb) {
            f32x4 acc = {0.f, 0.f, 0.f, 0.f};
#pragma unroll
            for (int t = 0; t < 3; ++t) { acc = mfma32(av[kb][t], bq[2 * t + 1], acc); acc = mfma32(av[kb][t], bq[2 * t], acc); }
            f32x2 lo2, hi2; lo2[0] = acc[0]; lo2[1] = acc[1]; hi2[0] = acc[2]; hi2[1] = acc[3];
            lds_st<f32x2>(blk + (kb * 2) * 512 + lane * 8, lo2);
            lds_st<f32x2>(blk + (kb * 2 + 1) * 512 + lane * 8, hi2);
        }
        flag_add(F_SADD, lane);
        if (w == 0) TS(20);
    }
    {
        wait_ge<false>(F_SADD, 8, dead);
        if (w == 0) TS(21);
        f32x2 s = {0.f, 0.f};
#pragma unroll
        for (int p = 0; p < 8; ++p) s += lds_ld<f32x2>(L_A + BLK * p + w * 512 + lane * 8);
        lds_st<f32x2>(L_SC + (w >> 1) * 1024 + lane * 16 + (w & 1) * 8, s);
        flag_add(F_SC, lane);
        wait_ge<false>(F_SC, 8, dead);
        if (w == 0) TS(22);
        f32x4 sc[4];
#pragma unroll
        for (int q = 0; q < 4; ++q) sc[q] = lds_ld<f32x4>(L_SC + q * 1024 + lane * 16);
        float mloc = fmaxf(fmaxf(fmaxf(sc[0][0], sc[0][1]), fmaxf(sc[0][2], sc[0][3])), fmaxf(fmaxf(sc[1][0], sc[1][1]), fmaxf(sc[1][2], sc[1][3])));
        mloc = fmaxf(mloc, fmaxf(fmaxf(fmaxf(sc[2][0], sc[2][1]), fmaxf(sc[2][2], sc[2][3])), fmaxf(fmaxf(sc[3][0], sc[3][1]), fmaxf(sc[3][2], sc[3][3]))));
        mloc = xg_max(mloc);
        u32x4 pf[2];
#pragma unroll
        for (int t = 0; t < 2; ++t)
#pragma unroll
            for (int e = 0; e < 4; ++e) {
                const f32x4 sq = sc[2 * t + (e >> 1)];
                pf[t][e] = pk2(__builtin_amdgcn_exp2f(sq[2 * (e & 1)] - mloc), __builtin_amdgcn_exp2f(sq[2 * (e & 1) + 1] - mloc));
            }
        f32x4 oa[6], lz;
#pragma unroll
        for (int u = 0; u < 6; ++u) { f32x4 z = {0.f, 0.f, 0.f, 0.f}; oa[u] = mfma32(vt[u][0], pf[0], z); oa[u] = mfma32(vt[u][1], pf[1], oa[u]); }
        {
            u32x4 one; one[0] = 0x3F803F80u; one[1] = 0x3F803F80u; one[2] = 0x3F803F80u; one[3] = 0x3F803F80u;
            f32x4 z = {0.f, 0.f, 0.f, 0.f};
            lz = mfma32(one, pf[0], z); lz = mfma32(one, pf[1], lz);
        }
        if (w == 0) TS(23);
        const u32 jj = x * 16 + (r & 15);
        const u32 ob = W_O + ((w * 128 + jj) * 2 + g2) * 3072u + (i16 * 4 + g) * 16;
        u32 cx = 0;
#pragma unroll
        for (int k = 0; k < 3; ++k) {
            u32x4 d;
            d[0] = pk2(oa[2 * k][0], oa[2 * k][1]); d[1] = pk2(oa[2 * k][2], oa[2 * k][3]);
            d[2] = pk2(oa[2 * k + 1][0], oa[2 * k + 1][1]); d[3] = pk2(oa[2 * k + 1][2], oa[2 * k + 1][3]);
            st128<SC1>(d, rWS, ob + k * 1024);
            cx ^= (d[0] ^ d[1]) ^ (d[2] ^ d[3]);
        }
        cx = xg_xor(cx);
        u32x4 pk;
        pk[0] = __float_as_uint(mloc); pk[1] = __float_as_uint(lz[0]);
        pk[2] = cx ^ pk[0] ^ pk[1] ^ T2; pk[3] = T1;
        st128<SC1>(pk, rWS, g == 0 ? W_PK + ((w * 128 + jj) * 32 + 16 * g2 + i16) * 16 : OOB);
        if (w == 0) TS(8);
    }

    {
        const u32 poff = W_PK + ((x * 128 + 16 * w + 4 * g + (i16 & 3)) * 32 + r) * 16;
        const u32 ooff = i16 < 12 ? W_O + ((x * 128 + 16 * w + 4 * g) * 2 + g2) * 3072u + (i16 >> 2) * 1024 + ((r & 15) * 4 + (i16 & 3)) * 16 : OOB;
        u32x4 bw[2][3];
        if (w < 6) {
            wait_ge<true>(F_LAND, T_UV, dead);
            const u32 sw = 2 * (g & 1) + (q4 >> 1);
#pragma unroll
            for (int d = 0; d < 2; ++d) {
                const u32 ub = L_X + (4 * g + q4) * 384 + (2 * ((2 * w + d) ^ sw) + (p4 >> 1)) * 16 + 8 * (p4 & 1);
#pragma unroll
                for (int t = 0; t < 3; ++t) bw[d][t] = cat(trd(ub + (32 * t) * 384), trd(ub + (32 * t + 16) * 384));
            }
        }
        if (ODLY) __builtin_amdgcn_s_sleep(ODLY);
        u32x4 pk, od[4];
        {
            u32 spins = 0;
            for (;;) {
                CBAR(); pk = ld128<SC1>(rWS, poff); CBAR();
                if (all_ok(pk[3] == T1)) break;
                POLL_STEP();
            }
        }
        if (w == 0) TS(35);
        {
            u32 spins = 0;
            u64 bad = ~0ull;
            for (;;) {
                CBAR();
#pragma unroll
                for (int k = 0; k < 4; ++k) if (bad & (0x1111111111111111ull << k)) od[k] = ld128<SC1>(rWS, ooff + k * 6144);
                if (!ELIGHT || spins) pk = ld128<SC1>(rWS, poff);
                CBAR();
                u32 c[4];
#pragma unroll
                for (int k = 0; k < 4; ++k) c[k] = (od[k][0] ^ od[k][1]) ^ (od[k][2] ^ od[k][3]);
                const bool b0 = lane & 1, b1 = lane & 2;
                const u32 t01 = (b0 ? c[1] : c[0]) ^ dppu<QX1>(b0 ? c[0] : c[1]), t23 = (b0 ? c[3] : c[2]) ^ dppu<QX1>(b0 ? c[2] : c[3]);
                u32 t = (b1 ? t23 : t01) ^ dppu<QX2>(b1 ? t01 : t23);
                t ^= dppu<ROR(4)>(t); t ^= dppu<ROR(8)>(t);
                const bool ok = pk[3] == T1 && (t ^ pk[0] ^ pk[1] ^ T2) == pk[2];
                bad = __builtin_amdgcn_ballot_w64(!ok);
                if (bad == 0ull) break;
                POLL_STEP();
            }
        }
        if (w == 0) TS(9);
        const float mj = __uint_as_float(pk[0]);
        float Mw = fmaxf(mj, dppf<QX1>(mj)); Mw = fmaxf(Mw, dppf<QX2>(Mw)); Mw = xg_max(Mw);
        const u32 wb = bf1(__builtin_amdgcn_exp2f(mj - Mw));
        const u32 sh = (lane & 1) ? 65536u : 1u;
        u32x4 aw;
        aw[0] = __umul24(dppu<0x00>(wb), sh); aw[1] = __umul24(dppu<0x55>(wb), sh); aw[2] = __umul24(dppu<0xAA>(wb), sh); aw[3] = __umul24(dppu<0xFF>(wb), sh);
        u32x4 yv;
#pragma unroll
        for (int u = 0; u < 4; ++u) {
            u32x4 bo; bo[0] = od[0][u]; bo[1] = od[1][u]; bo[2] = od[2][u]; bo[3] = od[3][u];
            f32x4 z = {0.f, 0.f, 0.f, 0.f};
            z = mfma32(aw, bo, z);
            yv[u] = pk2(z[0], z[1]);
        }
        float lw = __uint_as_float(wb << 16) * __uint_as_float(pk[1]);
        lw += dppf<QX1>(lw); lw += dppf<QX2>(lw); lw = xg_sum(lw);
        if (lane < 12) lds_st<u32x4>(L_AM + w * 208 + lane * 16, yv);
        { f32x2 ml; ml[0] = Mw; ml[1] = lw; if (lane == 0) lds_st<f32x2>(L_EML + w * 8, ml); }
        flag_add(F_AM, lane);
        if (w < 6) {
            wait_ge<false>(F_AM, 8, dead);
            u32x4 aa[3];
#pragma unroll
            for (int t = 0; t < 3; ++t) aa[t] = lds_ld<u32x4>(L_AM + (i16 & 7) * 208 + (4 * t + g) * 16);
            const f32x4 e0 = lds_ld<f32x4>(L_EML + (g & 1) * 32), e1 = lds_ld<f32x4>(L_EML + (g & 1) * 32 + 16);
            float Mx = fmaxf(fmaxf(e0[0], e0[2]), fmaxf(e1[0], e1[2]));
            { float a = Mx, bb = Mx; swap16(a, bb); Mx = fmaxf(a, bb); }
            const float f0 = __builtin_amdgcn_exp2f(e0[0] - Mx), f1 = __builtin_amdgcn_exp2f(e0[2] - Mx), f2 = __builtin_amdgcn_exp2f(e1[0] - Mx), f3 = __builtin_amdgcn_exp2f(e1[2] - Mx);
            const float ls = (f0 * e0[1] + f1 * e0[3]) + (f2 * e1[1] + f3 * e1[3]);
            const float rl = __builtin_amdgcn_rcpf(sc16(ls, ls));
            float res[2];
#pragma unroll
            for (int d = 0; d < 2; ++d) {
                f32x4 z = {0.f, 0.f, 0.f, 0.f};
#pragma unroll
                for (int t = 0; t < 3; ++t) z = mfma32(aa[t], bw[d][t], z);
                res[d] = (f0 * z[0] + f1 * z[1]) + (f2 * z[2] + f3 * z[3]);
            }
            const u32x4 o = pack_row(bf1(sc16(res[0], res[1]) * rl));
            const u32 dst = W_CTX + ((u32)r * 8 + x) * 384u + w * 64 + g * 32 + ((lane >> 3) & 1) * 16;
            st128<SC1>(o, rWS, (lane & 39) == 0 ? dst : OOB);
            if (w == 0) TS(10);
        }
    }

    if (typeA) {
        const u32 coff = i16 < 8 ? W_CTX + ((4 * x + (w >> 1)) * 8 + i16) * 384u + (w & 1) * 192 + g * 16 : OOB;
        wait_ge<true>(F_LAND, T_UV + 6 * (w + 1), dead);
        u32x4 bw[2][3];
        const u32 fbx = blk + B_W + (8 * g + q4) * 64 + (p4 >> 1) * 16 + 8 * (p4 & 1);
#pragma unroll
        for (int u = 0; u < 2; ++u) {
            const u32 ub = fbx + (u ^ (g & 1)) * 32;
#pragma unroll
            for (int t = 0; t < 3; ++t) bw[u][t] = cat(trd(ub + (32 * t) * 64), trd(ub + (32 * t + 4) * 64));
        }
        if (w == 0) TS(36);
        PSet<3> ca, pa, pb, pc;
        ppoll<3, STG2>(ca, pa, pb, pc,
            [&](PSet<3>& s) { s.v[0] = ld128<SC1>(rWS, coff); s.v[1] = ld128<SC1>(rWS, coff + 64); s.v[2] = ld128<SC1>(rWS, coff + 128); },
            [&](const PSet<3>& s) { return none_sent(mx4(mx4(mx4(0, s.v[0]), s.v[1]), s.v[2])); }, dead);
        if (w == 0) TS(11);
        float res[2];
#pragma unroll
        for (int u = 0; u < 2; ++u) {
            f32x4 z = {0.f, 0.f, 0.f, 0.f};
#pragma unroll
            for (int t = 0; t < 3; ++t) z = mfma32(ca.v[t], bw[u][t], z);
            res[u] = xg_sum((z[0] + z[1]) + (z[2] + z[3]));
        }
        if (g < 2) lds_st<float>(L_FRP + (w * 32 + 16 * g + i16) * 4, g == 0 ? res[0] : res[1]);
        if (flag_add_ret(F_FRC, lane) == 7) {
            float v = 0.f;
#pragma unroll
            for (int p = 0; p < 8; ++p) v += lds_ld<float>(L_FRP + (p * 32 + (lane & 31)) * 4);
            const u32x4 o = pack_row(bf1(v));
            const u32 dst = W_AT + (x * 640u + 32u * r + 16 * (g & 1) + (lane & 8)) * 2u;
            st128<SC1>(o, rWS, (g < 2 && (lane & 7) == 0) ? dst : OOB);
            TS(12);
        }
        retire(pa, pb, pc);
    } else {
        const u32 fb = x * 12 + (r - 20);
        const u32 cA = 8 * (lane & 7) + (lane >> 3);
        const bool hasB = (lane & 7) < 2;
        const u32 colA = 80 * w + cA, colB = colA + 64;
        float ga[2], sb[2], cb1[2], gb[2], xv[2];
        {
            xv[0] = __uint_as_float(ld16(rX, colA * 2) << 16); xv[1] = __uint_as_float(ld16(rX, hasB ? colB * 2 : OOB) << 16);
            u32x4 mr[2][2]; u32 spins = 0;
            const u32 posB = 64 + (lane >> 3) * 2 + (lane & 1);
            for (;;) {
                CBAR();
                u32 mx = 0;
#pragma unroll
                for (int kh = 0; kh < 2; ++kh) {
                    mr[kh][0] = ld128<SC1>(rWS, W_MR + ((kh * 8u + w) * 80u + lane) * 16u);
                    mr[kh][1] = ld128<SC1>(rWS, hasB ? W_MR + ((kh * 8u + w) * 80u + posB) * 16u : OOB);
                    mx = mx4(mx4(mx, mr[kh][0]), mr[kh][1]);
                }
                if (none_sent(mx)) break;
                POLL_STEP();
            }
#pragma unroll
            for (int c = 0; c < 2; ++c) {
                ga[c] = __uint_as_float(mr[0][c][0]) + __uint_as_float(mr[1][c][0]);
                sb[c] = __uint_as_float(mr[0][c][1]) + __uint_as_float(mr[1][c][1]);
                cb1[c] = 1.f + (__uint_as_float(mr[0][c][2]) + __uint_as_float(mr[1][c][2]));
                gb[c] = __uint_as_float(mr[0][c][3]) + __uint_as_float(mr[1][c][3]);
            }
        }
        if (w == 0) TS(13);
        const u32 gab = blk + B_GA;
#pragma unroll
        for (int c = 0; c < 2; ++c) {
            if (c == 0 || hasB) {
                const u32 cc = cA + 64 * c;
                const u32 h = bf1(cb1[c]), l = bf1(cb1[c] - __uint_as_float(h << 16)), h2 = bf1(sb[c]), l2 = bf1(sb[c] - __uint_as_float(h2 << 16));
                lds_st<unsigned short>(gab + 2 * 160 + cc * 2, (unsigned short)h); lds_st<unsigned short>(gab + 3 * 160 + cc * 2, (unsigned short)l);
                lds_st<unsigned short>(gab + 4 * 160 + cc * 2, (unsigned short)h2); lds_st<unsigned short>(gab + 5 * 160 + cc * 2, (unsigned short)l2);
                lds_st<float>(blk + B_GB + cc * 4, gb[c]);
            }
        }
        wait_ge<true>(F_LAND, T_UV + 6 * (w + 1), dead);
        u32x2 b1[5], b2[5];
#pragma unroll
        for (int t = 0; t < 5; ++t) b1[t] = trd(blk + B_W + 512 * t + 8 * lane);
#pragma unroll
        for (int u = 0; u < 5; ++u) b2[u] = trd(blk + B_W2 + (4 * g + q4) * 160 + 32 * u + 8 * p4);
        if (w == 0) TS(36);
        const u32 aoff = W_AT + ((lane >> 3) * 640u + 80u * w + 8 * (lane & 7)) * 2u, aoff2 = hasB ? aoff + 128 : OOB;
        PSet<2> at, pa, pb, pc;
        ppoll<2, STG2>(at, pa, pb, pc,
            [&](PSet<2>& s) { s.v[0] = ld128<SC1>(rWS, aoff); s.v[1] = ld128<SC1>(rWS, aoff2); },
            [&](const PSet<2>& s) { return none_sent(mx4(mx4(0, s.v[0]), s.v[1])); }, dead);
        if (w == 0) TS(14);
        float x1[2];
#pragma unroll
        for (int c = 0; c < 2; ++c) {
            const u32x4 a = at.v[c];
            float v[8];
#pragma unroll
            for (int j = 0; j < 4; ++j) { v[2 * j] = f_lo(a[j]); v[2 * j + 1] = f_hi(a[j]); }
            float t4[4], t2[2];
#pragma unroll
            for (int j = 0; j < 4; ++j) t4[j] = sc32(v[j], v[j + 4]);
#pragma unroll
            for (int j = 0; j < 2; ++j) t2[j] = sc16(t4[j], t4[j + 2]);
            const bool b3 = (lane & 8) != 0;
            const float keep = b3 ? t2[1] : t2[0], give = b3 ? t2[0] : t2[1];
            const float attn = keep + dppf<ROR(8)>(give);
            x1[c] = xv[c] + ga[c] * attn;
        }
        if (!hasB) x1[1] = 0.f;
#pragma unroll
        for (int c = 0; c < 2; ++c) {
            if (c == 0 || hasB) {
                const u32 cc = cA + 64 * c;
                const float uv = x1[c] * cb1[c];
                const u32 h = bf1(uv), l = bf1(uv - __uint_as_float(h << 16));
                lds_st<unsigned short>(gab + cc * 2, (unsigned short)h); lds_st<unsigned short>(gab + 160 + cc * 2, (unsigned short)l);
                lds_st<float>(blk + B_X1 + cc * 4, x1[c]);
            }
        }
        CBAR();
        f32x4 z = {0.f, 0.f, 0.f, 0.f};
        const u32 arow = (q4 < 2 ? q4 : 2) * 2 + (lane & 1);
#pragma unroll
        for (int t = 0; t < 5; ++t) z = mfma16(lds_ld<u32x2>(gab + arow * 160 + 32 * t + 8 * g), b1[t], z);
        const float s1 = wave_sum(x1[0] + x1[1]), s2 = wave_sum(x1[0] * x1[0] + x1[1] * x1[1]);
        lds_st<float>(L_GRP + w * 256 + lane * 4, g < 3 ? z[0] + z[1] : (p4 == 0 ? s1 : (p4 == 1 ? s2 : 0.f)));
        flag_add(F_GRC, lane);
        wait_ge<false>(F_GRC, 8, dead);
        f32x4 gs = lds_ld<f32x4>(L_GRP + (2 * p4) * 256 + q4 * 64 + g * 16) + lds_ld<f32x4>(L_GRP + (2 * p4 + 1) * 256 + q4 * 64 + g * 16);
#pragma unroll
        for (int e = 0; e < 4; ++e) { gs[e] += dppf<QX1>(gs[e]); gs[e] += dppf<QX2>(gs[e]); }
        const float mu = dppf<NBC(12)>(gs[0]) * (1.f / 640.f);
        const float var = fmaxf(dppf<NBC(12)>(gs[1]) * (1.f / 640.f) - mu * mu, 0.f);
        const float rstd = 1.f / sqrtf(var + 1e-5f);
        float av[4];
#pragma unroll
        for (int e = 0; e < 4; ++e) av[e] = fmaxf(rstd * (dppf<NBC(0)>(gs[e]) - mu * dppf<NBC(4)>(gs[e])) + dppf<NBC(8)>(gs[e]), 0.f);
        u32x2 af; { const u32 h0 = pk2(av[0], av[1]), h1b = pk2(av[2], av[3]);
            const u32 l0 = pk2(av[0] - f_lo(h0), av[1] - f_hi(h0)), l1 = pk2(av[2] - f_lo(h1b), av[3] - f_hi(h1b));
            af[0] = (lane & 1) ? l0 : h0; af[1] = (lane & 1) ? l1 : h1b; }
        float fo[5];
#pragma unroll
        for (int u = 0; u < 5; ++u) { f32x4 y = {0.f, 0.f, 0.f, 0.f}; y = mfma16(af, b2[u], y); fo[u] = y[0] + y[1]; }
        {
            const float v = g == 0 ? fo[0] : (g == 1 ? fo[1] : (g == 2 ? fo[2] : fo[3]));
            const u32x4 o = pack_row(bf1(v)), o2 = pack_row(bf1(fo[4]));
            st128<SC1>(o, rWS, (lane & 7) == 0 ? W_FP + (((5 * w + g) * 96u + fb) * 16 + (lane & 8)) * 2u : OOB);
            st128<SC1>(o2, rWS, (lane & 55) == 0 ? W_FP + (((5 * w + 4) * 96u + fb) * 16 + (lane & 8)) * 2u : OOB);
        }
        if (w == 0) TS(15);
        const int ci = (r - 20) * 8 + x;
        if (ci < 40 && ci / 5 == w) {
            const u32 fo0 = W_FP + (u32)ci * 3072u + lane * 16;
            PSet<3> fv, qa, qb, qc;
            ppoll<3, STG2>(fv, qa, qb, qc,
                [&](PSet<3>& s) { s.v[0] = ld128<SC1>(rWS, fo0); s.v[1] = ld128<SC1>(rWS, fo0 + 1024); s.v[2] = ld128<SC1>(rWS, fo0 + 2048); },
                [&](const PSet<3>& s) { return none_sent(mx4(mx4(mx4(0, s.v[0]), s.v[1]), s.v[2])); }, dead);
            TS(16);
            float v[8];
#pragma unroll
            for (int j = 0; j < 4; ++j) { v[2 * j] = (f_lo(fv.v[0][j]) + f_lo(fv.v[1][j])) + f_lo(fv.v[2][j]); v[2 * j + 1] = (f_hi(fv.v[0][j]) + f_hi(fv.v[1][j])) + f_hi(fv.v[2][j]); }
            float t4[4], t2[2];
#pragma unroll
            for (int j = 0; j < 4; ++j) t4[j] = sc32(v[j], v[j + 4]);
#pragma unroll
            for (int j = 0; j < 2; ++j) t2[j] = sc16(t4[j], t4[j + 2]);
            const bool b3 = (lane & 8) != 0;
            const float keep = b3 ? t2[1] : t2[0], give = b3 ? t2[0] : t2[1];
            float f = keep + dppf<ROR(8)>(give);
            f += dppf<SHL(4)>(f);
            f += dppf<SHL(2)>(f);
            const u32 cc = 16 * (ci % 5) + 8 * (lane & 1) + (lane >> 3);
            const float o = lds_ld<float>(blk + B_X1 + cc * 4) + lds_ld<float>(blk + B_GB + cc * 4) * f;
            const rsrc_t rO = mkrs(p_out, M_ * 2);
            __builtin_amdgcn_raw_buffer_store_b16((unsigned short)bf1(o), rO, (lane & 6) == 0 ? (16 * ci + 8 * (lane & 1) + (lane >> 3)) * 2 : OOB, 0, 0);
            if (ci == 0 && lane == 0) { u32x2 e; e[0] = T1; e[1] = T2; st64<SC1>(e, rWS, W_EPOCH); }
            TS(17);
        }
    }
    TSDUMP();
}

extern "C" __device__ __attribute__((noinline, used)) void adaln_code_end() { asm volatile("s_nop 0"); }

extern "C" void kernel_launch(void* const* d_in, const int* in_sizes, int n_in, void* d_out, int out_size, void* d_ws, size_t ws_size, hipStream_t stream)
{
    (void)in_sizes; (void)n_in; (void)out_size; (void)ws_size;
    (void)hipFuncSetAttribute((const void*)adaln_mla_ffn_kernel, hipFuncAttributeMaxDynamicSharedMemorySize, (int)LDS_BYTES);
    hipLaunchKernelGGL(adaln_mla_ffn_kernel, dim3(256), dim3(640), LDS_BYTES, stream,
                       (const void*)d_in[0], (const void*)d_in[1], (const void*)d_in[2], (const void*)d_in[3], (const void*)d_in[4], (const void*)d_in[5],
                       (const void*)d_in[7], (const void*)d_in[8], (const void*)d_in[9], (const void*)d_in[10], d_out, d_ws);
}
```

```cpp
#include <hip/hip_runtime.h>
#include <hip/hip_bf16.h>
#include <stdint.h>

#define LAS __attribute__((address_space(3)))
#define DI __device__ __forceinline__
typedef unsigned int u32;
typedef unsigned long long u64;
typedef u32 u32x2 __attribute__((ext_vector_type(2)));
typedef u32 u32x3 __attribute__((ext_vector_type(3)));
typedef u32 u32x4 __attribute__((ext_vector_type(4)));
typedef float f32x2 __attribute__((ext_vector_type(2)));
typedef float f32x4 __attribute__((ext_vector_type(4)));
typedef short s16x4 __attribute__((ext_vector_type(4)));
typedef __bf16 bf16x2 __attribute__((ext_vector_type(2)));
typedef __bf16 bf16x8 __attribute__((ext_vector_type(8)));
typedef __amdgpu_buffer_rsrc_t rsrc_t;

#define WIN 18
#define WIN2 18
#define PAUSE 0
#define STG 6
#define STG2 3
#define NT_S0 0
#define NT_MR 2
#define NT_Q 1
#define NT_UK 1
#define NT_KV 1
#define NT_UV 1
#define NT_O 1
#define NT_W1 1
#define NT_W2 1
#define LPRIO 3
#define CPF 1
#define FEND "adaln_code_end"
#define KNAME "adaln_mla_ffn_kernel"
#define QDLY 40
#define ODLY 0
#define ELIGHT 1
#define MRAT 1
#define TH1 6
#define TH2 9
#define WINT 24
#define QLO 0
#define OGA 4
#define OGB 0
#define QMASK 1
#define LSLP 0
#define O_BASE 1048576u

constexpr int M_ = 640, H_ = 32, D_ = 192, DC = 768, SK = 8192, DFF = 1536;
constexpr u32 SENT = 0xFFFFFFFFu;
constexpr u32 OOB = 0x80000000u;

constexpr u32 W_EPOCH = 0;
constexpr u32 W_FA = 256;
constexpr u32 W_G1 = 4096;
constexpr u32 W_Q2 = 32768;
constexpr u32 W_SMALL = W_Q2 + 98304;
constexpr u32 W_QA = W_SMALL;
constexpr u32 W_CTX = W_QA + 98304;
constexpr u32 W_AT = W_CTX + 98304;
constexpr u32 W_FP = W_AT + 10240;
constexpr u32 W_MR = W_FP + 122880;
constexpr u32 W_SMALL_END = W_MR + 20480;
constexpr u32 SMALL_CH = (W_SMALL_END - W_SMALL) / 16;
constexpr u32 SMALL_PER = (SMALL_CH + 255) / 256;
constexpr u32 W_O = O_BASE;
constexpr u32 W_PK = W_O + 8u * 128 * 2 * 3072;
constexpr u32 O_BYTES = 8u * 128 * 2 * 3072 + 8u * 128 * 32 * 16;
constexpr u32 W_PROF = W_SMALL_END;
constexpr u32 W_END = W_O + O_BYTES;
static_assert(W_PROF + 256 * 64 * 8 <= W_O, "ws map");
static_assert(SMALL_PER <= 128, "prefill");
static_assert(W_END <= 268435456u, "ws map");

constexpr u32 L_A = 0;
constexpr u32 BLK = 12288;
constexpr u32 L_S1W = 5 * BLK;
constexpr u32 L_X = 98304;
constexpr u32 L_Y = 135168;
constexpr u32 L_FLG = L_Y;
constexpr u32 L_S0P = L_Y + 13568;
constexpr u32 L_LAND = L_Y + 14336;
constexpr u32 L_EML = L_Y + 448;
constexpr u32 L_FRP = L_Y + 512;
constexpr u32 L_GRP = L_Y + 1536;
constexpr u32 L_SC = L_Y + 3584;
constexpr u32 L_HX = L_Y + 7680;
constexpr u32 L_QH = L_Y + 8448;
constexpr u32 L_AM = L_Y + 9216;
constexpr u32 L_SIL = L_Y + 12288;
constexpr u32 L_XN = L_Y + 17408;
constexpr u32 LDS_BYTES = 163840;
constexpr u32 B_W = 4096, B_W2 = 7168, B_GA = 10240, B_X1 = 11200, B_GB = 11520;

enum { F_LAND = 0, F_RDYA, F_S0CNT, F_S1DONE, F_HX, F_QH, F_S2DONE, F_BLK, F_QAOK, F_SADD, F_SC, F_AM, F_FRC, F_GRC, F_QAST, F_XN };

constexpr u32 CAP_G = 20000;
constexpr u32 CAP_L = 400000;

constexpr u32 T_S1 = 30, T_S2 = 66, T_TILE0 = 66, T_UV = 198;

DI rsrc_t mkrs(const void* p, u32 bytes) { return __builtin_amdgcn_make_buffer_rsrc((void*)p, (short)0, (int)bytes, 0x00020000); }
template <int AUX> DI u32x4 ld128(rsrc_t r, u32 off) { return __builtin_amdgcn_raw_buffer_load_b128(r, off, 0, AUX); }
template <int AUX> DI u32x3 ld96(rsrc_t r, u32 off) { return __builtin_amdgcn_raw_buffer_load_b96(r, off, 0, AUX); }
template <int AUX> DI u32x2 ld64(rsrc_t r, u32 off) { return __builtin_amdgcn_raw_buffer_load_b64(r, off, 0, AUX); }
template <int AUX> DI u32 ld32(rsrc_t r, u32 off) { return __builtin_amdgcn_raw_buffer_load_b32(r, off, 0, AUX); }
DI u32 ld16(rsrc_t r, u32 off) { return (u32)__builtin_amdgcn_raw_buffer_load_b16(r, off, 0, 0); }
template <int AUX> DI void st128(u32x4 v, rsrc_t r, u32 off) { __builtin_amdgcn_raw_buffer_store_b128(v, r, off, 0, AUX); }
template <int AUX> DI void st96(u32x3 v, rsrc_t r, u32 off) { __builtin_amdgcn_raw_buffer_store_b96(v, r, off, 0, AUX); }
template <int AUX> DI void st64(u32x2 v, rsrc_t r, u32 off) { __builtin_amdgcn_raw_buffer_store_b64(v, r, off, 0, AUX); }
template <int AUX> DI void st32(u32 v, rsrc_t r, u32 off) { __builtin_amdgcn_raw_buffer_store_b32(v, r, off, 0, AUX); }
constexpr int SC1 = 16, NT = 2;

DI float f_lo(u32 v) { return __uint_as_float(v << 16); }
DI float f_hi(u32 v) { return __uint_as_float(v & 0xFFFF0000u); }
DI u32 pk2(float a, float b) { bf16x2 v; v[0] = (__bf16)a; v[1] = (__bf16)b; return __builtin_bit_cast(u32, v); }
DI u32 bf1(float a) { return pk2(a, 0.f) & 0xFFFFu; }
DI u32 san(u32 v) { return v < 0xFFFFFFFEu ? v : 0xFFFFFFFEu; }
DI u32x4 san4(u32x4 v) { u32x4 r; r[0] = san(v[0]); r[1] = san(v[1]); r[2] = san(v[2]); r[3] = san(v[3]); return r; }
DI u32 umax(u32 a, u32 b) { return a > b ? a : b; }
DI u32 mx4(u32 acc, u32x4 v) { return umax(umax(acc, umax(v[0], v[1])), umax(v[2], v[3])); }

DI u32 tagw(float v, u32 tg) { return ((__float_as_uint(v) + 0x800u) & 0xFFFFF000u) | tg; }
DI float untag(u32 v) { return __uint_as_float(v & 0xFFFFF000u); }
DI bool tag_ok(u32x4 v, u32 tg) { return ((((v[0] ^ tg) | (v[1] ^ tg)) | ((v[2] ^ tg) | (v[3] ^ tg))) & 0xFFFu) == 0u; }
DI float dot2(u32 a, u32 b, float c) { return __builtin_amdgcn_fdot2_f32_bf16(__builtin_bit_cast(bf16x2, a), __builtin_bit_cast(bf16x2, b), c, false); }
template <int CTRL> DI u32 dppu(u32 v) { return (u32)__builtin_amdgcn_update_dpp(0, (int)v, CTRL, 0xF, 0xF, true); }
template <int CTRL> DI float dppf(float v) { return __uint_as_float(dppu<CTRL>(__float_as_uint(v))); }
constexpr int ROR(int n) { return 0x120 + n; }
constexpr int SHL(int n) { return 0x100 + n; }
constexpr int NBC(int n) { return 0x150 + n; }
constexpr int QX1 = 0xB1, QX2 = 0x4E;
DI float row_sum(float v) { v += dppf<ROR(8)>(v); v += dppf<ROR(4)>(v); v += dppf<ROR(2)>(v); v += dppf<ROR(1)>(v); return v; }
DI u32 row_xor(u32 v) { v ^= dppu<ROR(8)>(v); v ^= dppu<ROR(4)>(v); v ^= dppu<ROR(2)>(v); v ^= dppu<ROR(1)>(v); return v; }
DI u32 row_add(u32 v) { v += dppu<ROR(8)>(v); v += dppu<ROR(4)>(v); v += dppu<ROR(2)>(v); v += dppu<ROR(1)>(v); return v; }
DI void swap16(float& a, float& b) { auto r = __builtin_amdgcn_permlane16_swap(__float_as_uint(a), __float_as_uint(b), false, false); a = __uint_as_float(r[0]); b = __uint_as_float(r[1]); }
DI void swap32(float& a, float& b) { auto r = __builtin_amdgcn_permlane32_swap(__float_as_uint(a), __float_as_uint(b), false, false); a = __uint_as_float(r[0]); b = __uint_as_float(r[1]); }
DI float xg_sum(float v) { float a = v, b = v; swap16(a, b); v = a + b; a = v; b = v; swap32(a, b); return a + b; }
DI float xg_max(float v) { float a = v, b = v; swap16(a, b); v = fmaxf(a, b); a = v; b = v; swap32(a, b); return fmaxf(a, b); }
DI u32 xg_xor(u32 v) {
    auto r = __builtin_amdgcn_permlane16_swap(v, v, false, false); v = r[0] ^ r[1];
    auto q = __builtin_amdgcn_permlane32_swap(v, v, false, false); return q[0] ^ q[1];
}
DI float wave_sum(float v) { return xg_sum(row_sum(v)); }
DI float sc32(float a, float b) { swap32(a, b); return a + b; }
DI float sc16(float a, float b) { swap16(a, b); return a + b; }
DI u32 sel4(int e, u32 a, u32 b, u32 c, u32 d) { const u32 lo = (e & 1) ? b : a, hi = (e & 1) ? d : c; return (e & 2) ? hi : lo; }

DI f32x4 mfma32(u32x4 a, u32x4 b, f32x4 c) { return __builtin_amdgcn_mfma_f32_16x16x32_bf16(__builtin_bit_cast(bf16x8, a), __builtin_bit_cast(bf16x8, b), c, 0, 0, 0); }
DI f32x4 mfma16(u32x2 a, u32x2 b, f32x4 c) { return __builtin_amdgcn_mfma_f32_16x16x16bf16_1k(__builtin_bit_cast(s16x4, a), __builtin_bit_cast(s16x4, b), c, 0, 0, 0); }
DI u32x2 trd(u32 addr) { return __builtin_bit_cast(u32x2, __builtin_amdgcn_ds_read_tr16_b64_v4i16((LAS s16x4*)addr)); }
DI u32x4 cat(u32x2 a, u32x2 b) { u32x4 r; r[0] = a[0]; r[1] = a[1]; r[2] = b[0]; r[3] = b[1]; return r; }
template <class T> DI T lds_ld(u32 addr) { return *(LAS T*)addr; }
template <class T> DI void lds_st(u32 addr, T v) { *(LAS T*)addr = v; }
#define CBAR() asm volatile("" ::: "memory")

DI u32 flag_ld(int i) { return (u32)__builtin_amdgcn_readfirstlane((int)*(volatile LAS u32*)(i == F_LAND ? L_LAND : L_FLG + 4 * i)); }
DI void flag_st(int i, u32 v, int lane) { CBAR(); if (lane == 0) *(volatile LAS u32*)(L_FLG + 4 * i) = v; CBAR(); }
DI void flag_add(int i, int lane) { CBAR(); if (lane == 0) __hip_atomic_fetch_add((LAS u32*)(L_FLG + 4 * i), 1u, __ATOMIC_RELAXED, __HIP_MEMORY_SCOPE_WORKGROUP); CBAR(); }
DI u32 flag_add_ret(int i, int lane) {
    CBAR(); u32 o = 0;
    if (lane == 0) o = __hip_atomic_fetch_add((LAS u32*)(L_FLG + 4 * i), 1u, __ATOMIC_RELAXED, __HIP_MEMORY_SCOPE_WORKGROUP);
    o = (u32)__builtin_amdgcn_readfirstlane((int)o); CBAR(); return o;
}
template <bool SLEEP> DI void wait_ge(int i, u32 target, bool& dead) {
    u32 n = 0;
    while (!dead && flag_ld(i) < target) { if (++n > CAP_L) dead = true; if (SLEEP || LSLP) __builtin_amdgcn_s_sleep(1); }
    CBAR();
}
DI bool all_ok(bool ok) { return __builtin_amdgcn_ballot_w64(!ok) == 0ull; }
DI bool none_sent(u32 mx) { return __builtin_amdgcn_ballot_w64(mx == SENT) == 0ull; }
#define POLL_STEP() { if (++spins > CAP_G) { dead = true; break; } __builtin_amdgcn_s_sleep(1); }

template <int N> struct PSet { u32x4 v[N]; };
template <int N> DI void take(PSet<N>& res, const PSet<N>& s) {
#pragma unroll
    for (int k = 0; k < N; ++k) res.v[k] = s.v[k];
}
#define PSLP 0

template <int N, int STAGGER, class LD, class OK>
DI void ppoll(PSet<N>& res, PSet<N>& a, PSet<N>& b, PSet<N>& c, LD ld, OK ok, bool& dead) {
    (void)a; (void)b; (void)c;
    for (u32 spins = 0;;) {
        CBAR(); ld(res); CBAR();
        if (ok(res)) break;
        if (++spins > CAP_G) { dead = true; break; }
        if (PSLP) __builtin_amdgcn_s_sleep(PSLP);
    }
    CBAR();
}
template <int N> DI void retire(PSet<N>&, PSet<N>&, PSet<N>&) {}

#define TS(i) do { } while (0)
#define TCAP(v) do { } while (0)
#define TSV(i, t_) do { } while (0)
#define TSDUMP() do { } while (0)

DI u32x4 pack_row(u32 v16) {
    u32 pr = v16 | (dppu<SHL(1)>(v16) << 16);
    u32x4 r; r[0] = pr; r[1] = dppu<SHL(2)>(pr); r[2] = dppu<SHL(4)>(pr); r[3] = dppu<SHL(6)>(pr);
    return san4(r);
}

template <int NTL> DI void dma3(u32 lds, u32 v0, u32 v1, u32 v2, rsrc_t rs, u32 soff) {
    if constexpr (NTL) asm volatile("s_mov_b32 m0, %0\n\ts_nop 3\n\tbuffer_load_dwordx4 %1, %4, %5 offen nt lds\n\tbuffer_load_dwordx4 %2, %4, %5 offen offset:1024 nt lds\n\t"
                                    "buffer_load_dwordx4 %3, %4, %5 offen offset:2048 nt lds" ::"s"(lds), "v"(v0), "v"(v1), "v"(v2), "s"(rs), "s"(soff) : "memory");
    else asm volatile("s_mov_b32 m0, %0\n\ts_nop 3\n\tbuffer_load_dwordx4 %1, %4, %5 offen lds\n\tbuffer_load_dwordx4 %2, %4, %5 offen offset:1024 lds\n\t"
                      "buffer_load_dwordx4 %3, %4, %5 offen offset:2048 lds" ::"s"(lds), "v"(v0), "v"(v1), "v"(v2), "s"(rs), "s"(soff) : "memory");
}
template <int N> DI void waitvm() { asm volatile("s_waitcnt vmcnt(%0)" ::"i"(N) : "memory"); }
constexpr int WM1 = WIN > WIN2 ? WIN : WIN2, WMAX = WM1 > WINT ? WM1 : WINT;
struct Ldr { int issued; int pub; u32 ladr; bool dead; };
DI void publish(Ldr& s, int landed) { s.pub = landed > s.pub ? landed : s.pub; CBAR(); *(volatile LAS u32*)s.ladr = (u32)s.pub; CBAR(); }
template <int W> DI void step3(Ldr& s) { s.issued += 3; waitvm<W>(); publish(s, s.issued - W); }
constexpr u32 QHT = 2;
template <int W, int T1, int T2> DI void step3f(Ldr& s) {
    s.issued += 3;
    CBAR();
    const u32x2 f = *(volatile LAS u32x2*)(L_FLG + 4 * F_HX);
    const u32 hx = (u32)__builtin_amdgcn_readfirstlane((int)f[0]), qh = (u32)__builtin_amdgcn_readfirstlane((int)f[1]);
    if (qh >= QHT) { waitvm<W>(); publish(s, s.issued - W); }
    else if (hx >= 5) { waitvm<T2>(); publish(s, s.issued - T2); }
    else { waitvm<T1>(); publish(s, s.issued - T1); }
}
template <int N> DI void drain(Ldr& s) { waitvm<N>(); publish(s, s.issued - N); if constexpr (N >= 3) drain<N - 3>(s); }
template <int W> DI void park(Ldr& s, int flag, u32 target) {
    if (flag_ld(flag) < target) { drain<W - 3>(s); wait_ge<true>(flag, target, s.dead); }
    CBAR();
}
template <int W, int NTL> DI void seg3(Ldr& s, u32 lds0, const u32 (&v)[3], rsrc_t rs, u32 soff0, u32 sstep, int ng) {
    u32 so = soff0, ld = lds0;
#pragma unroll 1
    for (int g = 0; g < ng; ++g) {
        dma3<NTL>(ld, v[0], v[1], v[2], rs, so);
        so += sstep; ld += 3072u;
        step3<W>(s);
    }
}

extern __shared__ __attribute__((aligned(16))) char smem_dyn[];

extern "C" __global__ void __launch_bounds__(640, 3)
adaln_mla_ffn_kernel(const void* __restrict__ p_x, const void* __restrict__ p_ckv, const void* __restrict__ p_wq, const void* __restrict__ p_wuk,
                     const void* __restrict__ p_wuv, const void* __restrict__ p_wo, const void* __restrict__ p_w1, const void* __restrict__ p_w2,
                     const void* __restrict__ p_c, const void* __restrict__ p_wmod, void* __restrict__ p_out, void* __restrict__ p_ws)
{
    const int tid = threadIdx.x, lane = tid & 63;
    const int wave = __builtin_amdgcn_readfirstlane(tid >> 6);
    const int b = blockIdx.x, x = b & 7, r = b >> 3;
    const bool typeA = r < 20;
    const int g = lane >> 4, i16 = lane & 15, q4 = i16 >> 2, p4 = lane & 3;
    bool dead = false;
    (void)smem_dyn;
    asm volatile("" ::"s"(p_x), "s"(p_ckv), "s"(p_wq), "s"(p_wuk), "s"(p_wuv), "s"(p_wo), "s"(p_w1), "s"(p_w2), "s"(p_c), "s"(p_wmod), "s"(p_out), "s"(p_ws));
    const rsrc_t rWS = mkrs(p_ws, W_END);
    const rsrc_t rX = mkrs(p_x, M_ * 2);
    const bool doS0 = typeA && wave < 5;
    const u32 cg = (x & 1) * 20 + r, kp = x >> 1;
    u32x4 m0v, m1v, xa; u32 cv = 0, xb = 0; u32x2 ep;
    if (doS0) {
        const rsrc_t rc = mkrs(p_c, M_ * 2), rm = mkrs(p_wmod, M_ * 6 * M_ * 2);
        const u32 row0 = 160 * kp + 32 * wave + 2 * (lane >> 2);
        const u32 off = row0 * 7680u + cg * 64u + (lane & 3) * 16u;
        m0v = ld128<NT_S0>(rm, off); m1v = ld128<NT_S0>(rm, off + 7680u);
        cv = ld32<0>(rc, row0 * 2);
    }
    if (wave == 5) { xa = ld128<0>(rX, lane * 16); xb = ld32<0>(rX, 1024 + lane * 4); }
    ep[0] = 0; ep[1] = 0;
    if (lane == 0 && wave != 8) ep = ld64<SC1>(rWS, W_EPOCH);
    if (tid >= 384 && tid < 401) { u32x4 z; z[0] = 0; z[1] = 0; z[2] = 0; z[3] = 0; lds_st<u32x4>(tid < 400 ? L_FLG + (tid - 384) * 16 : L_LAND, z); }
    asm volatile("s_waitcnt lgkmcnt(0)\n\ts_barrier" ::: "memory");
    TCAP(tbw);

    if (wave == 8) {
        Ldr s; s.issued = 0; s.pub = 0; s.ladr = L_LAND + 4 * lane; s.dead = false;
        __builtin_amdgcn_s_setprio(LPRIO);
        u32 v1[3], vt[3], vu[3];
#pragma unroll
        for (int k = 0; k < 3; ++k) {
            const u32 p = 64 * k + lane;
            const u32 row = p / 12, ch = (p % 12) ^ (((row >> 2) & 1) << 1);
            v1[k] = row * 12288u + ch * 16 - 1024u * k; vt[k] = row * 1536u + ch * 16 - 1024u * k;
            const u32 r2 = p / 24, c2 = (p % 24) ^ (((r2 >> 1) & 3) << 1);
            vu[k] = r2 * 12288u + c2 * 16 - 1024u * k;
        }
        const rsrc_t rq = mkrs(p_wq, M_ * H_ * D_ * 2), ruk = mkrs(p_wuk, DC * H_ * D_ * 2), ruv = mkrs(p_wuv, DC * H_ * D_ * 2), rkv = mkrs(p_ckv, SK * DC * 2);
        const u32 ks = r >> 3, h1 = 4 * x + ((r >> 1) & 3), half = r & 1;
        const u32 so1 = 160u * ks * 12288u + (h1 * 192 + 96 * half) * 2;
        TCAP(tb8);
        seg3<WIN, NT_Q>(s, L_S1W, v1, rq, so1, 16u * 12288u, 10);
        TS(44); TSV(51, t_entry); TSV(54, tb8);
        const u32 so2 = 96u * x * 12288u + (u32)r * 384u;
#pragma unroll 1
        for (int gi = 0; gi < 12; ++gi) {
            dma3<NT_UK>(L_X + 3072u * gi, vu[0], vu[1], vu[2], ruk, so2 + 8u * 12288u * gi);
            step3f<WINT, TH1, TH2>(s);
        }
        TS(45);
        const u32 jt = x + 8 * (r & 15);
        const u32 sot = 64u * jt * 1536u;
#pragma unroll 1
        for (int gi = 0; gi < 32; ++gi) {
            if (gi == 20) park<WMAX>(s, F_S1DONE, 6);
            dma3<NT_KV>(L_A + BLK * (gi >> 2) + 3072u * (gi & 3), vt[0], vt[1], vt[2], rkv, sot + 192u * (gi >> 2) + 16u * 1536u * (gi & 3));
            step3f<WINT, TH1, TH2>(s);
        }
        TS(46);
        if (PAUSE) { park<WMAX>(s, F_QAOK, 8); TS(47); }
        park<WMAX>(s, F_S2DONE, 6);
        seg3<WIN2, NT_UV>(s, L_X, vu, ruv, so2, 8u * 12288u, 12);
        TS(48);
        park<WMAX>(s, F_BLK, 8);
        if (typeA) {
            const rsrc_t ro = mkrs(p_wo, H_ * D_ * M_ * 2);
            const u32 row = lane >> 2, ch = (lane & 3) ^ (((row >> 3) & 1) << 1);
            const u32 vf = row * 1280u + ch * 16;
            const u32 vo[3] = {vf, vf + 16u * 1280u - 1024u, vf + 32u * 1280u - 2048u};
#pragma unroll 1
            for (int w = 0; w < 8; ++w) {
                if (w == OGA) { park<WMAX>(s, F_AM, 8); TS(59); }
                seg3<WIN2, NT_O>(s, L_A + BLK * w + B_W, vo, ro, (768u * x + 96u * w) * 1280u + 64u * r, 48u * 1280u, 2);
            }
        } else {
            const rsrc_t r1 = mkrs(p_w1, M_ * DFF * 2), r2 = mkrs(p_w2, DFF * M_ * 2);
            const u32 fb = x * 12 + (r - 20);
            const u32 va = (lane >> 1) * 3072u + (lane & 1) * 16;
            const u32 v1a[3] = {va, va + 32u * 3072u - 1024u, lane < 32 ? va + 64u * 3072u - 2048u : OOB};
            u32 vb[3];
#pragma unroll
            for (int k = 0; k < 3; ++k) { const u32 p = 64 * k + lane; vb[k] = p < 160 ? (p / 10) * 1280u + (p % 10) * 16 - 1024u * k : OOB; }
#pragma unroll 1
            for (int w = 0; w < 8; ++w) {
                const u32 ld = L_A + BLK * w;
                if (w == OGB) { park<WMAX>(s, F_AM, 8); TS(59); }
                dma3<NT_W1>(ld + B_W, v1a[0], v1a[1], v1a[2], r1, 80u * w * 3072u + fb * 32u);
                step3<WIN2>(s);
                dma3<NT_W2>(ld + B_W2, vb[0], vb[1], vb[2], r2, 16u * fb * 1280u + 160u * w);
                step3<WIN2>(s);
            }
        }
        TS(49);
        drain<WMAX - 3>(s);
        TS(50);
        TSDUMP();
        return;
    }

    if (wave == 9) {
        u32x4 sv; sv[0] = SENT; sv[1] = SENT; sv[2] = SENT; sv[3] = SENT;
        {
            const u32 c0 = (u32)b * SMALL_PER + lane, c1 = c0 + 64;
            st128<SC1>(sv, rWS, c0 < SMALL_CH ? W_SMALL + c0 * 16 : OOB);
            st128<SC1>(sv, rWS, (lane + 64 < (int)SMALL_PER && c1 < SMALL_CH) ? W_SMALL + c1 * 16 : OOB);
        }
        {
            u64 pc; u32 before, len;
            asm volatile("s_getpc_b64 %0\n.Lpf%=:\n\ts_mov_b32 %1, .Lpf%=-" KNAME "\n\ts_mov_b32 %2, " FEND "-" KNAME : "=s"(pc), "=s"(before), "=s"(len));
            const rsrc_t rcode = mkrs((const void*)(pc - before), len < 65536u ? len : 0u);
            const u32x4 c0 = ld128<0>(rcode, (u32)r * 2048u + lane * 16u), c1 = ld128<0>(rcode, (u32)r * 2048u + 1024u + lane * 16u);
            asm volatile("" ::"v"(c0), "v"(c1));
        }
        const int it = 255 - b;
        const rsrc_t rc = mkrs(p_c, M_ * 2), rm = mkrs(p_wmod, M_ * 6 * M_ * 2);
        u32 cs[5];
        if (it < 160) {
#pragma unroll
            for (int k = 0; k < 5; ++k) cs[k] = ld16(rc, (320 * (it & 1) + 64 * k + lane) * 2);
        }
        TCAP(tb9);
        waitvm<0>();
        TSV(52, t_entry); TSV(55, tb9);
        const u64 tag = (((u64)(u32)__builtin_amdgcn_readfirstlane((int)ep[1]) << 32) | (u32)__builtin_amdgcn_readfirstlane((int)ep[0])) + 1ull;
        u32x2 tg; tg[0] = (u32)tag; tg[1] = (u32)(tag >> 32);
        if (lane == 0) st64<SC1>(tg, rWS, W_FA + 8 * b);
        TS(39);
        wait_ge<true>(F_HX, 5, dead);
        {
            u32 spins = 0;
            for (;;) {
                CBAR();
                const u32x4 a = ld128<SC1>(rWS, W_FA + lane * 16), c = ld128<SC1>(rWS, W_FA + 1024 + lane * 16);
                const bool ok = a[0] == tg[0] && a[1] == tg[1] && a[2] == tg[0] && a[3] == tg[1] && c[0] == tg[0] && c[1] == tg[1] && c[2] == tg[0] && c[3] == tg[1];
                if (all_ok(ok)) break;
                POLL_STEP();
            }
            flag_st(F_RDYA, 1, lane);
            TS(40);
        }
        if (it < 160) {
            const int cb = it >> 1, kh = it & 1;
#pragma unroll
            for (int k = 0; k < 5; ++k) {
                const float cv = __uint_as_float(cs[k] << 16);
                lds_st<float>(L_SIL + (64 * k + lane) * 4, cv * __builtin_amdgcn_rcpf(1.f + __expf(-cv)));
            }
            if (MRAT) wait_ge<true>(F_LAND, T_TILE0 + 96, dead); else wait_ge<true>(F_QAOK, 8, dead);
            TS(42);
            u32x4 wv[20];
            const u32 vo = (u32)(320 * kh + (lane >> 2)) * 7680u + (1280u + 32u * cb) * 2u + (lane & 3) * 16u;
#pragma unroll
            for (int k = 0; k < 20; ++k) wv[k] = ld128<NT_MR>(rm, vo + (u32)k * 16u * 7680u);
            float acc[8];
#pragma unroll
            for (int j = 0; j < 8; ++j) acc[j] = 0.f;
#pragma unroll
            for (int k = 0; k < 20; ++k) {
                const float sl = lds_ld<float>(L_SIL + (16 * k + (lane >> 2)) * 4);
#pragma unroll
                for (int j = 0; j < 4; ++j) { acc[2 * j] += sl * f_lo(wv[k][j]); acc[2 * j + 1] += sl * f_hi(wv[k][j]); }
            }
            float t4[4], t2[2];
#pragma unroll
            for (int j = 0; j < 4; ++j) t4[j] = sc32(acc[j], acc[j + 4]);
#pragma unroll
            for (int j = 0; j < 2; ++j) t2[j] = sc16(t4[j], t4[j + 2]);
#pragma unroll
            for (int j = 0; j < 2; ++j) { t2[j] += dppf<ROR(8)>(t2[j]); t2[j] += dppf<ROR(4)>(t2[j]); }
            const u32 pm = cb / 20;
#pragma unroll
            for (int j = 0; j < 2; ++j) {
                const u32 col = (cb % 20) * 32u + 8 * (lane & 3) + 4 * (lane >> 5) + 2 * ((lane >> 4) & 1) + j;
                const u32 cw = col / 80, cc = col % 80;
                const u32 pos = cc < 64 ? (cc & 7) * 8 + (cc >> 3) : 64 + (cc & 7) * 2 + ((cc >> 3) & 1);
                st32<SC1>(san(__float_as_uint(t2[j])), rWS, (lane & 12) == 0 ? W_MR + ((kh * 8u + cw) * 80u + pos) * 16u + pm * 4u : OOB);
            }
            TS(43);
        }
        TSDUMP();
        return;
    }

    const int w = wave;
    const u32 ks = r >> 3;
    const bool front = w < 5;
    if (w == 0) TSV(53, tbw);
    if (w == 5) TSV(56, tbw);
    if (w == 7) { TSV(57, tbw); TSV(58, t_entry); }
    if (w == 0) TSV(30, t_entry);
    if (w == 0) TS(0);
    const u64 tag = (((u64)(u32)__builtin_amdgcn_readfirstlane((int)ep[1]) << 32) | (u32)__builtin_amdgcn_readfirstlane((int)ep[0])) + 1ull;
    const u32 T1 = (u32)tag, T2 = (u32)(tag >> 32);
    const u32 tagl = T1 & 0xFFu, tg12 = T1 & 0xFFFu;

    if (doS0) {
        const float cA = f_lo(cv), cB = f_hi(cv);
        const float sA = cA * __builtin_amdgcn_rcpf(1.f + __expf(-cA)), sB = cB * __builtin_amdgcn_rcpf(1.f + __expf(-cB));
        const u32 sph = pk2(sA, sB), spl = pk2(sA - f_lo(sph), sB - f_hi(sph));
        float acc[8];
#pragma unroll
        for (int j = 0; j < 4; ++j) {
            const u32 pa = __builtin_amdgcn_perm(m1v[j], m0v[j], 0x05040100u), pb = __builtin_amdgcn_perm(m1v[j], m0v[j], 0x07060302u);
            acc[2 * j] = dot2(pa, spl, dot2(pa, sph, 0.f));
            acc[2 * j + 1] = dot2(pb, spl, dot2(pb, sph, 0.f));
        }
        if (w == 0) TS(31);
        float t4[4]; f32x2 t2;
#pragma unroll
        for (int j = 0; j < 4; ++j) t4[j] = sc32(acc[j], acc[j + 4]);
#pragma unroll
        for (int j = 0; j < 2; ++j) { float t = sc16(t4[j], t4[j + 2]); t += dppf<ROR(8)>(t); t += dppf<ROR(4)>(t); t2[j] = t; }
        if ((lane & 12) == 0) lds_st<f32x2>(L_S0P + (w * 32 + 8 * (lane & 3) + 4 * (lane >> 5) + 2 * ((lane >> 4) & 1)) * 4, t2);
        if (flag_add_ret(F_S0CNT, lane) == 4) {
            f32x4 v = lds_ld<f32x4>(L_S0P + (lane & 7) * 16);
#pragma unroll
            for (int p = 1; p < 5; ++p) v += lds_ld<f32x4>(L_S0P + p * 128 + (lane & 7) * 16);
            u32x4 o;
#pragma unroll
            for (int j = 0; j < 4; ++j) o[j] = tagw(v[j], tg12);
            const u32 wh = cg >= 20 ? 1u : 0u, m0 = 32 * (cg - 20 * wh);
            st128<SC1>(o, rWS, lane < 8 ? W_G1 + ((wh * 4 + kp) * 640u + m0 + 4 * lane) * 4u : OOB);
            TS(1);
        }
    }

    const u32 h1 = 4 * x + ((r >> 1) & 3), half = r & 1;
    u32x4 bw[5];
    auto read_bw = [&]() {
        wait_ge<true>(F_LAND, T_S1, dead);
        const u32 ab = L_S1W + (4 * g + q4) * 192 + (((2 * w + (p4 >> 1)) ^ ((g & 1) << 1)) * 16) + 8 * (p4 & 1);
#pragma unroll
        for (int t = 0; t < 5; ++t) bw[t] = cat(trd(ab + (32 * t) * 192), trd(ab + (32 * t + 16) * 192));
        asm volatile("s_waitcnt lgkmcnt(0)" ::: "memory");
        flag_add(F_S1DONE, lane);
        if (w == 0) TS(32);
    };
    if (w == 5) {
        float xv[10];
#pragma unroll
        for (int k = 0; k < 4; ++k) { xv[2 * k] = f_lo(xa[k]); xv[2 * k + 1] = f_hi(xa[k]); }
        xv[8] = f_lo(xb); xv[9] = f_hi(xb);
        float sum = 0.f;
#pragma unroll
        for (int k = 0; k < 10; ++k) sum += xv[k];
        const float mu = wave_sum(sum) * (1.f / 640.f);
        float sq = 0.f;
#pragma unroll
        for (int k = 0; k < 10; ++k) { xv[k] -= mu; sq += xv[k] * xv[k]; }
        const float rstd = 1.f / sqrtf(wave_sum(sq) * (1.f / 640.f) + 1e-5f);
        f32x4 n0, n1; f32x2 n2;
#pragma unroll
        for (int k = 0; k < 4; ++k) { n0[k] = xv[k] * rstd; n1[k] = xv[4 + k] * rstd; }
        n2[0] = xv[8] * rstd; n2[1] = xv[9] * rstd;
        lds_st<f32x4>(L_XN + lane * 32, n0); lds_st<f32x4>(L_XN + lane * 32 + 16, n1); lds_st<f32x2>(L_XN + 2048 + lane * 8, n2);
        flag_add(F_XN, lane);
        read_bw();
    }
    if (front) {
        read_bw();
        wait_ge<true>(F_XN, 1, dead);
        const f32x4 xn4 = lds_ld<f32x4>(L_XN + (160 * ks + 32 * w + 4 * (lane & 7)) * 4);
        float aj[4], bj[4];
#pragma unroll
        for (int j = 0; j < 4; ++j) { aj[j] = lane >= 32 ? xn4[j] : 1.f; bj[j] = lane < 8 ? xn4[j] : 0.f; }
        const u32 goff = W_G1 + ((u32)(lane >> 3) * 640u + 160 * ks + 32 * w + 4 * (lane & 7)) * 4u;
        const u32 hadr = L_HX + ((lane >> 3) & 1) * 320 + (32 * w + 8 * (lane & 3) + 4 * ((lane >> 2) & 1) + 2 * (lane >> 5) + ((lane >> 4) & 1)) * 2;
        PSet<1> gv, pa, pb, pc;
        ppoll<1, STG>(gv, pa, pb, pc,
            [&](PSet<1>& s) { s.v[0] = ld128<SC1>(rWS, goff); },
            [&](const PSet<1>& s) { return all_ok(tag_ok(s.v[0], tg12)); }, dead);
        if (w == 4) TS(2);
        float t[4];
#pragma unroll
        for (int j = 0; j < 4; ++j) t[j] = aj[j] * untag(gv.v[0][j]) + bj[j];
        float hv = sc16(sc32(t[0], t[2]), sc32(t[1], t[3]));
        hv += dppf<ROR(8)>(hv);
        const u32 hi = bf1(hv), lo = bf1(hv - __uint_as_float(hi << 16));
        lds_st<unsigned short>(hadr, (unsigned short)((lane & 8) ? lo : hi));
        flag_add(F_HX, lane);
        retire(pa, pb, pc);
    }
    u32x4 bw2[6];
    if (w < 6) {
        wait_ge<false>(F_HX, 5, dead);
        f32x4 acc = {0.f, 0.f, 0.f, 0.f};
#pragma unroll
        for (int t = 0; t < 5; ++t) acc = mfma32(lds_ld<u32x4>(L_HX + (lane & 1) * 320 + 64 * t + 16 * g), bw[t], acc);
        const u32 qb = tagw(acc[0] + acc[1], tg12);
        u32x4 o; o[0] = qb; o[1] = dppu<SHL(1)>(qb); o[2] = dppu<SHL(2)>(qb); o[3] = dppu<SHL(3)>(qb);
        st128<SC1>(o, rWS, (lane & 51) == 0 ? W_Q2 + (ks * 6144u + h1 * 192 + 96 * half + 16 * w + lane) * 4u : OOB);
        if (w == 0) TS(3);
        wait_ge<true>(F_LAND, T_S2, dead);
        const u32 rowb = L_X + (16 * w + i16) * 384, sw = ((i16 >> 1) & 3) << 1;
#pragma unroll
        for (int t = 0; t < 6; ++t) bw2[t] = lds_ld<u32x4>(rowb + (((4 * t + g) ^ sw) * 16));
        flag_add(F_S2DONE, lane);
        if (w == 0) TS(33);
    }
    if (w >= 6) {
        wait_ge<true>(F_HX, 5, dead);
        const u32 c0 = 128 * (w - 6) + 4 * i16;
        const u32 qo = W_Q2 + ((u32)g * 6144u + (u32)r * 192u + c0) * 4u, qo2 = w == 6 ? qo + 256 : qo;
        const u32 qadr = L_QH + (c0 + 2 * (lane >> 5) + ((lane >> 4) & 1)) * 2;
        PSet<2> qv1, pa, pb, pc;
        ppoll<2, STG>(qv1, pa, pb, pc,
            [&](PSet<2>& s) { s.v[0] = ld128<SC1>(rWS, qo); if (w == 6) s.v[1] = ld128<SC1>(rWS, qo2); else s.v[1] = s.v[0]; },
            [&](const PSet<2>& s) { return all_ok(tag_ok(s.v[0], tg12) && tag_ok(s.v[1], tg12)); }, dead);
        if (w == 7) TS(4);
#pragma unroll
        for (int k = 0; k < 2; ++k) {
            if (k == 0 || w == 6) {
                const float q = sc16(sc32(untag(qv1.v[k][0]), untag(qv1.v[k][2])), sc32(untag(qv1.v[k][1]), untag(qv1.v[k][3])));
                const u32 hi = bf1(q), lo = bf1(q - __uint_as_float(hi << 16));
                lds_st<unsigned short>(qadr + 128 * k, (unsigned short)hi); lds_st<unsigned short>(qadr + 128 * k + 384, (unsigned short)lo);
            }
        }
        flag_add(F_QH, lane);
        retire(pa, pb, pc);
    }
    if (w < 6) {
        wait_ge<false>(F_QH, QHT, dead);
        f32x4 a0 = {0.f, 0.f, 0.f, 0.f}, a1 = {0.f, 0.f, 0.f, 0.f};
#pragma unroll
        for (int t = 0; t < 6; t += 2) {
            a0 = mfma32(lds_ld<u32x4>(L_QH + (lane & 1) * 384 + 64 * t + 16 * g), bw2[t], a0);
            a1 = mfma32(lds_ld<u32x4>(L_QH + (lane & 1) * 384 + 64 * (t + 1) + 16 * g), bw2[t + 1], a1);
        }
        const float qa = ((a0[0] + a0[1]) + (a1[0] + a1[1])) * (0.07216878364870322f * 1.4426950408889634f);
        const u32 qh = bf1(qa), ql = bf1(qa - __uint_as_float(qh << 16));
        const u32x4 o = pack_row((QLO && (g & 1)) ? ql : qh);
        const u32 dst = QLO ? W_QA + ((u32)r * 8 + x) * 384u + (w >> 1) * 128 + g * 64 + (w & 1) * 32 + ((lane >> 3) & 1) * 16
                            : W_QA + ((u32)r * 8 + x) * 256u + w * 32 + ((lane >> 3) & 1) * 16;
        wait_ge<false>(F_RDYA, 1, dead);
        st128<SC1>(o, rWS, (lane & (QLO ? 39 : 55)) == 0 ? dst : OOB);
        flag_add(F_QAST, lane);
        if (w == 0) TS(5);
    }

    const u32 g2 = r >> 4;
    const u32 blk = L_A + BLK * w;
    const u32 t_blk = T_TILE0 + 12 * (w + 1);
    u32x4 vt[6][2];
    {
        constexpr int NQ = QLO ? 6 : 3;
        u32x4 bq[NQ];
        const u32 qoff = W_QA + ((16 * g2 + i16) * 8 + w) * (QLO ? 384u : 256u) + g * 16u;
        auto issue_q = [&]() {
            CBAR();
#pragma unroll
            for (int k = 0; k < NQ; ++k) bq[k] = ld128<SC1>(rWS, qoff + k * 64);
            CBAR();
        };
        bool seen = false, gate = false, landed = false; u32 tq = 0;
        auto gate_step = [&]() {
            if (!seen) { if (flag_ld(F_QAST) >= 6) { seen = true; tq = (u32)__builtin_amdgcn_s_memtime(); } }
            else gate = (u32)__builtin_amdgcn_s_memtime() - tq >= QDLY * 64u;
        };
        wait_ge<false>(F_RDYA, 1, dead);
        for (u32 n = 0; !dead;) {
            landed = flag_ld(F_LAND) >= t_blk;
            if (landed) break;
            gate_step();
            if (gate) break;
            if (++n > CAP_L) dead = true;
            __builtin_amdgcn_s_sleep(1);
        }
        if (!landed) { issue_q(); wait_ge<true>(F_LAND, t_blk, dead); }
        if (w == 0) TS(6);
        if (w == 7) TS(27);
        u32x4 av[4][3];
        const u32 ar = blk + i16 * 192, asw = ((i16 >> 2) & 1) << 1;
#pragma unroll
        for (int kb = 0; kb < 4; ++kb)
#pragma unroll
            for (int t = 0; t < 3; ++t) av[kb][t] = lds_ld<u32x4>(ar + kb * 16 * 192 + ((4 * t + g) ^ asw) * 16);
        const u32 vb = blk + (4 * g + q4) * 192 + (p4 >> 1) * 16 + 8 * (p4 & 1);
#pragma unroll
        for (int u = 0; u < 6; ++u) {
            const u32 cu = vb + (u ^ (g & 1)) * 32;
#pragma unroll
            for (int t = 0; t < 2; ++t) vt[u][t] = cat(trd(cu + (32 * t) * 192), trd(cu + (32 * t + 16) * 192));
        }
        asm volatile("s_waitcnt lgkmcnt(0)" ::: "memory");
        flag_add(F_BLK, lane);
        if (w == 7) TS(28);
        if (landed) {
            for (u32 n = 0; !dead && !gate;) { gate_step(); if (++n > CAP_L) dead = true; if (!gate) __builtin_amdgcn_s_sleep(1); }
            issue_q();
        }
        {
            u32 spins = 0;
            for (;;) {
                u32 mx = 0;
#pragma unroll
                for (int k = 0; k < NQ; ++k) mx = mx4(mx, bq[k]);
                if (none_sent(mx)) break;
                POLL_STEP();
                if (QMASK) { if (mx == SENT) issue_q(); } else issue_q();
            }
        }
        flag_add(F_QAOK, lane);
        if (w == 0) TS(7);
        if (w == 7) TS(26);
#pragma unroll
        for (int kb = 0; kb < 4; ++kb) {
            f32x4 acc = {0.f, 0.f, 0.f, 0.f};
#pragma unroll
            for (int t = 0; t < 3; ++t) { if (QLO) { acc = mfma32(av[kb][t], bq[(2 * t + 1) % NQ], acc); acc = mfma32(av[kb][t], bq[(2 * t) % NQ], acc); } else acc = mfma32(av[kb][t], bq[t], acc); }
            f32x2 lo2, hi2; lo2[0] = acc[0]; lo2[1] = acc[1]; hi2[0] = acc[2]; hi2[1] = acc[3];
            lds_st<f32x2>(blk + (kb * 2) * 512 + lane * 8, lo2);
            lds_st<f32x2>(blk + (kb * 2 + 1) * 512 + lane * 8, hi2);
        }
        flag_add(F_SADD, lane);
        if (w == 0) TS(20);
    }
    {
        wait_ge<false>(F_SADD, 8, dead);
        if (w == 0) TS(21);
        f32x2 s = {0.f, 0.f};
#pragma unroll
        for (int p = 0; p < 8; ++p) s += lds_ld<f32x2>(L_A + BLK * p + w * 512 + lane * 8);
        lds_st<f32x2>(L_SC + (w >> 1) * 1024 + lane * 16 + (w & 1) * 8, s);
        flag_add(F_SC, lane);
        wait_ge<false>(F_SC, 8, dead);
        if (w == 0) TS(22);
        f32x4 sc[4];
#pragma unroll
        for (int q = 0; q < 4; ++q) sc[q] = lds_ld<f32x4>(L_SC + q * 1024 + lane * 16);
        float mloc = fmaxf(fmaxf(fmaxf(sc[0][0], sc[0][1]), fmaxf(sc[0][2], sc[0][3])), fmaxf(fmaxf(sc[1][0], sc[1][1]), fmaxf(sc[1][2], sc[1][3])));
        mloc = fmaxf(mloc, fmaxf(fmaxf(fmaxf(sc[2][0], sc[2][1]), fmaxf(sc[2][2], sc[2][3])), fmaxf(fmaxf(sc[3][0], sc[3][1]), fmaxf(sc[3][2], sc[3][3]))));
        mloc = xg_max(mloc);
        u32x4 pf[2];
#pragma unroll
        for (int t = 0; t < 2; ++t)
#pragma unroll
            for (int e = 0; e < 4; ++e) {
                const f32x4 sq = sc[2 * t + (e >> 1)];
                pf[t][e] = pk2(__builtin_amdgcn_exp2f(sq[2 * (e & 1)] - mloc), __builtin_amdgcn_exp2f(sq[2 * (e & 1) + 1] - mloc));
            }
        f32x4 oa[6], lz;
#pragma unroll
        for (int u = 0; u < 6; ++u) { f32x4 z = {0.f, 0.f, 0.f, 0.f}; oa[u] = mfma32(vt[u][0], pf[0], z); oa[u] = mfma32(vt[u][1], pf[1], oa[u]); }
        {
            u32x4 one; one[0] = 0x3F803F80u; one[1] = 0x3F803F80u; one[2] = 0x3F803F80u; one[3] = 0x3F803F80u;
            f32x4 z = {0.f, 0.f, 0.f, 0.f};
            lz = mfma32(one, pf[0], z); lz = mfma32(one, pf[1], lz);
        }
        if (w == 0) TS(23);
        const u32 jj = x * 16 + (r & 15);
        const u32 ob = W_O + ((w * 128 + jj) * 2 + g2) * 3072u + (i16 * 4 + g) * 16;
        u32 cx = 0;
#pragma unroll
        for (int k = 0; k < 3; ++k) {
            u32x4 d;
            d[0] = pk2(oa[2 * k][0], oa[2 * k][1]); d[1] = pk2(oa[2 * k][2], oa[2 * k][3]);
            d[2] = pk2(oa[2 * k + 1][0], oa[2 * k + 1][1]); d[3] = pk2(oa[2 * k + 1][2], oa[2 * k + 1][3]);
            st128<SC1>(d, rWS, ob + k * 1024);
            cx ^= (d[0] ^ d[1]) ^ (d[2] ^ d[3]);
        }
        cx = xg_xor(cx);
        u32x4 pk;
        pk[0] = __float_as_uint(mloc); pk[1] = __float_as_uint(lz[0]);
        pk[2] = cx ^ pk[0] ^ pk[1] ^ T2; pk[3] = T1;
        st128<SC1>(pk, rWS, g == 0 ? W_PK + ((w * 128 + jj) * 32 + 16 * g2 + i16) * 16 : OOB);
        if (w == 0) TS(8);
    }

    {
        const u32 poff = W_PK + ((x * 128 + 16 * w + 4 * g + (i16 & 3)) * 32 + r) * 16;
        const u32 ooff = i16 < 12 ? W_O + ((x * 128 + 16 * w + 4 * g) * 2 + g2) * 3072u + (i16 >> 2) * 1024 + ((r & 15) * 4 + (i16 & 3)) * 16 : OOB;
        u32x4 bw[2][3];
        if (w < 6) {
            wait_ge<true>(F_LAND, T_UV, dead);
            const u32 sw = 2 * (g & 1) + (q4 >> 1);
#pragma unroll
            for (int d = 0; d < 2; ++d) {
                const u32 ub = L_X + (4 * g + q4) * 384 + (2 * ((2 * w + d) ^ sw) + (p4 >> 1)) * 16 + 8 * (p4 & 1);
#pragma unroll
                for (int t = 0; t < 3; ++t) bw[d][t] = cat(trd(ub + (32 * t) * 384), trd(ub + (32 * t + 16) * 384));
            }
        }
        if (ODLY) __builtin_amdgcn_s_sleep(ODLY);
        u32x4 pk, od[4];
        {
            u32 spins = 0;
            for (;;) {
                CBAR(); pk = ld128<SC1>(rWS, poff); CBAR();
                if (all_ok(pk[3] == T1)) break;
                POLL_STEP();
            }
        }
        if (w == 0) TS(35);
        {
            u32 spins = 0;
            u64 bad = ~0ull;
            for (;;) {
                CBAR();
#pragma unroll
                for (int k = 0; k < 4; ++k) if (bad & (0x1111111111111111ull << k)) od[k] = ld128<SC1>(rWS, ooff + k * 6144);
                if (!ELIGHT || spins) pk = ld128<SC1>(rWS, poff);
                CBAR();
                u32 c[4];
#pragma unroll
                for (int k = 0; k < 4; ++k) c[k] = (od[k][0] ^ od[k][1]) ^ (od[k][2] ^ od[k][3]);
                const bool b0 = lane & 1, b1 = lane & 2;
                const u32 t01 = (b0 ? c[1] : c[0]) ^ dppu<QX1>(b0 ? c[0] : c[1]), t23 = (b0 ? c[3] : c[2]) ^ dppu<QX1>(b0 ? c[2] : c[3]);
                u32 t = (b1 ? t23 : t01) ^ dppu<QX2>(b1 ? t01 : t23);
                t ^= dppu<ROR(4)>(t); t ^= dppu<ROR(8)>(t);
                const bool ok = pk[3] == T1 && (t ^ pk[0] ^ pk[1] ^ T2) == pk[2];
                bad = __builtin_amdgcn_ballot_w64(!ok);
                if (bad == 0ull) break;
                POLL_STEP();
            }
        }
        if (w == 0) TS(9);
        const float mj = __uint_as_float(pk[0]);
        float Mw = fmaxf(mj, dppf<QX1>(mj)); Mw = fmaxf(Mw, dppf<QX2>(Mw)); Mw = xg_max(Mw);
        const u32 wb = bf1(__builtin_amdgcn_exp2f(mj - Mw));
        const u32 sh = (lane & 1) ? 65536u : 1u;
        u32x4 aw;
        aw[0] = __umul24(dppu<0x00>(wb), sh); aw[1] = __umul24(dppu<0x55>(wb), sh); aw[2] = __umul24(dppu<0xAA>(wb), sh); aw[3] = __umul24(dppu<0xFF>(wb), sh);
        u32x4 yv;
#pragma unroll
        for (int u = 0; u < 4; ++u) {
            u32x4 bo; bo[0] = od[0][u]; bo[1] = od[1][u]; bo[2] = od[2][u]; bo[3] = od[3][u];
            f32x4 z = {0.f, 0.f, 0.f, 0.f};
            z = mfma32(aw, bo, z);
            yv[u] = pk2(z[0], z[1]);
        }
        float lw = __uint_as_float(wb << 16) * __uint_as_float(pk[1]);
        lw += dppf<QX1>(lw); lw += dppf<QX2>(lw); lw = xg_sum(lw);
        if (lane < 12) lds_st<u32x4>(L_AM + w * 208 + lane * 16, yv);
        { f32x2 ml; ml[0] = Mw; ml[1] = lw; if (lane == 0) lds_st<f32x2>(L_EML + w * 8, ml); }
        flag_add(F_AM, lane);
        if (w < 6) {
            wait_ge<false>(F_AM, 8, dead);
            u32x4 aa[3];
#pragma unroll
            for (int t = 0; t < 3; ++t) aa[t] = lds_ld<u32x4>(L_AM + (i16 & 7) * 208 + (4 * t + g) * 16);
            const f32x4 e0 = lds_ld<f32x4>(L_EML + (g & 1) * 32), e1 = lds_ld<f32x4>(L_EML + (g & 1) * 32 + 16);
            float Mx = fmaxf(fmaxf(e0[0], e0[2]), fmaxf(e1[0], e1[2]));
            { float a = Mx, bb = Mx; swap16(a, bb); Mx = fmaxf(a, bb); }
            const float f0 = __builtin_amdgcn_exp2f(e0[0] - Mx), f1 = __builtin_amdgcn_exp2f(e0[2] - Mx), f2 = __builtin_amdgcn_exp2f(e1[0] - Mx), f3 = __builtin_amdgcn_exp2f(e1[2] - Mx);
            const float ls = (f0 * e0[1] + f1 * e0[3]) + (f2 * e1[1] + f3 * e1[3]);
            const float rl = __builtin_amdgcn_rcpf(sc16(ls, ls));
            float res[2];
#pragma unroll
            for (int d = 0; d < 2; ++d) {
                f32x4 z = {0.f, 0.f, 0.f, 0.f};
#pragma unroll
                for (int t = 0; t < 3; ++t) z = mfma32(aa[t], bw[d][t], z);
                res[d] = (f0 * z[0] + f1 * z[1]) + (f2 * z[2] + f3 * z[3]);
            }
            const u32x4 o = pack_row(bf1(sc16(res[0], res[1]) * rl));
            const u32 dst = W_CTX + ((u32)r * 8 + x) * 384u + w * 64 + g * 32 + ((lane >> 3) & 1) * 16;
            st128<SC1>(o, rWS, (lane & 39) == 0 ? dst : OOB);
            if (w == 0) TS(10);
        }
    }

    if (typeA) {
        const u32 coff = i16 < 8 ? W_CTX + ((4 * x + (w >> 1)) * 8 + i16) * 384u + (w & 1) * 192 + g * 16 : OOB;
        wait_ge<true>(F_LAND, T_UV + 6 * (w + 1), dead);
        u32x4 bw[2][3];
        const u32 fbx = blk + B_W + (8 * g + q4) * 64 + (p4 >> 1) * 16 + 8 * (p4 & 1);
#pragma unroll
        for (int u = 0; u < 2; ++u) {
            const u32 ub = fbx + (u ^ (g & 1)) * 32;
#pragma unroll
            for (int t = 0; t < 3; ++t) bw[u][t] = cat(trd(ub + (32 * t) * 64), trd(ub + (32 * t + 4) * 64));
        }
        if (w == 0) TS(36);
        PSet<3> ca, pa, pb, pc;
        ppoll<3, STG2>(ca, pa, pb, pc,
            [&](PSet<3>& s) { s.v[0] = ld128<SC1>(rWS, coff); s.v[1] = ld128<SC1>(rWS, coff + 64); s.v[2] = ld128<SC1>(rWS, coff + 128); },
            [&](const PSet<3>& s) { return none_sent(mx4(mx4(mx4(0, s.v[0]), s.v[1]), s.v[2])); }, dead);
        if (w == 0) TS(11);
        float res[2];
#pragma unroll
        for (int u = 0; u < 2; ++u) {
            f32x4 z = {0.f, 0.f, 0.f, 0.f};
#pragma unroll
            for (int t = 0; t < 3; ++t) z = mfma32(ca.v[t], bw[u][t], z);
            res[u] = xg_sum((z[0] + z[1]) + (z[2] + z[3]));
        }
        if (g < 2) lds_st<float>(L_FRP + (w * 32 + 16 * g + i16) * 4, g == 0 ? res[0] : res[1]);
        if (flag_add_ret(F_FRC, lane) == 7) {
            float v = 0.f;
#pragma unroll
            for (int p = 0; p < 8; ++p) v += lds_ld<float>(L_FRP + (p * 32 + (lane & 31)) * 4);
            const u32x4 o = pack_row(bf1(v));
            const u32 dst = W_AT + (x * 640u + 32u * r + 16 * (g & 1) + (lane & 8)) * 2u;
            st128<SC1>(o, rWS, (g < 2 && (lane & 7) == 0) ? dst : OOB);
            TS(12);
        }
        retire(pa, pb, pc);
    } else {
        const u32 fb = x * 12 + (r - 20);
        const u32 cA = 8 * (lane & 7) + (lane >> 3);
        const bool hasB = (lane & 7) < 2;
        const u32 colA = 80 * w + cA, colB = colA + 64;
        float ga[2], sb[2], cb1[2], gb[2], xv[2];
        {
            xv[0] = __uint_as_float(ld16(rX, colA * 2) << 16); xv[1] = __uint_as_float(ld16(rX, hasB ? colB * 2 : OOB) << 16);
            u32x4 mr[2][2]; u32 spins = 0;
            const u32 posB = 64 + (lane >> 3) * 2 + (lane & 1);
            for (;;) {
                CBAR();
                u32 mx = 0;
#pragma unroll
                for (int kh = 0; kh < 2; ++kh) {
                    mr[kh][0] = ld128<SC1>(rWS, W_MR + ((kh * 8u + w) * 80u + lane) * 16u);
                    mr[kh][1] = ld128<SC1>(rWS, hasB ? W_MR + ((kh * 8u + w) * 80u + posB) * 16u : OOB);
                    mx = mx4(mx4(mx, mr[kh][0]), mr[kh][1]);
                }
                if (none_sent(mx)) break;
                POLL_STEP();
            }
#pragma unroll
            for (int c = 0; c < 2; ++c) {
                ga[c] = __uint_as_float(mr[0][c][0]) + __uint_as_float(mr[1][c][0]);
                sb[c] = __uint_as_float(mr[0][c][1]) + __uint_as_float(mr[1][c][1]);
                cb1[c] = 1.f + (__uint_as_float(mr[0][c][2]) + __uint_as_float(mr[1][c][2]));
                gb[c] = __uint_as_float(mr[0][c][3]) + __uint_as_float(mr[1][c][3]);
            }
        }
        if (w == 0) TS(13);
        const u32 gab = blk + B_GA;
#pragma unroll
        for (int c = 0; c < 2; ++c) {
            if (c == 0 || hasB) {
                const u32 cc = cA + 64 * c;
                const u32 h = bf1(cb1[c]), l = bf1(cb1[c] - __uint_as_float(h << 16)), h2 = bf1(sb[c]), l2 = bf1(sb[c] - __uint_as_float(h2 << 16));
                lds_st<unsigned short>(gab + 2 * 160 + cc * 2, (unsigned short)h); lds_st<unsigned short>(gab + 3 * 160 + cc * 2, (unsigned short)l);
                lds_st<unsigned short>(gab + 4 * 160 + cc * 2, (unsigned short)h2); lds_st<unsigned short>(gab + 5 * 160 + cc * 2, (unsigned short)l2);
                lds_st<float>(blk + B_GB + cc * 4, gb[c]);
            }
        }
        wait_ge<true>(F_LAND, T_UV + 6 * (w + 1), dead);
        u32x2 b1[5], b2[5];
#pragma unroll
        for (int t = 0; t < 5; ++t) b1[t] = trd(blk + B_W + 512 * t + 8 * lane);
#pragma unroll
        for (int u = 0; u < 5; ++u) b2[u] = trd(blk + B_W2 + (4 * g + q4) * 160 + 32 * u + 8 * p4);
        if (w == 0) TS(36);
        const u32 aoff = W_AT + ((lane >> 3) * 640u + 80u * w + 8 * (lane & 7)) * 2u, aoff2 = hasB ? aoff + 128 : OOB;
        PSet<2> at, pa, pb, pc;
        ppoll<2, STG2>(at, pa, pb, pc,
            [&](PSet<2>& s) { s.v[0] = ld128<SC1>(rWS, aoff); s.v[1] = ld128<SC1>(rWS, aoff2); },
            [&](const PSet<2>& s) { return none_sent(mx4(mx4(0, s.v[0]), s.v[1])); }, dead);
        if (w == 0) TS(14);
        float x1[2];
#pragma unroll
        for (int c = 0; c < 2; ++c) {
            const u32x4 a = at.v[c];
            float v[8];
#pragma unroll
            for (int j = 0; j < 4; ++j) { v[2 * j] = f_lo(a[j]); v[2 * j + 1] = f_hi(a[j]); }
            float t4[4], t2[2];
#pragma unroll
            for (int j = 0; j < 4; ++j) t4[j] = sc32(v[j], v[j + 4]);
#pragma unroll
            for (int j = 0; j < 2; ++j) t2[j] = sc16(t4[j], t4[j + 2]);
            const bool b3 = (lane & 8) != 0;
            const float keep = b3 ? t2[1] : t2[0], give = b3 ? t2[0] : t2[1];
            const float attn = keep + dppf<ROR(8)>(give);
            x1[c] = xv[c] + ga[c] * attn;
        }
        if (!hasB) x1[1] = 0.f;
#pragma unroll
        for (int c = 0; c < 2; ++c) {
            if (c == 0 || hasB) {
                const u32 cc = cA + 64 * c;
                const float uv = x1[c] * cb1[c];
                const u32 h = bf1(uv), l = bf1(uv - __uint_as_float(h << 16));
                lds_st<unsigned short>(gab + cc * 2, (unsigned short)h); lds_st<unsigned short>(gab + 160 + cc * 2, (unsigned short)l);
                lds_st<float>(blk + B_X1 + cc * 4, x1[c]);
            }
        }
        CBAR();
        f32x4 z = {0.f, 0.f, 0.f, 0.f};
        const u32 arow = (q4 < 2 ? q4 : 2) * 2 + (lane & 1);
#pragma unroll
        for (int t = 0; t < 5; ++t) z = mfma16(lds_ld<u32x2>(gab + arow * 160 + 32 * t + 8 * g), b1[t], z);
        const float s1 = wave_sum(x1[0] + x1[1]), s2 = wave_sum(x1[0] * x1[0] + x1[1] * x1[1]);
        lds_st<float>(L_GRP + w * 256 + lane * 4, g < 3 ? z[0] + z[1] : (p4 == 0 ? s1 : (p4 == 1 ? s2 : 0.f)));
        flag_add(F_GRC, lane);
        wait_ge<false>(F_GRC, 8, dead);
        f32x4 gs = lds_ld<f32x4>(L_GRP + (2 * p4) * 256 + q4 * 64 + g * 16) + lds_ld<f32x4>(L_GRP + (2 * p4 + 1) * 256 + q4 * 64 + g * 16);
#pragma unroll
        for (int e = 0; e < 4; ++e) { gs[e] += dppf<QX1>(gs[e]); gs[e] += dppf<QX2>(gs[e]); }
        const float mu = dppf<NBC(12)>(gs[0]) * (1.f / 640.f);
        const float var = fmaxf(dppf<NBC(12)>(gs[1]) * (1.f / 640.f) - mu * mu, 0.f);
        const float rstd = 1.f / sqrtf(var + 1e-5f);
        float av[4];
#pragma unroll
        for (int e = 0; e < 4; ++e) av[e] = fmaxf(rstd * (dppf<NBC(0)>(gs[e]) - mu * dppf<NBC(4)>(gs[e])) + dppf<NBC(8)>(gs[e]), 0.f);
        u32x2 af; { const u32 h0 = pk2(av[0], av[1]), h1b = pk2(av[2], av[3]);
            const u32 l0 = pk2(av[0] - f_lo(h0), av[1] - f_hi(h0)), l1 = pk2(av[2] - f_lo(h1b), av[3] - f_hi(h1b));
            af[0] = (lane & 1) ? l0 : h0; af[1] = (lane & 1) ? l1 : h1b; }
        float fo[5];
#pragma unroll
        for (int u = 0; u < 5; ++u) { f32x4 y = {0.f, 0.f, 0.f, 0.f}; y = mfma16(af, b2[u], y); fo[u] = y[0] + y[1]; }
        {
            const float v = g == 0 ? fo[0] : (g == 1 ? fo[1] : (g == 2 ? fo[2] : fo[3]));
            const u32x4 o = pack_row(bf1(v)), o2 = pack_row(bf1(fo[4]));
            st128<SC1>(o, rWS, (lane & 7) == 0 ? W_FP + (((5 * w + g) * 96u + fb) * 16 + (lane & 8)) * 2u : OOB);
            st128<SC1>(o2, rWS, (lane & 55) == 0 ? W_FP + (((5 * w + 4) * 96u + fb) * 16 + (lane & 8)) * 2u : OOB);
        }
        if (w == 0) TS(15);
        const int ci = (r - 20) * 8 + x;
        if (ci < 40 && ci / 5 == w) {
            const u32 fo0 = W_FP + (u32)ci * 3072u + lane * 16;
            PSet<3> fv, qa, qb, qc;
            ppoll<3, STG2>(fv, qa, qb, qc,
                [&](PSet<3>& s) { s.v[0] = ld128<SC1>(rWS, fo0); s.v[1] = ld128<SC1>(rWS, fo0 + 1024); s.v[2] = ld128<SC1>(rWS, fo0 + 2048); },
                [&](const PSet<3>& s) { return none_sent(mx4(mx4(mx4(0, s.v[0]), s.v[1]), s.v[2])); }, dead);
            TS(16);
            float v[8];
#pragma unroll
            for (int j = 0; j < 4; ++j) { v[2 * j] = (f_lo(fv.v[0][j]) + f_lo(fv.v[1][j])) + f_lo(fv.v[2][j]); v[2 * j + 1] = (f_hi(fv.v[0][j]) + f_hi(fv.v[1][j])) + f_hi(fv.v[2][j]); }
            float t4[4], t2[2];
#pragma unroll
            for (int j = 0; j < 4; ++j) t4[j] = sc32(v[j], v[j + 4]);
#pragma unroll
            for (int j = 0; j < 2; ++j) t2[j] = sc16(t4[j], t4[j + 2]);
            const bool b3 = (lane & 8) != 0;
            const float keep = b3 ? t2[1] : t2[0], give = b3 ? t2[0] : t2[1];
            float f = keep + dppf<ROR(8)>(give);
            f += dppf<SHL(4)>(f);
            f += dppf<SHL(2)>(f);
            const u32 cc = 16 * (ci % 5) + 8 * (lane & 1) + (lane >> 3);
            const float o = lds_ld<float>(blk + B_X1 + cc * 4) + lds_ld<float>(blk + B_GB + cc * 4) * f;
            const rsrc_t rO = mkrs(p_out, M_ * 2);
            __builtin_amdgcn_raw_buffer_store_b16((unsigned short)bf1(o), rO, (lane & 6) == 0 ? (16 * ci + 8 * (lane & 1) + (lane >> 3)) * 2 : OOB, 0, 0);
            if (ci == 0 && lane == 0) { u32x2 e; e[0] = T1; e[1] = T2; st64<SC1>(e, rWS, W_EPOCH); }
            TS(17);
        }
    }
    TSDUMP();
}

extern "C" __device__ __attribute__((noinline, used)) void adaln_code_end() { asm volatile("s_nop 0"); }

extern "C" void kernel_launch(void* const* d_in, const int* in_sizes, int n_in, void* d_out, int out_size, void* d_ws, size_t ws_size, hipStream_t stream)
{
    (void)in_sizes; (void)n_in; (void)out_size; (void)ws_size;
    (void)hipFuncSetAttribute((const void*)adaln_mla_ffn_kernel, hipFuncAttributeMaxDynamicSharedMemorySize, (int)LDS_BYTES);
    hipLaunchKernelGGL(adaln_mla_ffn_kernel, dim3(256), dim3(640), LDS_BYTES, stream,
                       (const void*)d_in[0], (const void*)d_in[1], (const void*)d_in[2], (const void*)d_in[3], (const void*)d_in[4], (const void*)d_in[5],
                       (const void*)d_in[7], (const void*)d_in[8], (const void*)d_in[9], (const void*)d_in[10], d_out, d_ws);
}
```

```cpp
#include <hip/hip_runtime.h>
#include <hip/hip_bf16.h>
#include <stdint.h>

#define LAS __attribute__((address_space(3)))
#define DI __device__ __forceinline__
typedef unsigned int u32;
typedef unsigned long long u64;
typedef u32 u32x2 __attribute__((ext_vector_type(2)));
typedef u32 u32x3 __attribute__((ext_vector_type(3)));
typedef u32 u32x4 __attribute__((ext_vector_type(4)));
typedef float f32x2 __attribute__((ext_vector_type(2)));
typedef float f32x4 __attribute__((ext_vector_type(4)));
typedef short s16x4 __attribute__((ext_vector_type(4)));
typedef __bf16 bf16x2 __attribute__((ext_vector_type(2)));
typedef __bf16 bf16x8 __attribute__((ext_vector_type(8)));
typedef __amdgpu_buffer_rsrc_t rsrc_t;

#define WIN 18
#define WIN2 18
#define PAUSE 0
#define STG 6
#define STG2 3
#define NT_S0 0
#define NT_MR 2
#define NT_Q 1
#define NT_UK 1
#define NT_KV 1
#define NT_UV 1
#define NT_O 1
#define NT_W1 1
#define NT_W2 1
#define LPRIO 3
#define CPF 1
#define FEND "adaln_code_end"
#define KNAME "adaln_mla_ffn_kernel"
#define QDLY 40
#define ODLY 0
#define ELIGHT 1
#define MRAT 1
#define TH1 6
#define TH2 9
#define WINT 24
#define QLO 1
#define OGA 4
#define OGB 0
#define QMASK 1
#define WSOFF -1
#define LSLP 0
#define O_BASE 1048576u

constexpr int M_ = 640, H_ = 32, D_ = 192, DC = 768, SK = 8192, DFF = 1536;
constexpr u32 SENT = 0xFFFFFFFFu;
constexpr u32 OOB = 0x80000000u;

constexpr u32 W_EPOCH = 0;
constexpr u32 W_FA = 256;
constexpr u32 W_G1 = 4096;
constexpr u32 W_Q2 = 32768;
constexpr u32 W_SMALL = W_Q2 + 98304;
constexpr u32 W_QA = W_SMALL;
constexpr u32 W_CTX = W_QA + 98304;
constexpr u32 W_AT = W_CTX + 98304;
constexpr u32 W_FP = W_AT + 10240;
constexpr u32 W_MR = W_FP + 122880;
constexpr u32 W_SMALL_END = W_MR + 20480;
constexpr u32 SMALL_CH = (W_SMALL_END - W_SMALL) / 16;
constexpr u32 SMALL_PER = (SMALL_CH + 255) / 256;
constexpr u32 W_O = O_BASE;
constexpr u32 W_PK = W_O + 8u * 128 * 2 * 3072;
constexpr u32 O_BYTES = 8u * 128 * 2 * 3072 + 8u * 128 * 32 * 16;
constexpr u32 W_PROF = W_SMALL_END;
constexpr u32 W_END = W_O + O_BYTES;
static_assert(W_PROF + 256 * 64 * 8 <= W_O, "ws map");
static_assert(SMALL_PER <= 128, "prefill");
static_assert(W_END <= 268435456u, "ws map");

constexpr u32 L_A = 0;
constexpr u32 BLK = 12288;
constexpr u32 L_S1W = 5 * BLK;
constexpr u32 L_X = 98304;
constexpr u32 L_Y = 135168;
constexpr u32 L_FLG = L_Y;
constexpr u32 L_S0P = L_Y + 13568;
constexpr u32 L_LAND = L_Y + 14336;
constexpr u32 L_EML = L_Y + 448;
constexpr u32 L_FRP = L_Y + 512;
constexpr u32 L_GRP = L_Y + 1536;
constexpr u32 L_SC = L_Y + 3584;
constexpr u32 L_HX = L_Y + 7680;
constexpr u32 L_QH = L_Y + 8448;
constexpr u32 L_AM = L_Y + 9216;
constexpr u32 L_SIL = L_Y + 12288;
constexpr u32 L_XN = L_Y + 17408;
constexpr u32 LDS_BYTES = 163840;
constexpr u32 B_W = 4096, B_W2 = 7168, B_GA = 10240, B_X1 = 11200, B_GB = 11520;

enum { F_LAND = 0, F_RDYA, F_S0CNT, F_S1DONE, F_HX, F_QH, F_S2DONE, F_BLK, F_QAOK, F_SADD, F_SC, F_AM, F_FRC, F_GRC, F_QAST, F_XN };

constexpr u32 CAP_G = 20000;
constexpr u32 CAP_L = 400000;

constexpr u32 T_S1 = 30, T_S2 = 66, T_TILE0 = 66, T_UV = 198;

DI rsrc_t mkrs(const void* p, u32 bytes) { return __builtin_amdgcn_make_buffer_rsrc((void*)p, (short)0, (int)bytes, 0x00020000); }
template <int AUX> DI u32x4 ld128(rsrc_t r, u32 off) { return __builtin_amdgcn_raw_buffer_load_b128(r, off, 0, AUX); }
template <int AUX> DI u32x3 ld96(rsrc_t r, u32 off) { return __builtin_amdgcn_raw_buffer_load_b96(r, off, 0, AUX); }
template <int AUX> DI u32x2 ld64(rsrc_t r, u32 off) { return __builtin_amdgcn_raw_buffer_load_b64(r, off, 0, AUX); }
template <int AUX> DI u32 ld32(rsrc_t r, u32 off) { return __builtin_amdgcn_raw_buffer_load_b32(r, off, 0, AUX); }
DI u32 ld16(rsrc_t r, u32 off) { return (u32)__builtin_amdgcn_raw_buffer_load_b16(r, off, 0, 0); }
template <int AUX> DI void st128(u32x4 v, rsrc_t r, u32 off) { __builtin_amdgcn_raw_buffer_store_b128(v, r, off, 0, AUX); }
template <int AUX> DI void st96(u32x3 v, rsrc_t r, u32 off) { __builtin_amdgcn_raw_buffer_store_b96(v, r, off, 0, AUX); }
template <int AUX> DI void st64(u32x2 v, rsrc_t r, u32 off) { __builtin_amdgcn_raw_buffer_store_b64(v, r, off, 0, AUX); }
template <int AUX> DI void st32(u32 v, rsrc_t r, u32 off) { __builtin_amdgcn_raw_buffer_store_b32(v, r, off, 0, AUX); }
constexpr int SC1 = 16, NT = 2;

DI float f_lo(u32 v) { return __uint_as_float(v << 16); }
DI float f_hi(u32 v) { return __uint_as_float(v & 0xFFFF0000u); }
DI u32 pk2(float a, float b) { bf16x2 v; v[0] = (__bf16)a; v[1] = (__bf16)b; return __builtin_bit_cast(u32, v); }
DI u32 bf1(float a) { return pk2(a, 0.f) & 0xFFFFu; }
DI u32 san(u32 v) { return v < 0xFFFFFFFEu ? v : 0xFFFFFFFEu; }
DI u32x4 san4(u32x4 v) { u32x4 r; r[0] = san(v[0]); r[1] = san(v[1]); r[2] = san(v[2]); r[3] = san(v[3]); return r; }
DI u32 umax(u32 a, u32 b) { return a > b ? a : b; }
DI u32 mx4(u32 acc, u32x4 v) { return umax(umax(acc, umax(v[0], v[1])), umax(v[2], v[3])); }

DI u32 tagw(float v, u32 tg) { return ((__float_as_uint(v) + 0x800u) & 0xFFFFF000u) | tg; }
DI float untag(u32 v) { return __uint_as_float(v & 0xFFFFF000u); }
DI bool tag_ok(u32x4 v, u32 tg) { return ((((v[0] ^ tg) | (v[1] ^ tg)) | ((v[2] ^ tg) | (v[3] ^ tg))) & 0xFFFu) == 0u; }
DI float dot2(u32 a, u32 b, float c) { return __builtin_amdgcn_fdot2_f32_bf16(__builtin_bit_cast(bf16x2, a), __builtin_bit_cast(bf16x2, b), c, false); }
template <int CTRL> DI u32 dppu(u32 v) { return (u32)__builtin_amdgcn_update_dpp(0, (int)v, CTRL, 0xF, 0xF, true); }
template <int CTRL> DI float dppf(float v) { return __uint_as_float(dppu<CTRL>(__float_as_uint(v))); }
constexpr int ROR(int n) { return 0x120 + n; }
constexpr int SHL(int n) { return 0x100 + n; }
constexpr int NBC(int n) { return 0x150 + n; }
constexpr int QX1 = 0xB1, QX2 = 0x4E;
DI float row_sum(float v) { v += dppf<ROR(8)>(v); v += dppf<ROR(4)>(v); v += dppf<ROR(2)>(v); v += dppf<ROR(1)>(v); return v; }
DI u32 row_xor(u32 v) { v ^= dppu<ROR(8)>(v); v ^= dppu<ROR(4)>(v); v ^= dppu<ROR(2)>(v); v ^= dppu<ROR(1)>(v); return v; }
DI u32 row_add(u32 v) { v += dppu<ROR(8)>(v); v += dppu<ROR(4)>(v); v += dppu<ROR(2)>(v); v += dppu<ROR(1)>(v); return v; }
DI void swap16(float& a, float& b) { auto r = __builtin_amdgcn_permlane16_swap(__float_as_uint(a), __float_as_uint(b), false, false); a = __uint_as_float(r[0]); b = __uint_as_float(r[1]); }
DI void swap32(float& a, float& b) { auto r = __builtin_amdgcn_permlane32_swap(__float_as_uint(a), __float_as_uint(b), false, false); a = __uint_as_float(r[0]); b = __uint_as_float(r[1]); }
DI float xg_sum(float v) { float a = v, b = v; swap16(a, b); v = a + b; a = v; b = v; swap32(a, b); return a + b; }
DI float xg_max(float v) { float a = v, b = v; swap16(a, b); v = fmaxf(a, b); a = v; b = v; swap32(a, b); return fmaxf(a, b); }
DI u32 xg_xor(u32 v) {
    auto r = __builtin_amdgcn_permlane16_swap(v, v, false, false); v = r[0] ^ r[1];
    auto q = __builtin_amdgcn_permlane32_swap(v, v, false, false); return q[0] ^ q[1];
}
DI float wave_sum(float v) { return xg_sum(row_sum(v)); }
DI float sc32(float a, float b) { swap32(a, b); return a + b; }
DI float sc16(float a, float b) { swap16(a, b); return a + b; }
DI u32 sel4(int e, u32 a, u32 b, u32 c, u32 d) { const u32 lo = (e & 1) ? b : a, hi = (e & 1) ? d : c; return (e & 2) ? hi : lo; }

DI f32x4 mfma32(u32x4 a, u32x4 b, f32x4 c) { return __builtin_amdgcn_mfma_f32_16x16x32_bf16(__builtin_bit_cast(bf16x8, a), __builtin_bit_cast(bf16x8, b), c, 0, 0, 0); }
DI f32x4 mfma16(u32x2 a, u32x2 b, f32x4 c) { return __builtin_amdgcn_mfma_f32_16x16x16bf16_1k(__builtin_bit_cast(s16x4, a), __builtin_bit_cast(s16x4, b), c, 0, 0, 0); }
DI u32x2 trd(u32 addr) { return __builtin_bit_cast(u32x2, __builtin_amdgcn_ds_read_tr16_b64_v4i16((LAS s16x4*)addr)); }
DI u32x4 cat(u32x2 a, u32x2 b) { u32x4 r; r[0] = a[0]; r[1] = a[1]; r[2] = b[0]; r[3] = b[1]; return r; }
template <class T> DI T lds_ld(u32 addr) { return *(LAS T*)addr; }
template <class T> DI void lds_st(u32 addr, T v) { *(LAS T*)addr = v; }
#define CBAR() asm volatile("" ::: "memory")

DI u32 flag_ld(int i) { return (u32)__builtin_amdgcn_readfirstlane((int)*(volatile LAS u32*)(i == F_LAND ? L_LAND : L_FLG + 4 * i)); }
DI void flag_st(int i, u32 v, int lane) { CBAR(); if (lane == 0) *(volatile LAS u32*)(L_FLG + 4 * i) = v; CBAR(); }
DI void flag_add(int i, int lane) { CBAR(); if (lane == 0) __hip_atomic_fetch_add((LAS u32*)(L_FLG + 4 * i), 1u, __ATOMIC_RELAXED, __HIP_MEMORY_SCOPE_WORKGROUP); CBAR(); }
DI u32 flag_add_ret(int i, int lane) {
    CBAR(); u32 o = 0;
    if (lane == 0) o = __hip_atomic_fetch_add((LAS u32*)(L_FLG + 4 * i), 1u, __ATOMIC_RELAXED, __HIP_MEMORY_SCOPE_WORKGROUP);
    o = (u32)__builtin_amdgcn_readfirstlane((int)o); CBAR(); return o;
}
template <bool SLEEP> DI void wait_ge(int i, u32 target, bool& dead) {
    u32 n = 0;
    while (!dead && flag_ld(i) < target) { if (++n > CAP_L) dead = true; if (SLEEP || LSLP) __builtin_amdgcn_s_sleep(1); }
    CBAR();
}
DI bool all_ok(bool ok) { return __builtin_amdgcn_ballot_w64(!ok) == 0ull; }
DI bool none_sent(u32 mx) { return __builtin_amdgcn_ballot_w64(mx == SENT) == 0ull; }
#define POLL_STEP() { if (++spins > CAP_G) { dead = true; break; } __builtin_amdgcn_s_sleep(1); }

template <int N> struct PSet { u32x4 v[N]; };
template <int N> DI void take(PSet<N>& res, const PSet<N>& s) {
#pragma unroll
    for (int k = 0; k < N; ++k) res.v[k] = s.v[k];
}
#define PSLP 0

template <int N, int STAGGER, class LD, class OK>
DI void ppoll(PSet<N>& res, PSet<N>& a, PSet<N>& b, PSet<N>& c, LD ld, OK ok, bool& dead) {
    (void)a; (void)b; (void)c;
    for (u32 spins = 0;;) {
        CBAR(); ld(res); CBAR();
        if (ok(res)) break;
        if (++spins > CAP_G) { dead = true; break; }
        if (PSLP) __builtin_amdgcn_s_sleep(PSLP);
    }
    CBAR();
}
template <int N> DI void retire(PSet<N>&, PSet<N>&, PSet<N>&) {}

#define TS(i) do { } while (0)
#define TCAP(v) do { } while (0)
#define TSV(i, t_) do { } while (0)
#define TSDUMP() do { } while (0)

DI u32x4 pack_row(u32 v16) {
    u32 pr = v16 | (dppu<SHL(1)>(v16) << 16);
    u32x4 r; r[0] = pr; r[1] = dppu<SHL(2)>(pr); r[2] = dppu<SHL(4)>(pr); r[3] = dppu<SHL(6)>(pr);
    return san4(r);
}

template <int NTL> DI void dma3(u32 lds, u32 v0, u32 v1, u32 v2, rsrc_t rs, u32 soff) {
    if constexpr (NTL) asm volatile("s_mov_b32 m0, %0\n\ts_nop 3\n\tbuffer_load_dwordx4 %1, %4, %5 offen nt lds\n\tbuffer_load_dwordx4 %2, %4, %5 offen offset:1024 nt lds\n\t"
                                    "buffer_load_dwordx4 %3, %4, %5 offen offset:2048 nt lds" ::"s"(lds), "v"(v0), "v"(v1), "v"(v2), "s"(rs), "s"(soff) : "memory");
    else asm volatile("s_mov_b32 m0, %0\n\ts_nop 3\n\tbuffer_load_dwordx4 %1, %4, %5 offen lds\n\tbuffer_load_dwordx4 %2, %4, %5 offen offset:1024 lds\n\t"
                      "buffer_load_dwordx4 %3, %4, %5 offen offset:2048 lds" ::"s"(lds), "v"(v0), "v"(v1), "v"(v2), "s"(rs), "s"(soff) : "memory");
}
template <int N> DI void waitvm() { asm volatile("s_waitcnt vmcnt(%0)" ::"i"(N) : "memory"); }
constexpr int WM1 = WIN > WIN2 ? WIN : WIN2, WMAX = WM1 > WINT ? WM1 : WINT;
struct Ldr { int issued; int pub; u32 ladr; bool dead; };
DI void publish(Ldr& s, int landed) { s.pub = landed > s.pub ? landed : s.pub; CBAR(); *(volatile LAS u32*)s.ladr = (u32)s.pub; CBAR(); }
template <int W> DI void step3(Ldr& s) { s.issued += 3; waitvm<W>(); publish(s, s.issued - W); }
constexpr u32 QHT = 2;
template <int W, int T1, int T2> DI void step3f(Ldr& s) {
    s.issued += 3;
    CBAR();
    const u32x2 f = *(volatile LAS u32x2*)(L_FLG + 4 * F_HX);
    const u32 hx = (u32)__builtin_amdgcn_readfirstlane((int)f[0]), qh = (u32)__builtin_amdgcn_readfirstlane((int)f[1]);
    if (qh >= QHT) { waitvm<W>(); publish(s, s.issued - W); }
    else if (hx >= 5) { waitvm<T2>(); publish(s, s.issued - T2); }
    else { waitvm<T1>(); publish(s, s.issued - T1); }
}
template <int N> DI void drain(Ldr& s) { waitvm<N>(); publish(s, s.issued - N); if constexpr (N >= 3) drain<N - 3>(s); }
template <int W> DI void park(Ldr& s, int flag, u32 target) {
    if (flag_ld(flag) < target) { drain<W - 3>(s); wait_ge<true>(flag, target, s.dead); }
    CBAR();
}
template <int W, int NTL> DI void seg3(Ldr& s, u32 lds0, const u32 (&v)[3], rsrc_t rs, u32 soff0, u32 sstep, int ng) {
    u32 so = soff0, ld = lds0;
#pragma unroll 1
    for (int g = 0; g < ng; ++g) {
        dma3<NTL>(ld, v[0], v[1], v[2], rs, so);
        so += sstep; ld += 3072u;
        step3<W>(s);
    }
}

extern __shared__ __attribute__((aligned(16))) char smem_dyn[];

extern "C" __global__ void __launch_bounds__(640, 3)
adaln_mla_ffn_kernel(const void* __restrict__ p_x, const void* __restrict__ p_ckv, const void* __restrict__ p_wq, const void* __restrict__ p_wuk,
                     const void* __restrict__ p_wuv, const void* __restrict__ p_wo, const void* __restrict__ p_w1, const void* __restrict__ p_w2,
                     const void* __restrict__ p_c, const void* __restrict__ p_wmod, void* __restrict__ p_out, void* __restrict__ p_ws)
{
    const int tid = threadIdx.x, lane = tid & 63;
    const int wave = __builtin_amdgcn_readfirstlane(tid >> 6);
    const int b = blockIdx.x, x = b & 7, r = b >> 3;
    const bool typeA = r < 20;
    const int g = lane >> 4, i16 = lane & 15, q4 = i16 >> 2, p4 = lane & 3;
    bool dead = false;
    (void)smem_dyn;
    asm volatile("" ::"s"(p_x), "s"(p_ckv), "s"(p_wq), "s"(p_wuk), "s"(p_wuv), "s"(p_wo), "s"(p_w1), "s"(p_w2), "s"(p_c), "s"(p_wmod), "s"(p_out), "s"(p_ws));
    const rsrc_t rWS = mkrs(p_ws, W_END);
    const rsrc_t rX = mkrs(p_x, M_ * 2);
    const bool doS0 = typeA && wave < 5;
    const u32 cg = (x & 1) * 20 + r, kp = x >> 1;
    u32x4 m0v, m1v, xa; u32 cv = 0, xb = 0; u32x2 ep;
    if (doS0) {
        const rsrc_t rc = mkrs(p_c, M_ * 2), rm = mkrs(p_wmod, M_ * 6 * M_ * 2);
        const u32 row0 = 160 * kp + 32 * wave + 2 * (lane >> 2);
        const u32 off = row0 * 7680u + cg * 64u + (lane & 3) * 16u;
        m0v = ld128<NT_S0>(rm, off); m1v = ld128<NT_S0>(rm, off + 7680u);
        cv = ld32<0>(rc, row0 * 2);
    }
    if (wave == 5) { xa = ld128<0>(rX, lane * 16); xb = ld32<0>(rX, 1024 + lane * 4); }
    ep[0] = 0; ep[1] = 0;
    if (lane == 0 && wave != 8) ep = ld64<SC1>(rWS, W_EPOCH);
    if (tid >= 384 && tid < 401) { u32x4 z; z[0] = 0; z[1] = 0; z[2] = 0; z[3] = 0; lds_st<u32x4>(tid < 400 ? L_FLG + (tid - 384) * 16 : L_LAND, z); }
    asm volatile("s_waitcnt lgkmcnt(0)\n\ts_barrier" ::: "memory");
    TCAP(tbw);

    if (wave == 8) {
        Ldr s; s.issued = 0; s.pub = 0; s.ladr = L_LAND + 4 * lane; s.dead = false;
        __builtin_amdgcn_s_setprio(LPRIO);
        u32 v1[3], vt[3], vu[3];
#pragma unroll
        for (int k = 0; k < 3; ++k) {
            const u32 p = 64 * k + lane;
            const u32 row = p / 12, ch = (p % 12) ^ (((row >> 2) & 1) << 1);
            v1[k] = row * 12288u + ch * 16 - 1024u * k; vt[k] = row * 1536u + ch * 16 - 1024u * k;
            const u32 r2 = p / 24, c2 = (p % 24) ^ (((r2 >> 1) & 3) << 1);
            vu[k] = r2 * 12288u + c2 * 16 - 1024u * k;
        }
        const rsrc_t rq = mkrs(p_wq, M_ * H_ * D_ * 2), ruk = mkrs(p_wuk, DC * H_ * D_ * 2), ruv = mkrs(p_wuv, DC * H_ * D_ * 2), rkv = mkrs(p_ckv, SK * DC * 2);
        const u32 ks = r >> 3, h1 = 4 * x + ((r >> 1) & 3), half = r & 1;
        const u32 so1 = 160u * ks * 12288u + (h1 * 192 + 96 * half) * 2;
        TCAP(tb8);
        seg3<WIN, NT_Q>(s, L_S1W, v1, rq, so1, 16u * 12288u, 10);
        TS(44); TSV(51, t_entry); TSV(54, tb8);
        const u32 so2 = 96u * x * 12288u + (u32)r * 384u;
#pragma unroll 1
        for (int gi = 0; gi < 12; ++gi) {
            dma3<NT_UK>(L_X + 3072u * gi, vu[0], vu[1], vu[2], ruk, so2 + 8u * 12288u * gi);
            step3f<WINT, TH1, TH2>(s);
        }
        TS(45);
        const u32 jt = x + 8 * (r & 15);
        const u32 sot = 64u * jt * 1536u;
#pragma unroll 1
        for (int gi = 0; gi < 32; ++gi) {
            if (gi == 20) park<WMAX>(s, F_S1DONE, 6);
            dma3<NT_KV>(L_A + BLK * (gi >> 2) + 3072u * (gi & 3), vt[0], vt[1], vt[2], rkv, sot + 192u * (gi >> 2) + 16u * 1536u * (gi & 3));
            step3f<WINT, TH1, TH2>(s);
        }
        TS(46);
        if (PAUSE) { park<WMAX>(s, F_QAOK, 8); TS(47); }
        park<WMAX>(s, F_S2DONE, 6);
        seg3<WIN2, NT_UV>(s, L_X, vu, ruv, so2, 8u * 12288u, 12);
        TS(48);
        park<WMAX>(s, F_BLK, 8);
        if (typeA) {
            const rsrc_t ro = mkrs(p_wo, H_ * D_ * M_ * 2);
            const u32 row = lane >> 2, ch = (lane & 3) ^ (((row >> 3) & 1) << 1);
            const u32 vf = row * 1280u + ch * 16;
            const u32 vo[3] = {vf, vf + 16u * 1280u - 1024u, vf + 32u * 1280u - 2048u};
#pragma unroll 1
            for (int w = 0; w < 8; ++w) {
                if (w == OGA) { park<WMAX>(s, F_AM, 8); TS(59); }
                seg3<WIN2, NT_O>(s, L_A + BLK * w + B_W, vo, ro, (768u * x + 96u * w) * 1280u + 64u * r, 48u * 1280u, 2);
            }
        } else {
            const rsrc_t r1 = mkrs(p_w1, M_ * DFF * 2), r2 = mkrs(p_w2, DFF * M_ * 2);
            const u32 fb = x * 12 + (r - 20);
            const u32 va = (lane >> 1) * 3072u + (lane & 1) * 16;
            const u32 v1a[3] = {va, va + 32u * 3072u - 1024u, lane < 32 ? va + 64u * 3072u - 2048u : OOB};
            u32 vb[3];
#pragma unroll
            for (int k = 0; k < 3; ++k) { const u32 p = 64 * k + lane; vb[k] = p < 160 ? (p / 10) * 1280u + (p % 10) * 16 - 1024u * k : OOB; }
#pragma unroll 1
            for (int w = 0; w < 8; ++w) {
                const u32 ld = L_A + BLK * w;
                if (w == OGB) { park<WMAX>(s, F_AM, 8); TS(59); }
                dma3<NT_W1>(ld + B_W, v1a[0], v1a[1], v1a[2], r1, 80u * w * 3072u + fb * 32u);
                step3<WIN2>(s);
                dma3<NT_W2>(ld + B_W2, vb[0], vb[1], vb[2], r2, 16u * fb * 1280u + 160u * w);
                step3<WIN2>(s);
            }
        }
        TS(49);
        drain<WMAX - 3>(s);
        TS(50);
        TSDUMP();
        return;
    }

    if (wave == 9) {
        u32x4 sv; sv[0] = SENT; sv[1] = SENT; sv[2] = SENT; sv[3] = SENT;
        {
            const u32 c0 = (u32)b * SMALL_PER + lane, c1 = c0 + 64;
            st128<SC1>(sv, rWS, c0 < SMALL_CH ? W_SMALL + c0 * 16 : OOB);
            st128<SC1>(sv, rWS, (lane + 64 < (int)SMALL_PER && c1 < SMALL_CH) ? W_SMALL + c1 * 16 : OOB);
        }
        {
            u64 pc; u32 before, len;
            asm volatile("s_getpc_b64 %0\n.Lpf%=:\n\ts_mov_b32 %1, .Lpf%=-" KNAME "\n\ts_mov_b32 %2, " FEND "-" KNAME : "=s"(pc), "=s"(before), "=s"(len));
            const rsrc_t rcode = mkrs((const void*)(pc - before), len < 65536u ? len : 0u);
            const u32x4 c0 = ld128<0>(rcode, (u32)r * 2048u + lane * 16u), c1 = ld128<0>(rcode, (u32)r * 2048u + 1024u + lane * 16u);
            asm volatile("" ::"v"(c0), "v"(c1));
        }
        const int it = 255 - b;
        const rsrc_t rc = mkrs(p_c, M_ * 2), rm = mkrs(p_wmod, M_ * 6 * M_ * 2);
        u32 cs[5];
        if (it < 160) {
#pragma unroll
            for (int k = 0; k < 5; ++k) cs[k] = ld16(rc, (320 * (it & 1) + 64 * k + lane) * 2);
        }
        TCAP(tb9);
        waitvm<0>();
        TSV(52, t_entry); TSV(55, tb9);
        const u64 tag = (((u64)(u32)__builtin_amdgcn_readfirstlane((int)ep[1]) << 32) | (u32)__builtin_amdgcn_readfirstlane((int)ep[0])) + 1ull;
        u32x2 tg; tg[0] = (u32)tag; tg[1] = (u32)(tag >> 32);
        if (lane == 0) st64<SC1>(tg, rWS, W_FA + 8 * b);
        TS(39);
        wait_ge<true>(F_HX, 5, dead);
        {
            u32 spins = 0;
            for (;;) {
                CBAR();
                const u32x4 a = ld128<SC1>(rWS, W_FA + lane * 16), c = ld128<SC1>(rWS, W_FA + 1024 + lane * 16);
                const bool ok = a[0] == tg[0] && a[1] == tg[1] && a[2] == tg[0] && a[3] == tg[1] && c[0] == tg[0] && c[1] == tg[1] && c[2] == tg[0] && c[3] == tg[1];
                if (all_ok(ok)) break;
                POLL_STEP();
            }
            flag_st(F_RDYA, 1, lane);
            TS(40);
        }
        if (it < 160) {
            const int cb = it >> 1, kh = it & 1;
#pragma unroll
            for (int k = 0; k < 5; ++k) {
                const float cv = __uint_as_float(cs[k] << 16);
                lds_st<float>(L_SIL + (64 * k + lane) * 4, cv * __builtin_amdgcn_rcpf(1.f + __expf(-cv)));
            }
            if (MRAT == 2) wait_ge<true>(F_AM, 8, dead); else if (MRAT == 3) wait_ge<true>(F_SADD, 8, dead); else if (MRAT) wait_ge<true>(F_LAND, T_TILE0 + 96, dead); else wait_ge<true>(F_QAOK, 8, dead);
            TS(42);
            u32x4 wv[20];
            const u32 vo = (u32)(320 * kh + (lane >> 2)) * 7680u + (1280u + 32u * cb) * 2u + (lane & 3) * 16u;
#pragma unroll
            for (int k = 0; k < 20; ++k) wv[k] = ld128<NT_MR>(rm, vo + (u32)k * 16u * 7680u);
            float acc[8];
#pragma unroll
            for (int j = 0; j < 8; ++j) acc[j] = 0.f;
#pragma unroll
            for (int k = 0; k < 20; ++k) {
                const float sl = lds_ld<float>(L_SIL + (16 * k + (lane >> 2)) * 4);
#pragma unroll
                for (int j = 0; j < 4; ++j) { acc[2 * j] += sl * f_lo(wv[k][j]); acc[2 * j + 1] += sl * f_hi(wv[k][j]); }
            }
            float t4[4], t2[2];
#pragma unroll
            for (int j = 0; j < 4; ++j) t4[j] = sc32(acc[j], acc[j + 4]);
#pragma unroll
            for (int j = 0; j < 2; ++j) t2[j] = sc16(t4[j], t4[j + 2]);
#pragma unroll
            for (int j = 0; j < 2; ++j) { t2[j] += dppf<ROR(8)>(t2[j]); t2[j] += dppf<ROR(4)>(t2[j]); }
            const u32 pm = cb / 20;
#pragma unroll
            for (int j = 0; j < 2; ++j) {
                const u32 col = (cb % 20) * 32u + 8 * (lane & 3) + 4 * (lane >> 5) + 2 * ((lane >> 4) & 1) + j;
                const u32 cw = col / 80, cc = col % 80;
                const u32 pos = cc < 64 ? (cc & 7) * 8 + (cc >> 3) : 64 + (cc & 7) * 2 + ((cc >> 3) & 1);
                st32<SC1>(san(__float_as_uint(t2[j])), rWS, (lane & 12) == 0 ? W_MR + ((kh * 8u + cw) * 80u + pos) * 16u + pm * 4u : OOB);
            }
            TS(43);
        }
        TSDUMP();
        return;
    }

    const int w = wave;
    const u32 ks = r >> 3;
    const bool front = w < 5;
    if (w == 0) TSV(53, tbw);
    if (w == 5) TSV(56, tbw);
    if (w == 7) { TSV(57, tbw); TSV(58, t_entry); }
    if (w == 0) TSV(30, t_entry);
    if (w == 0) TS(0);
    const u64 tag = (((u64)(u32)__builtin_amdgcn_readfirstlane((int)ep[1]) << 32) | (u32)__builtin_amdgcn_readfirstlane((int)ep[0])) + 1ull;
    const u32 T1 = (u32)tag, T2 = (u32)(tag >> 32);
    const u32 tagl = T1 & 0xFFu, tg12 = T1 & 0xFFFu;

    if (doS0) {
        const float cA = f_lo(cv), cB = f_hi(cv);
        const float sA = cA * __builtin_amdgcn_rcpf(1.f + __expf(-cA)), sB = cB * __builtin_amdgcn_rcpf(1.f + __expf(-cB));
        const u32 sph = pk2(sA, sB), spl = pk2(sA - f_lo(sph), sB - f_hi(sph));
        float acc[8];
#pragma unroll
        for (int j = 0; j < 4; ++j) {
            const u32 pa = __builtin_amdgcn_perm(m1v[j], m0v[j], 0x05040100u), pb = __builtin_amdgcn_perm(m1v[j], m0v[j], 0x07060302u);
            acc[2 * j] = dot2(pa, spl, dot2(pa, sph, 0.f));
            acc[2 * j + 1] = dot2(pb, spl, dot2(pb, sph, 0.f));
        }
        if (w == 0) TS(31);
        float t4[4]; f32x2 t2;
#pragma unroll
        for (int j = 0; j < 4; ++j) t4[j] = sc32(acc[j], acc[j + 4]);
#pragma unroll
        for (int j = 0; j < 2; ++j) { float t = sc16(t4[j], t4[j + 2]); t += dppf<ROR(8)>(t); t += dppf<ROR(4)>(t); t2[j] = t; }
        if ((lane & 12) == 0) lds_st<f32x2>(L_S0P + (w * 32 + 8 * (lane & 3) + 4 * (lane >> 5) + 2 * ((lane >> 4) & 1)) * 4, t2);
        if (flag_add_ret(F_S0CNT, lane) == 4) {
            f32x4 v = lds_ld<f32x4>(L_S0P + (lane & 7) * 16);
#pragma unroll
            for (int p = 1; p < 5; ++p) v += lds_ld<f32x4>(L_S0P + p * 128 + (lane & 7) * 16);
            u32x4 o;
#pragma unroll
            for (int j = 0; j < 4; ++j) o[j] = tagw(v[j], tg12);
            const u32 wh = cg >= 20 ? 1u : 0u, m0 = 32 * (cg - 20 * wh);
            st128<SC1>(o, rWS, lane < 8 ? W_G1 + ((wh * 4 + kp) * 640u + m0 + 4 * lane) * 4u : OOB);
            TS(1);
        }
    }

    const u32 h1 = 4 * x + ((r >> 1) & 3), half = r & 1;
    u32x4 bw[5];
    auto read_bw = [&]() {
        wait_ge<true>(F_LAND, T_S1, dead);
        const u32 ab = L_S1W + (4 * g + q4) * 192 + (((2 * w + (p4 >> 1)) ^ ((g & 1) << 1)) * 16) + 8 * (p4 & 1);
#pragma unroll
        for (int t = 0; t < 5; ++t) bw[t] = cat(trd(ab + (32 * t) * 192), trd(ab + (32 * t + 16) * 192));
        asm volatile("s_waitcnt lgkmcnt(0)" ::: "memory");
        flag_add(F_S1DONE, lane);
        if (w == 0) TS(32);
    };
    if (w == 5) {
        float xv[10];
#pragma unroll
        for (int k = 0; k < 4; ++k) { xv[2 * k] = f_lo(xa[k]); xv[2 * k + 1] = f_hi(xa[k]); }
        xv[8] = f_lo(xb); xv[9] = f_hi(xb);
        float sum = 0.f;
#pragma unroll
        for (int k = 0; k < 10; ++k) sum += xv[k];
        const float mu = wave_sum(sum) * (1.f / 640.f);
        float sq = 0.f;
#pragma unroll
        for (int k = 0; k < 10; ++k) { xv[k] -= mu; sq += xv[k] * xv[k]; }
        const float rstd = 1.f / sqrtf(wave_sum(sq) * (1.f / 640.f) + 1e-5f);
        f32x4 n0, n1; f32x2 n2;
#pragma unroll
        for (int k = 0; k < 4; ++k) { n0[k] = xv[k] * rstd; n1[k] = xv[4 + k] * rstd; }
        n2[0] = xv[8] * rstd; n2[1] = xv[9] * rstd;
        lds_st<f32x4>(L_XN + lane * 32, n0); lds_st<f32x4>(L_XN + lane * 32 + 16, n1); lds_st<f32x2>(L_XN + 2048 + lane * 8, n2);
        flag_add(F_XN, lane);
        read_bw();
    }
    if (front) {
        read_bw();
        wait_ge<true>(F_XN, 1, dead);
        const f32x4 xn4 = lds_ld<f32x4>(L_XN + (160 * ks + 32 * w + 4 * (lane & 7)) * 4);
        float aj[4], bj[4];
#pragma unroll
        for (int j = 0; j < 4; ++j) { aj[j] = lane >= 32 ? xn4[j] : 1.f; bj[j] = lane < 8 ? xn4[j] : 0.f; }
        const u32 goff = W_G1 + ((u32)(lane >> 3) * 640u + 160 * ks + 32 * w + 4 * (lane & 7)) * 4u;
        const u32 hadr = L_HX + ((lane >> 3) & 1) * 320 + (32 * w + 8 * (lane & 3) + 4 * ((lane >> 2) & 1) + 2 * (lane >> 5) + ((lane >> 4) & 1)) * 2;
        PSet<1> gv, pa, pb, pc;
        ppoll<1, STG>(gv, pa, pb, pc,
            [&](PSet<1>& s) { s.v[0] = ld128<SC1>(rWS, goff); },
            [&](const PSet<1>& s) { return all_ok(tag_ok(s.v[0], tg12)); }, dead);
        if (w == 4) TS(2);
        float t[4];
#pragma unroll
        for (int j = 0; j < 4; ++j) t[j] = aj[j] * untag(gv.v[0][j]) + bj[j];
        float hv = sc16(sc32(t[0], t[2]), sc32(t[1], t[3]));
        hv += dppf<ROR(8)>(hv);
        const u32 hi = bf1(hv), lo = bf1(hv - __uint_as_float(hi << 16));
        lds_st<unsigned short>(hadr, (unsigned short)((lane & 8) ? lo : hi));
        flag_add(F_HX, lane);
        retire(pa, pb, pc);
    }
    u32x4 bw2[6];
    if (w < 6) {
        wait_ge<false>(F_HX, 5, dead);
        f32x4 acc = {0.f, 0.f, 0.f, 0.f};
#pragma unroll
        for (int t = 0; t < 5; ++t) acc = mfma32(lds_ld<u32x4>(L_HX + (lane & 1) * 320 + 64 * t + 16 * g), bw[t], acc);
        const u32 qb = tagw(acc[0] + acc[1], tg12);
        u32x4 o; o[0] = qb; o[1] = dppu<SHL(1)>(qb); o[2] = dppu<SHL(2)>(qb); o[3] = dppu<SHL(3)>(qb);
        st128<SC1>(o, rWS, (lane & 51) == 0 ? W_Q2 + (ks * 6144u + h1 * 192 + 96 * half + 16 * w + lane) * 4u : OOB);
        if (w == 0) TS(3);
        wait_ge<true>(F_LAND, T_S2, dead);
        const u32 rowb = L_X + (16 * w + i16) * 384, sw = ((i16 >> 1) & 3) << 1;
#pragma unroll
        for (int t = 0; t < 6; ++t) bw2[t] = lds_ld<u32x4>(rowb + (((4 * t + g) ^ sw) * 16));
        flag_add(F_S2DONE, lane);
        if (w == 0) TS(33);
    }
    if (w >= 6) {
        wait_ge<true>(F_HX, 5, dead);
        const u32 c0 = 128 * (w - 6) + 4 * i16;
        const u32 qo = W_Q2 + ((u32)g * 6144u + (u32)r * 192u + c0) * 4u, qo2 = w == 6 ? qo + 256 : qo;
        const u32 qadr = L_QH + (c0 + 2 * (lane >> 5) + ((lane >> 4) & 1)) * 2;
        PSet<2> qv1, pa, pb, pc;
        ppoll<2, STG>(qv1, pa, pb, pc,
            [&](PSet<2>& s) { s.v[0] = ld128<SC1>(rWS, qo); if (w == 6) s.v[1] = ld128<SC1>(rWS, qo2); else s.v[1] = s.v[0]; },
            [&](const PSet<2>& s) { return all_ok(tag_ok(s.v[0], tg12) && tag_ok(s.v[1], tg12)); }, dead);
        if (w == 7) TS(4);
#pragma unroll
        for (int k = 0; k < 2; ++k) {
            if (k == 0 || w == 6) {
                const float q = sc16(sc32(untag(qv1.v[k][0]), untag(qv1.v[k][2])), sc32(untag(qv1.v[k][1]), untag(qv1.v[k][3])));
                const u32 hi = bf1(q), lo = bf1(q - __uint_as_float(hi << 16));
                lds_st<unsigned short>(qadr + 128 * k, (unsigned short)hi); lds_st<unsigned short>(qadr + 128 * k + 384, (unsigned short)lo);
            }
        }
        flag_add(F_QH, lane);
        retire(pa, pb, pc);
    }
    if (w < 6) {
        wait_ge<false>(F_QH, QHT, dead);
        f32x4 a0 = {0.f, 0.f, 0.f, 0.f}, a1 = {0.f, 0.f, 0.f, 0.f};
#pragma unroll
        for (int t = 0; t < 6; t += 2) {
            a0 = mfma32(lds_ld<u32x4>(L_QH + (lane & 1) * 384 + 64 * t + 16 * g), bw2[t], a0);
            a1 = mfma32(lds_ld<u32x4>(L_QH + (lane & 1) * 384 + 64 * (t + 1) + 16 * g), bw2[t + 1], a1);
        }
        const float qa = ((a0[0] + a0[1]) + (a1[0] + a1[1])) * (0.07216878364870322f * 1.4426950408889634f);
        const u32 qh = bf1(qa), ql = bf1(qa - __uint_as_float(qh << 16));
        const u32x4 o = pack_row((QLO && (g & 1)) ? ql : qh);
        const u32 dst = QLO ? W_QA + ((u32)r * 8 + x) * 384u + (w >> 1) * 128 + g * 64 + (w & 1) * 32 + ((lane >> 3) & 1) * 16
                            : W_QA + ((u32)r * 8 + x) * 256u + w * 32 + ((lane >> 3) & 1) * 16;
        wait_ge<false>(F_RDYA, 1, dead);
        st128<SC1>(o, rWS, (lane & (QLO ? 39 : 55)) == 0 ? dst : OOB);
        flag_add(F_QAST, lane);
        if (w == 0) TS(5);
    }

    const u32 g2 = r >> 4;
    const u32 blk = L_A + BLK * w;
    const u32 t_blk = T_TILE0 + 12 * (w + 1);
    u32x4 vt[6][2];
    {
        constexpr int NQ = QLO ? 6 : 3;
        u32x4 bq[NQ];
        const u32 qoff = W_QA + ((16 * g2 + i16) * 8 + w) * (QLO ? 384u : 256u) + g * 16u;
        auto issue_q = [&]() {
            CBAR();
#pragma unroll
            for (int k = 0; k < NQ; ++k) bq[k] = ld128<SC1>(rWS, qoff + k * 64);
            CBAR();
        };
        bool seen = false, gate = false, landed = false; u32 tq = 0;
        auto gate_step = [&]() {
            if (!seen) { if (flag_ld(F_QAST) >= 6) { seen = true; tq = (u32)__builtin_amdgcn_s_memtime(); } }
            else gate = (u32)__builtin_amdgcn_s_memtime() - tq >= QDLY * 64u;
        };
        wait_ge<false>(F_RDYA, 1, dead);
        for (u32 n = 0; !dead;) {
            landed = flag_ld(F_LAND) >= t_blk;
            if (landed) break;
            gate_step();
            if (gate) break;
            if (++n > CAP_L) dead = true;
            __builtin_amdgcn_s_sleep(1);
        }
        if (!landed) { issue_q(); wait_ge<true>(F_LAND, t_blk, dead); }
        if (w == 0) TS(6);
        if (w == 7) TS(27);
        u32x4 av[4][3];
        const u32 ar = blk + i16 * 192, asw = ((i16 >> 2) & 1) << 1;
#pragma unroll
        for (int kb = 0; kb < 4; ++kb)
#pragma unroll
            for (int t = 0; t < 3; ++t) av[kb][t] = lds_ld<u32x4>(ar + kb * 16 * 192 + ((4 * t + g) ^ asw) * 16);
        const u32 vb = blk + (4 * g + q4) * 192 + (p4 >> 1) * 16 + 8 * (p4 & 1);
#pragma unroll
        for (int u = 0; u < 6; ++u) {
            const u32 cu = vb + (u ^ (g & 1)) * 32;
#pragma unroll
            for (int t = 0; t < 2; ++t) vt[u][t] = cat(trd(cu + (32 * t) * 192), trd(cu + (32 * t + 16) * 192));
        }
        asm volatile("s_waitcnt lgkmcnt(0)" ::: "memory");
        flag_add(F_BLK, lane);
        if (w == 7) TS(28);
        if (landed) {
            for (u32 n = 0; !dead && !gate;) { gate_step(); if (++n > CAP_L) dead = true; if (!gate) __builtin_amdgcn_s_sleep(1); }
            issue_q();
        }
        {
            u32 spins = 0;
            for (;;) {
                u32 mx = 0;
#pragma unroll
                for (int k = 0; k < NQ; ++k) mx = mx4(mx, bq[k]);
                if (none_sent(mx)) break;
                POLL_STEP();
                if (QMASK) { if (mx == SENT) issue_q(); } else issue_q();
            }
        }
        flag_add(F_QAOK, lane);
        if (w == 0) TS(7);
        if (w == 7) TS(26);
#pragma unroll
        for (int kb = 0; kb < 4; ++kb) {
            f32x4 acc = {0.f, 0.f, 0.f, 0.f};
#pragma unroll
            for (int t = 0; t < 3; ++t) { if (QLO) { acc = mfma32(av[kb][t], bq[(2 * t + 1) % NQ], acc); acc = mfma32(av[kb][t], bq[(2 * t) % NQ], acc); } else acc = mfma32(av[kb][t], bq[t], acc); }
            f32x2 lo2, hi2; lo2[0] = acc[0]; lo2[1] = acc[1]; hi2[0] = acc[2]; hi2[1] = acc[3];
            lds_st<f32x2>(blk + (kb * 2) * 512 + lane * 8, lo2);
            lds_st<f32x2>(blk + (kb * 2 + 1) * 512 + lane * 8, hi2);
        }
        flag_add(F_SADD, lane);
        if (w == 0) TS(20);
    }
    {
        wait_ge<false>(F_SADD, 8, dead);
        if (w == 0) TS(21);
        f32x2 s = {0.f, 0.f};
#pragma unroll
        for (int p = 0; p < 8; ++p) s += lds_ld<f32x2>(L_A + BLK * p + w * 512 + lane * 8);
        lds_st<f32x2>(L_SC + (w >> 1) * 1024 + lane * 16 + (w & 1) * 8, s);
        flag_add(F_SC, lane);
        wait_ge<false>(F_SC, 8, dead);
        if (w == 0) TS(22);
        f32x4 sc[4];
#pragma unroll
        for (int q = 0; q < 4; ++q) sc[q] = lds_ld<f32x4>(L_SC + q * 1024 + lane * 16);
        float mloc = fmaxf(fmaxf(fmaxf(sc[0][0], sc[0][1]), fmaxf(sc[0][2], sc[0][3])), fmaxf(fmaxf(sc[1][0], sc[1][1]), fmaxf(sc[1][2], sc[1][3])));
        mloc = fmaxf(mloc, fmaxf(fmaxf(fmaxf(sc[2][0], sc[2][1]), fmaxf(sc[2][2], sc[2][3])), fmaxf(fmaxf(sc[3][0], sc[3][1]), fmaxf(sc[3][2], sc[3][3]))));
        mloc = xg_max(mloc);
        u32x4 pf[2];
#pragma unroll
        for (int t = 0; t < 2; ++t)
#pragma unroll
            for (int e = 0; e < 4; ++e) {
                const f32x4 sq = sc[2 * t + (e >> 1)];
                pf[t][e] = pk2(__builtin_amdgcn_exp2f(sq[2 * (e & 1)] - mloc), __builtin_amdgcn_exp2f(sq[2 * (e & 1) + 1] - mloc));
            }
        f32x4 oa[6], lz;
#pragma unroll
        for (int u = 0; u < 6; ++u) { f32x4 z = {0.f, 0.f, 0.f, 0.f}; oa[u] = mfma32(vt[u][0], pf[0], z); oa[u] = mfma32(vt[u][1], pf[1], oa[u]); }
        {
            u32x4 one; one[0] = 0x3F803F80u; one[1] = 0x3F803F80u; one[2] = 0x3F803F80u; one[3] = 0x3F803F80u;
            f32x4 z = {0.f, 0.f, 0.f, 0.f};
            lz = mfma32(one, pf[0], z); lz = mfma32(one, pf[1], lz);
        }
        if (w == 0) TS(23);
        const u32 jj = x * 16 + (r & 15);
        const u32 ob = W_O + ((w * 128 + jj) * 2 + g2) * 3072u + (i16 * 4 + g) * 16;
        u32 cx = 0;
#pragma unroll
        for (int k = 0; k < 3; ++k) {
            u32x4 d;
            d[0] = pk2(oa[2 * k][0], oa[2 * k][1]); d[1] = pk2(oa[2 * k][2], oa[2 * k][3]);
            d[2] = pk2(oa[2 * k + 1][0], oa[2 * k + 1][1]); d[3] = pk2(oa[2 * k + 1][2], oa[2 * k + 1][3]);
            st128<SC1>(d, rWS, ob + k * 1024);
            cx ^= (d[0] ^ d[1]) ^ (d[2] ^ d[3]);
        }
        cx = xg_xor(cx);
        u32x4 pk;
        pk[0] = __float_as_uint(mloc); pk[1] = __float_as_uint(lz[0]);
        pk[2] = cx ^ pk[0] ^ pk[1] ^ T2; pk[3] = T1;
        st128<SC1>(pk, rWS, g == 0 ? W_PK + ((w * 128 + jj) * 32 + 16 * g2 + i16) * 16 : OOB);
        if (w == 0) TS(8);
    }

    {
        const u32 poff = W_PK + ((x * 128 + 16 * w + 4 * g + (i16 & 3)) * 32 + r) * 16;
        const u32 ooff = i16 < 12 ? W_O + ((x * 128 + 16 * w + 4 * g) * 2 + g2) * 3072u + (i16 >> 2) * 1024 + ((r & 15) * 4 + (i16 & 3)) * 16 : OOB;
        u32x4 bw[2][3];
        if (w < 6) {
            wait_ge<true>(F_LAND, T_UV, dead);
            const u32 sw = 2 * (g & 1) + (q4 >> 1);
#pragma unroll
            for (int d = 0; d < 2; ++d) {
                const u32 ub = L_X + (4 * g + q4) * 384 + (2 * ((2 * w + d) ^ sw) + (p4 >> 1)) * 16 + 8 * (p4 & 1);
#pragma unroll
                for (int t = 0; t < 3; ++t) bw[d][t] = cat(trd(ub + (32 * t) * 384), trd(ub + (32 * t + 16) * 384));
            }
        }
        if (ODLY) __builtin_amdgcn_s_sleep(ODLY);
        u32x4 pk, od[4];
        {
            u32 spins = 0;
            for (;;) {
                CBAR(); pk = ld128<SC1>(rWS, poff); CBAR();
                if (all_ok(pk[3] == T1)) break;
                POLL_STEP();
            }
        }
        if (w == 0) TS(35);
        {
            u32 spins = 0;
            u64 bad = ~0ull;
            for (;;) {
                CBAR();
#pragma unroll
                for (int k = 0; k < 4; ++k) if (bad & (0x1111111111111111ull << k)) od[k] = ld128<SC1>(rWS, ooff + k * 6144);
                if (!ELIGHT || spins) pk = ld128<SC1>(rWS, poff);
                CBAR();
                u32 c[4];
#pragma unroll
                for (int k = 0; k < 4; ++k) c[k] = (od[k][0] ^ od[k][1]) ^ (od[k][2] ^ od[k][3]);
                const bool b0 = lane & 1, b1 = lane & 2;
                const u32 t01 = (b0 ? c[1] : c[0]) ^ dppu<QX1>(b0 ? c[0] : c[1]), t23 = (b0 ? c[3] : c[2]) ^ dppu<QX1>(b0 ? c[2] : c[3]);
                u32 t = (b1 ? t23 : t01) ^ dppu<QX2>(b1 ? t01 : t23);
                t ^= dppu<ROR(4)>(t); t ^= dppu<ROR(8)>(t);
                const bool ok = pk[3] == T1 && (t ^ pk[0] ^ pk[1] ^ T2) == pk[2];
                bad = __builtin_amdgcn_ballot_w64(!ok);
                if (bad == 0ull) break;
                POLL_STEP();
            }
        }
        if (w == 0) TS(9);
        const float mj = __uint_as_float(pk[0]);
        float Mw = fmaxf(mj, dppf<QX1>(mj)); Mw = fmaxf(Mw, dppf<QX2>(Mw)); Mw = xg_max(Mw);
        const u32 wb = bf1(__builtin_amdgcn_exp2f(mj - Mw));
        const u32 sh = (lane & 1) ? 65536u : 1u;
        u32x4 aw;
        aw[0] = __umul24(dppu<0x00>(wb), sh); aw[1] = __umul24(dppu<0x55>(wb), sh); aw[2] = __umul24(dppu<0xAA>(wb), sh); aw[3] = __umul24(dppu<0xFF>(wb), sh);
        u32x4 yv;
#pragma unroll
        for (int u = 0; u < 4; ++u) {
            u32x4 bo; bo[0] = od[0][u]; bo[1] = od[1][u]; bo[2] = od[2][u]; bo[3] = od[3][u];
            f32x4 z = {0.f, 0.f, 0.f, 0.f};
            z = mfma32(aw, bo, z);
            yv[u] = pk2(z[0], z[1]);
        }
        float lw = __uint_as_float(wb << 16) * __uint_as_float(pk[1]);
        lw += dppf<QX1>(lw); lw += dppf<QX2>(lw); lw = xg_sum(lw);
        if (lane < 12) lds_st<u32x4>(L_AM + w * 208 + lane * 16, yv);
        { f32x2 ml; ml[0] = Mw; ml[1] = lw; if (lane == 0) lds_st<f32x2>(L_EML + w * 8, ml); }
        flag_add(F_AM, lane);
        if (w < 6) {
            wait_ge<false>(F_AM, 8, dead);
            u32x4 aa[3];
#pragma unroll
            for (int t = 0; t < 3; ++t) aa[t] = lds_ld<u32x4>(L_AM + (i16 & 7) * 208 + (4 * t + g) * 16);
            const f32x4 e0 = lds_ld<f32x4>(L_EML + (g & 1) * 32), e1 = lds_ld<f32x4>(L_EML + (g & 1) * 32 + 16);
            float Mx = fmaxf(fmaxf(e0[0], e0[2]), fmaxf(e1[0], e1[2]));
            { float a = Mx, bb = Mx; swap16(a, bb); Mx = fmaxf(a, bb); }
            const float f0 = __builtin_amdgcn_exp2f(e0[0] - Mx), f1 = __builtin_amdgcn_exp2f(e0[2] - Mx), f2 = __builtin_amdgcn_exp2f(e1[0] - Mx), f3 = __builtin_amdgcn_exp2f(e1[2] - Mx);
            const float ls = (f0 * e0[1] + f1 * e0[3]) + (f2 * e1[1] + f3 * e1[3]);
            const float rl = __builtin_amdgcn_rcpf(sc16(ls, ls));
            float res[2];
#pragma unroll
            for (int d = 0; d < 2; ++d) {
                f32x4 z = {0.f, 0.f, 0.f, 0.f};
#pragma unroll
                for (int t = 0; t < 3; ++t) z = mfma32(aa[t], bw[d][t], z);
                res[d] = (f0 * z[0] + f1 * z[1]) + (f2 * z[2] + f3 * z[3]);
            }
            const u32x4 o = pack_row(bf1(sc16(res[0], res[1]) * rl));
            const u32 dst = W_CTX + ((u32)r * 8 + x) * 384u + w * 64 + g * 32 + ((lane >> 3) & 1) * 16;
            st128<SC1>(o, rWS, (lane & 39) == 0 ? dst : OOB);
            if (w == 0) TS(10);
        }
    }

    if (typeA) {
        const u32 coff = i16 < 8 ? W_CTX + ((4 * x + (w >> 1)) * 8 + i16) * 384u + (w & 1) * 192 + g * 16 : OOB;
        wait_ge<true>(F_LAND, T_UV + 6 * (w + 1), dead);
        u32x4 bw[2][3];
        const u32 fbx = blk + B_W + (8 * g + q4) * 64 + (p4 >> 1) * 16 + 8 * (p4 & 1);
#pragma unroll
        for (int u = 0; u < 2; ++u) {
            const u32 ub = fbx + (u ^ (g & 1)) * 32;
#pragma unroll
            for (int t = 0; t < 3; ++t) bw[u][t] = cat(trd(ub + (32 * t) * 64), trd(ub + (32 * t + 4) * 64));
        }
        if (w == 0) TS(36);
        PSet<3> ca, pa, pb, pc;
        ppoll<3, STG2>(ca, pa, pb, pc,
            [&](PSet<3>& s) { s.v[0] = ld128<SC1>(rWS, coff); s.v[1] = ld128<SC1>(rWS, coff + 64); s.v[2] = ld128<SC1>(rWS, coff + 128); },
            [&](const PSet<3>& s) { return none_sent(mx4(mx4(mx4(0, s.v[0]), s.v[1]), s.v[2])); }, dead);
        if (w == 0) TS(11);
        float res[2];
#pragma unroll
        for (int u = 0; u < 2; ++u) {
            f32x4 z = {0.f, 0.f, 0.f, 0.f};
#pragma unroll
            for (int t = 0; t < 3; ++t) z = mfma32(ca.v[t], bw[u][t], z);
            res[u] = xg_sum((z[0] + z[1]) + (z[2] + z[3]));
        }
        if (g < 2) lds_st<float>(L_FRP + (w * 32 + 16 * g + i16) * 4, g == 0 ? res[0] : res[1]);
        if (flag_add_ret(F_FRC, lane) == 7) {
            float v = 0.f;
#pragma unroll
            for (int p = 0; p < 8; ++p) v += lds_ld<float>(L_FRP + (p * 32 + (lane & 31)) * 4);
            const u32x4 o = pack_row(bf1(v));
            const u32 dst = W_AT + (x * 640u + 32u * r + 16 * (g & 1) + (lane & 8)) * 2u;
            st128<SC1>(o, rWS, (g < 2 && (lane & 7) == 0) ? dst : OOB);
            TS(12);
        }
        retire(pa, pb, pc);
    } else {
        const u32 fb = x * 12 + (r - 20);
        const u32 cA = 8 * (lane & 7) + (lane >> 3);
        const bool hasB = (lane & 7) < 2;
        const u32 colA = 80 * w + cA, colB = colA + 64;
        float ga[2], sb[2], cb1[2], gb[2], xv[2];
        {
            xv[0] = __uint_as_float(ld16(rX, colA * 2) << 16); xv[1] = __uint_as_float(ld16(rX, hasB ? colB * 2 : OOB) << 16);
            u32x4 mr[2][2]; u32 spins = 0;
            const u32 posB = 64 + (lane >> 3) * 2 + (lane & 1);
            for (;;) {
                CBAR();
                u32 mx = 0;
#pragma unroll
                for (int kh = 0; kh < 2; ++kh) {
                    mr[kh][0] = ld128<SC1>(rWS, W_MR + ((kh * 8u + w) * 80u + lane) * 16u);
                    mr[kh][1] = ld128<SC1>(rWS, hasB ? W_MR + ((kh * 8u + w) * 80u + posB) * 16u : OOB);
                    mx = mx4(mx4(mx, mr[kh][0]), mr[kh][1]);
                }
                if (none_sent(mx)) break;
                POLL_STEP();
            }
#pragma unroll
            for (int c = 0; c < 2; ++c) {
                ga[c] = __uint_as_float(mr[0][c][0]) + __uint_as_float(mr[1][c][0]);
                sb[c] = __uint_as_float(mr[0][c][1]) + __uint_as_float(mr[1][c][1]);
                cb1[c] = 1.f + (__uint_as_float(mr[0][c][2]) + __uint_as_float(mr[1][c][2]));
                gb[c] = __uint_as_float(mr[0][c][3]) + __uint_as_float(mr[1][c][3]);
            }
        }
        if (w == 0) TS(13);
        const u32 gab = blk + B_GA;
#pragma unroll
        for (int c = 0; c < 2; ++c) {
            if (c == 0 || hasB) {
                const u32 cc = cA + 64 * c;
                const u32 h = bf1(cb1[c]), l = bf1(cb1[c] - __uint_as_float(h << 16)), h2 = bf1(sb[c]), l2 = bf1(sb[c] - __uint_as_float(h2 << 16));
                lds_st<unsigned short>(gab + 2 * 160 + cc * 2, (unsigned short)h); lds_st<unsigned short>(gab + 3 * 160 + cc * 2, (unsigned short)l);
                lds_st<unsigned short>(gab + 4 * 160 + cc * 2, (unsigned short)h2); lds_st<unsigned short>(gab + 5 * 160 + cc * 2, (unsigned short)l2);
                lds_st<float>(blk + B_GB + cc * 4, gb[c]);
            }
        }
        wait_ge<true>(F_LAND, T_UV + 6 * (w + 1), dead);
        u32x2 b1[5], b2[5];
#pragma unroll
        for (int t = 0; t < 5; ++t) b1[t] = trd(blk + B_W + 512 * t + 8 * lane);
#pragma unroll
        for (int u = 0; u < 5; ++u) b2[u] = trd(blk + B_W2 + (4 * g + q4) * 160 + 32 * u + 8 * p4);
        if (w == 0) TS(36);
        const u32 aoff = W_AT + ((lane >> 3) * 640u + 80u * w + 8 * (lane & 7)) * 2u, aoff2 = hasB ? aoff + 128 : OOB;
        PSet<2> at, pa, pb, pc;
        ppoll<2, STG2>(at, pa, pb, pc,
            [&](PSet<2>& s) { s.v[0] = ld128<SC1>(rWS, aoff); s.v[1] = ld128<SC1>(rWS, aoff2); },
            [&](const PSet<2>& s) { return none_sent(mx4(mx4(0, s.v[0]), s.v[1])); }, dead);
        if (w == 0) TS(14);
        float x1[2];
#pragma unroll
        for (int c = 0; c < 2; ++c) {
            const u32x4 a = at.v[c];
            float v[8];
#pragma unroll
            for (int j = 0; j < 4; ++j) { v[2 * j] = f_lo(a[j]); v[2 * j + 1] = f_hi(a[j]); }
            float t4[4], t2[2];
#pragma unroll
            for (int j = 0; j < 4; ++j) t4[j] = sc32(v[j], v[j + 4]);
#pragma unroll
            for (int j = 0; j < 2; ++j) t2[j] = sc16(t4[j], t4[j + 2]);
            const bool b3 = (lane & 8) != 0;
            const float keep = b3 ? t2[1] : t2[0], give = b3 ? t2[0] : t2[1];
            const float attn = keep + dppf<ROR(8)>(give);
            x1[c] = xv[c] + ga[c] * attn;
        }
        if (!hasB) x1[1] = 0.f;
#pragma unroll
        for (int c = 0; c < 2; ++c) {
            if (c == 0 || hasB) {
                const u32 cc = cA + 64 * c;
                const float uv = x1[c] * cb1[c];
                const u32 h = bf1(uv), l = bf1(uv - __uint_as_float(h << 16));
                lds_st<unsigned short>(gab + cc * 2, (unsigned short)h); lds_st<unsigned short>(gab + 160 + cc * 2, (unsigned short)l);
                lds_st<float>(blk + B_X1 + cc * 4, x1[c]);
            }
        }
        CBAR();
        f32x4 z = {0.f, 0.f, 0.f, 0.f};
        const u32 arow = (q4 < 2 ? q4 : 2) * 2 + (lane & 1);
#pragma unroll
        for (int t = 0; t < 5; ++t) z = mfma16(lds_ld<u32x2>(gab + arow * 160 + 32 * t + 8 * g), b1[t], z);
        const float s1 = wave_sum(x1[0] + x1[1]), s2 = wave_sum(x1[0] * x1[0] + x1[1] * x1[1]);
        lds_st<float>(L_GRP + w * 256 + lane * 4, g < 3 ? z[0] + z[1] : (p4 == 0 ? s1 : (p4 == 1 ? s2 : 0.f)));
        flag_add(F_GRC, lane);
        wait_ge<false>(F_GRC, 8, dead);
        f32x4 gs = lds_ld<f32x4>(L_GRP + (2 * p4) * 256 + q4 * 64 + g * 16) + lds_ld<f32x4>(L_GRP + (2 * p4 + 1) * 256 + q4 * 64 + g * 16);
#pragma unroll
        for (int e = 0; e < 4; ++e) { gs[e] += dppf<QX1>(gs[e]); gs[e] += dppf<QX2>(gs[e]); }
        const float mu = dppf<NBC(12)>(gs[0]) * (1.f / 640.f);
        const float var = fmaxf(dppf<NBC(12)>(gs[1]) * (1.f / 640.f) - mu * mu, 0.f);
        const float rstd = 1.f / sqrtf(var + 1e-5f);
        float av[4];
#pragma unroll
        for (int e = 0; e < 4; ++e) av[e] = fmaxf(rstd * (dppf<NBC(0)>(gs[e]) - mu * dppf<NBC(4)>(gs[e])) + dppf<NBC(8)>(gs[e]), 0.f);
        u32x2 af; { const u32 h0 = pk2(av[0], av[1]), h1b = pk2(av[2], av[3]);
            const u32 l0 = pk2(av[0] - f_lo(h0), av[1] - f_hi(h0)), l1 = pk2(av[2] - f_lo(h1b), av[3] - f_hi(h1b));
            af[0] = (lane & 1) ? l0 : h0; af[1] = (lane & 1) ? l1 : h1b; }
        float fo[5];
#pragma unroll
        for (int u = 0; u < 5; ++u) { f32x4 y = {0.f, 0.f, 0.f, 0.f}; y = mfma16(af, b2[u], y); fo[u] = y[0] + y[1]; }
        {
            const float v = g == 0 ? fo[0] : (g == 1 ? fo[1] : (g == 2 ? fo[2] : fo[3]));
            const u32x4 o = pack_row(bf1(v)), o2 = pack_row(bf1(fo[4]));
            st128<SC1>(o, rWS, (lane & 7) == 0 ? W_FP + (((5 * w + g) * 96u + fb) * 16 + (lane & 8)) * 2u : OOB);
            st128<SC1>(o2, rWS, (lane & 55) == 0 ? W_FP + (((5 * w + 4) * 96u + fb) * 16 + (lane & 8)) * 2u : OOB);
        }
        if (w == 0) TS(15);
        const int ci = (r - 20) * 8 + x;
        if (ci < 40 && ci / 5 == w) {
            const u32 fo0 = W_FP + (u32)ci * 3072u + lane * 16;
            PSet<3> fv, qa, qb, qc;
            ppoll<3, STG2>(fv, qa, qb, qc,
                [&](PSet<3>& s) { s.v[0] = ld128<SC1>(rWS, fo0); s.v[1] = ld128<SC1>(rWS, fo0 + 1024); s.v[2] = ld128<SC1>(rWS, fo0 + 2048); },
                [&](const PSet<3>& s) { return none_sent(mx4(mx4(mx4(0, s.v[0]), s.v[1]), s.v[2])); }, dead);
            TS(16);
            float v[8];
#pragma unroll
            for (int j = 0; j < 4; ++j) { v[2 * j] = (f_lo(fv.v[0][j]) + f_lo(fv.v[1][j])) + f_lo(fv.v[2][j]); v[2 * j + 1] = (f_hi(fv.v[0][j]) + f_hi(fv.v[1][j])) + f_hi(fv.v[2][j]); }
            float t4[4], t2[2];
#pragma unroll
            for (int j = 0; j < 4; ++j) t4[j] = sc32(v[j], v[j + 4]);
#pragma unroll
            for (int j = 0; j < 2; ++j) t2[j] = sc16(t4[j], t4[j + 2]);
            const bool b3 = (lane & 8) != 0;
            const float keep = b3 ? t2[1] : t2[0], give = b3 ? t2[0] : t2[1];
            float f = keep + dppf<ROR(8)>(give);
            f += dppf<SHL(4)>(f);
            f += dppf<SHL(2)>(f);
            const u32 cc = 16 * (ci % 5) + 8 * (lane & 1) + (lane >> 3);
            const float o = lds_ld<float>(blk + B_X1 + cc * 4) + lds_ld<float>(blk + B_GB + cc * 4) * f;
            const rsrc_t rO = mkrs(p_out, M_ * 2);
            __builtin_amdgcn_raw_buffer_store_b16((unsigned short)bf1(o), rO, (lane & 6) == 0 ? (16 * ci + 8 * (lane & 1) + (lane >> 3)) * 2 : OOB, 0, 0);
            if (ci == 0 && lane == 0) { u32x2 e; e[0] = T1; e[1] = T2; st64<SC1>(e, rWS, W_EPOCH); }
            TS(17);
        }
    }
    TSDUMP();
}

extern "C" __device__ __attribute__((noinline, used)) void adaln_code_end() { asm volatile("s_nop 0"); }

extern "C" void kernel_launch(void* const* d_in, const int* in_sizes, int n_in, void* d_out, int out_size, void* d_ws, size_t ws_size, hipStream_t stream)
{
    (void)in_sizes; (void)n_in; (void)out_size;
    const size_t ws_off = WSOFF < 0 ? ((ws_size - 8388608u) & ~(size_t)4095) : (size_t)WSOFF;
    (void)hipFuncSetAttribute((const void*)adaln_mla_ffn_kernel, hipFuncAttributeMaxDynamicSharedMemorySize, (int)LDS_BYTES);
    hipLaunchKernelGGL(adaln_mla_ffn_kernel, dim3(256), dim3(640), LDS_BYTES, stream,
                       (const void*)d_in[0], (const void*)d_in[1], (const void*)d_in[2], (const void*)d_in[3], (const void*)d_in[4], (const void*)d_in[5],
                       (const void*)d_in[7], (const void*)d_in[8], (const void*)d_in[9], (const void*)d_in[10], d_out, (void*)((char*)d_ws + ws_off));
}
```

```cpp
#include <hip/hip_runtime.h>
#include <hip/hip_bf16.h>
#include <stdint.h>

#define LAS __attribute__((address_space(3)))
#define DI __device__ __forceinline__
typedef unsigned int u32;
typedef unsigned long long u64;
typedef u32 u32x2 __attribute__((ext_vector_type(2)));
typedef u32 u32x3 __attribute__((ext_vector_type(3)));
typedef u32 u32x4 __attribute__((ext_vector_type(4)));
typedef float f32x2 __attribute__((ext_vector_type(2)));
typedef float f32x4 __attribute__((ext_vector_type(4)));
typedef short s16x4 __attribute__((ext_vector_type(4)));
typedef __bf16 bf16x2 __attribute__((ext_vector_type(2)));
typedef __bf16 bf16x8 __attribute__((ext_vector_type(8)));
typedef __amdgpu_buffer_rsrc_t rsrc_t;

#define WQ 30
#define UKA 6
#define TLA 99
#define TLH 99
#define WUV 18
#define OST 0
#define UVA 12
#define WIN3 18
#define WIN4 48
#define OGI 2
#define QABS 0
#define OABS 0
#define G1DLY 85
#define Q2DLY 24
#define WIN2 18
#define PAUSE 1
#define STG 6
#define STG2 3
#define NT_S0 0
#define NT_MR 2
#define NT_Q 1
#define NT_UK 1
#define NT_KV 1
#define NT_UV 1
#define NT_O 1
#define NT_W1 1
#define NT_W2 1
#define LPRIO 3
#define CPF 1
#define FEND "adaln_code_end"
#define KNAME "adaln_mla_ffn_kernel"
#define QDLY 40
#define ODLY 60
#define ELIGHT 1
#define MRAT 0
#define WINT 24
#define QLO 1
#define OGA 0
#define OGB 0
#define QMASK 1
#define WSOFF -1
#define LSLP 0
#define O_BASE 1048576u

constexpr int M_ = 640, H_ = 32, D_ = 192, DC = 768, SK = 8192, DFF = 1536;
constexpr u32 SENT = 0xFFFFFFFFu;
constexpr u32 OOB = 0x80000000u;

constexpr u32 W_EPOCH = 0;
constexpr u32 W_FA = 256;
constexpr u32 W_G1 = 4096;
constexpr u32 W_Q2 = 32768;
constexpr u32 W_SMALL = W_Q2 + 98304;
constexpr u32 W_QA = W_SMALL;
constexpr u32 W_CTX = W_QA + 98304;
constexpr u32 W_AT = W_CTX + 98304;
constexpr u32 W_FP = W_AT + 20480;
constexpr u32 W_MR = W_FP + 122880;
constexpr u32 W_SMALL_END = W_MR + 20480;
constexpr u32 SMALL_CH = (W_SMALL_END - W_SMALL) / 16;
constexpr u32 SMALL_PER = (SMALL_CH + 255) / 256;
constexpr u32 W_O = O_BASE;
constexpr u32 W_PK = W_O + 8u * 128 * 2 * 3072;
constexpr u32 O_BYTES = 8u * 128 * 2 * 3072 + 8u * 128 * 32 * 16;
constexpr u32 W_PROF = W_SMALL_END;
constexpr u32 W_END = W_O + O_BYTES;
static_assert(W_PROF + 256 * 64 * 8 <= W_O, "ws map");
static_assert(SMALL_PER <= 128, "prefill");
static_assert(W_END <= 268435456u, "ws map");

constexpr u32 L_A = 0;
constexpr u32 BLK = 12288;
constexpr u32 L_S1W = 5 * BLK;
constexpr u32 L_X = 98304;
constexpr u32 L_Y = 135168;
constexpr u32 L_FLG = L_Y;
constexpr u32 L_S0P = L_Y + 13568;
constexpr u32 L_LAND = L_Y + 14336;
constexpr u32 L_EML = L_Y + 448;
constexpr u32 L_FRP = L_Y + 512;
constexpr u32 L_GRP = L_Y + 1536;
constexpr u32 L_SC = L_Y + 3584;
constexpr u32 L_HX = L_Y + 7680;
constexpr u32 L_QH = L_Y + 8448;
constexpr u32 L_AM = L_Y + 9216;
constexpr u32 L_SIL = L_Y + 12288;
constexpr u32 L_XN = L_Y + 17408;
constexpr u32 LDS_BYTES = 163840;
constexpr u32 B_W = 4096, B_W2 = 7168, B_GA = 10240, B_X1 = 11200, B_GB = 11520;

enum { F_LAND = 0, F_RDYA, F_S0CNT, F_S1DONE, F_HX, F_QH, F_S2DONE, F_BLK, F_QAOK, F_SADD, F_SC, F_AM, F_FRC, F_GRC, F_QAST, F_XN, F_OST, F_OGI };

constexpr u32 CAP_G = 20000;
constexpr u32 CAP_L = 400000;

constexpr u32 T_S1 = 30, T_S2 = 66, T_TILE0 = 66, T_UV = 198;

DI rsrc_t mkrs(const void* p, u32 bytes) { return __builtin_amdgcn_make_buffer_rsrc((void*)p, (short)0, (int)bytes, 0x00020000); }
template <int AUX> DI u32x4 ld128(rsrc_t r, u32 off) { return __builtin_amdgcn_raw_buffer_load_b128(r, off, 0, AUX); }
template <int AUX> DI u32x3 ld96(rsrc_t r, u32 off) { return __builtin_amdgcn_raw_buffer_load_b96(r, off, 0, AUX); }
template <int AUX> DI u32x2 ld64(rsrc_t r, u32 off) { return __builtin_amdgcn_raw_buffer_load_b64(r, off, 0, AUX); }
template <int AUX> DI u32 ld32(rsrc_t r, u32 off) { return __builtin_amdgcn_raw_buffer_load_b32(r, off, 0, AUX); }
DI u32 ld16(rsrc_t r, u32 off) { return (u32)__builtin_amdgcn_raw_buffer_load_b16(r, off, 0, 0); }
template <int AUX> DI void st128(u32x4 v, rsrc_t r, u32 off) { __builtin_amdgcn_raw_buffer_store_b128(v, r, off, 0, AUX); }
template <int AUX> DI void st96(u32x3 v, rsrc_t r, u32 off) { __builtin_amdgcn_raw_buffer_store_b96(v, r, off, 0, AUX); }
template <int AUX> DI void st64(u32x2 v, rsrc_t r, u32 off) { __builtin_amdgcn_raw_buffer_store_b64(v, r, off, 0, AUX); }
template <int AUX> DI void st32(u32 v, rsrc_t r, u32 off) { __builtin_amdgcn_raw_buffer_store_b32(v, r, off, 0, AUX); }
constexpr int SC1 = 16, NT = 2;

DI float f_lo(u32 v) { return __uint_as_float(v << 16); }
DI float f_hi(u32 v) { return __uint_as_float(v & 0xFFFF0000u); }
DI u32 pk2(float a, float b) { bf16x2 v; v[0] = (__bf16)a; v[1] = (__bf16)b; return __builtin_bit_cast(u32, v); }
DI u32 bf1(float a) { return pk2(a, 0.f) & 0xFFFFu; }
DI u32 san(u32 v) { return v < 0xFFFFFFFEu ? v : 0xFFFFFFFEu; }
DI u32x4 san4(u32x4 v) { u32x4 r; r[0] = san(v[0]); r[1] = san(v[1]); r[2] = san(v[2]); r[3] = san(v[3]); return r; }
DI u32 umax(u32 a, u32 b) { return a > b ? a : b; }
DI u32 mx4(u32 acc, u32x4 v) { return umax(umax(acc, umax(v[0], v[1])), umax(v[2], v[3])); }

DI u32 tagw(float v, u32 tg) { return ((__float_as_uint(v) + 0x800u) & 0xFFFFF000u) | tg; }
DI float untag(u32 v) { return __uint_as_float(v & 0xFFFFF000u); }
DI bool tag_ok(u32x4 v, u32 tg) { return ((((v[0] ^ tg) | (v[1] ^ tg)) | ((v[2] ^ tg) | (v[3] ^ tg))) & 0xFFFu) == 0u; }
DI float dot2(u32 a, u32 b, float c) { return __builtin_amdgcn_fdot2_f32_bf16(__builtin_bit_cast(bf16x2, a), __builtin_bit_cast(bf16x2, b), c, false); }
template <int CTRL> DI u32 dppu(u32 v) { return (u32)__builtin_amdgcn_update_dpp(0, (int)v, CTRL, 0xF, 0xF, true); }
template <int CTRL> DI float dppf(float v) { return __uint_as_float(dppu<CTRL>(__float_as_uint(v))); }
constexpr int ROR(int n) { return 0x120 + n; }
constexpr int SHL(int n) { return 0x100 + n; }
constexpr int NBC(int n) { return 0x150 + n; }
constexpr int QX1 = 0xB1, QX2 = 0x4E;
DI float row_sum(float v) { v += dppf<ROR(8)>(v); v += dppf<ROR(4)>(v); v += dppf<ROR(2)>(v); v += dppf<ROR(1)>(v); return v; }
DI u32 row_xor(u32 v) { v ^= dppu<ROR(8)>(v); v ^= dppu<ROR(4)>(v); v ^= dppu<ROR(2)>(v); v ^= dppu<ROR(1)>(v); return v; }
DI u32 row_add(u32 v) { v += dppu<ROR(8)>(v); v += dppu<ROR(4)>(v); v += dppu<ROR(2)>(v); v += dppu<ROR(1)>(v); return v; }
DI void swap16(float& a, float& b) { auto r = __builtin_amdgcn_permlane16_swap(__float_as_uint(a), __float_as_uint(b), false, false); a = __uint_as_float(r[0]); b = __uint_as_float(r[1]); }
DI void swap32(float& a, float& b) { auto r = __builtin_amdgcn_permlane32_swap(__float_as_uint(a), __float_as_uint(b), false, false); a = __uint_as_float(r[0]); b = __uint_as_float(r[1]); }
DI float xg_sum(float v) { float a = v, b = v; swap16(a, b); v = a + b; a = v; b = v; swap32(a, b); return a + b; }
DI float xg_max(float v) { float a = v, b = v; swap16(a, b); v = fmaxf(a, b); a = v; b = v; swap32(a, b); return fmaxf(a, b); }
DI u32 xg_xor(u32 v) {
    auto r = __builtin_amdgcn_permlane16_swap(v, v, false, false); v = r[0] ^ r[1];
    auto q = __builtin_amdgcn_permlane32_swap(v, v, false, false); return q[0] ^ q[1];
}
DI float wave_sum(float v) { return xg_sum(row_sum(v)); }
DI float sc32(float a, float b) { swap32(a, b); return a + b; }
DI float sc16(float a, float b) { swap16(a, b); return a + b; }
DI u32 sel4(int e, u32 a, u32 b, u32 c, u32 d) { const u32 lo = (e & 1) ? b : a, hi = (e & 1) ? d : c; return (e & 2) ? hi : lo; }

DI f32x4 mfma32(u32x4 a, u32x4 b, f32x4 c) { return __builtin_amdgcn_mfma_f32_16x16x32_bf16(__builtin_bit_cast(bf16x8, a), __builtin_bit_cast(bf16x8, b), c, 0, 0, 0); }
DI f32x4 mfma16(u32x2 a, u32x2 b, f32x4 c) { return __builtin_amdgcn_mfma_f32_16x16x16bf16_1k(__builtin_bit_cast(s16x4, a), __builtin_bit_cast(s16x4, b), c, 0, 0, 0); }
DI u32x2 trd(u32 addr) { return __builtin_bit_cast(u32x2, __builtin_amdgcn_ds_read_tr16_b64_v4i16((LAS s16x4*)addr)); }
DI u32x4 cat(u32x2 a, u32x2 b) { u32x4 r; r[0] = a[0]; r[1] = a[1]; r[2] = b[0]; r[3] = b[1]; return r; }
template <class T> DI T lds_ld(u32 addr) { return *(LAS T*)addr; }
template <class T> DI void lds_st(u32 addr, T v) { *(LAS T*)addr = v; }
#define CBAR() asm volatile("" ::: "memory")

DI u32 flag_ld(int i) { return (u32)__builtin_amdgcn_readfirstlane((int)*(volatile LAS u32*)(i == F_LAND ? L_LAND : L_FLG + 4 * i)); }
DI void flag_st(int i, u32 v, int lane) { CBAR(); if (lane == 0) *(volatile LAS u32*)(L_FLG + 4 * i) = v; CBAR(); }
DI void flag_add(int i, int lane) { CBAR(); if (lane == 0) __hip_atomic_fetch_add((LAS u32*)(L_FLG + 4 * i), 1u, __ATOMIC_RELAXED, __HIP_MEMORY_SCOPE_WORKGROUP); CBAR(); }
DI u32 flag_add_ret(int i, int lane) {
    CBAR(); u32 o = 0;
    if (lane == 0) o = __hip_atomic_fetch_add((LAS u32*)(L_FLG + 4 * i), 1u, __ATOMIC_RELAXED, __HIP_MEMORY_SCOPE_WORKGROUP);
    o = (u32)__builtin_amdgcn_readfirstlane((int)o); CBAR(); return o;
}
template <bool SLEEP> DI void wait_ge(int i, u32 target, bool& dead) {
    u32 n = 0;
    while (!dead && flag_ld(i) < target) { if (++n > CAP_L) dead = true; if (SLEEP || LSLP) __builtin_amdgcn_s_sleep(1); }
    CBAR();
}
DI bool all_ok(bool ok) { return __builtin_amdgcn_ballot_w64(!ok) == 0ull; }
DI bool none_sent(u32 mx) { return __builtin_amdgcn_ballot_w64(mx == SENT) == 0ull; }
#define POLL_STEP() { if (++spins > CAP_G) { dead = true; break; } __builtin_amdgcn_s_sleep(1); }

template <int N> struct PSet { u32x4 v[N]; };
template <int N> DI void take(PSet<N>& res, const PSet<N>& s) {
#pragma unroll
    for (int k = 0; k < N; ++k) res.v[k] = s.v[k];
}
#define PSLP 0

template <int N, int STAGGER, class LD, class OK>
DI void ppoll(PSet<N>& res, PSet<N>& a, PSet<N>& b, PSet<N>& c, LD ld, OK ok, bool& dead) {
    (void)a; (void)b; (void)c;
    for (u32 spins = 0;;) {
        CBAR(); ld(res); CBAR();
        if (ok(res)) break;
        if (++spins > CAP_G) { dead = true; break; }
        if (PSLP) __builtin_amdgcn_s_sleep(PSLP);
    }
    CBAR();
}
template <int N> DI void retire(PSet<N>&, PSet<N>&, PSet<N>&) {}

#define TS(i) do { } while (0)
#define TCAP(v) do { } while (0)
#define TSV(i, t_) do { } while (0)
#define TSDUMP() do { } while (0)

DI u32x4 pack_row(u32 v16) {
    u32 pr = v16 | (dppu<SHL(1)>(v16) << 16);
    u32x4 r; r[0] = pr; r[1] = dppu<SHL(2)>(pr); r[2] = dppu<SHL(4)>(pr); r[3] = dppu<SHL(6)>(pr);
    return san4(r);
}

template <int NTL> DI void dma3(u32 lds, u32 v0, u32 v1, u32 v2, rsrc_t rs, u32 soff) {
    if constexpr (NTL) asm volatile("s_mov_b32 m0, %0\n\ts_nop 3\n\tbuffer_load_dwordx4 %1, %4, %5 offen nt lds\n\tbuffer_load_dwordx4 %2, %4, %5 offen offset:1024 nt lds\n\t"
                                    "buffer_load_dwordx4 %3, %4, %5 offen offset:2048 nt lds" ::"s"(lds), "v"(v0), "v"(v1), "v"(v2), "s"(rs), "s"(soff) : "memory");
    else asm volatile("s_mov_b32 m0, %0\n\ts_nop 3\n\tbuffer_load_dwordx4 %1, %4, %5 offen lds\n\tbuffer_load_dwordx4 %2, %4, %5 offen offset:1024 lds\n\t"
                      "buffer_load_dwordx4 %3, %4, %5 offen offset:2048 lds" ::"s"(lds), "v"(v0), "v"(v1), "v"(v2), "s"(rs), "s"(soff) : "memory");
}
template <int N> DI void waitvm() { asm volatile("s_waitcnt vmcnt(%0)" ::"i"(N) : "memory"); }
constexpr int WM0 = WQ > WUV ? WQ : WUV, WM1 = WM0 > WIN2 ? WM0 : WIN2, WM2 = WM1 > WINT ? WM1 : WINT, WM3 = WM2 > WIN3 ? WM2 : WIN3, WMAX = WM3 > WIN4 ? WM3 : WIN4;
struct Ldr { int issued; int pub; u32 ladr; bool dead; };
DI void publish(Ldr& s, int landed) {
    s.pub = landed > s.pub ? landed : s.pub; CBAR(); *(volatile LAS u32*)s.ladr = (u32)s.pub; CBAR(); }
template <int W> DI void step3(Ldr& s) { s.issued += 3; waitvm<W>(); publish(s, s.issued - W); }
constexpr u32 QHT = 2;
template <int N> DI void drain(Ldr& s) { waitvm<N>(); publish(s, s.issued - N); if constexpr (N >= 3) drain<N - 3>(s); }
template <int W> DI void park(Ldr& s, int flag, u32 target) {
    if (flag_ld(flag) < target) { drain<W - 3>(s); wait_ge<true>(flag, target, s.dead); }
    CBAR();
}
template <int W, int NTL> DI void seg3(Ldr& s, u32 lds0, const u32 (&v)[3], rsrc_t rs, u32 soff0, u32 sstep, int ng) {
    u32 so = soff0, ld = lds0;
#pragma unroll 1
    for (int g = 0; g < ng; ++g) {
        dma3<NTL>(ld, v[0], v[1], v[2], rs, so);
        so += sstep; ld += 3072u;
        step3<W>(s);
    }
}

extern __shared__ __attribute__((aligned(16))) char smem_dyn[];

extern "C" __global__ void __launch_bounds__(640, 3)
adaln_mla_ffn_kernel(const void* __restrict__ p_x, const void* __restrict__ p_ckv, const void* __restrict__ p_wq, const void* __restrict__ p_wuk,
                     const void* __restrict__ p_wuv, const void* __restrict__ p_wo, const void* __restrict__ p_w1, const void* __restrict__ p_w2,
                     const void* __restrict__ p_c, const void* __restrict__ p_wmod, void* __restrict__ p_out, void* __restrict__ p_ws)
{
    const int tid = threadIdx.x, lane = tid & 63;
    const int wave = __builtin_amdgcn_readfirstlane(tid >> 6);
    const int b = blockIdx.x, x = b & 7, r = b >> 3;
    const bool typeA = r < 20;
    const int g = lane >> 4, i16 = lane & 15, q4 = i16 >> 2, p4 = lane & 3;
    bool dead = false;
    const u32 t0c = G1DLY ? (u32)__builtin_amdgcn_s_memtime() : 0u;
    const u32 t0r = (u32)__builtin_amdgcn_s_memrealtime();
    (void)smem_dyn;
    asm volatile("" ::"s"(p_x), "s"(p_ckv), "s"(p_wq), "s"(p_wuk), "s"(p_wuv), "s"(p_wo), "s"(p_w1), "s"(p_w2), "s"(p_c), "s"(p_wmod), "s"(p_out), "s"(p_ws));
    const rsrc_t rWS = mkrs(p_ws, W_END);
    const rsrc_t rX = mkrs(p_x, M_ * 2);
    const bool doS0 = typeA && wave < 5;
    const u32 cg = (x & 1) * 20 + r, kp = x >> 1;
    u32x4 m0v, m1v, xa; u32 cv = 0, xb = 0; u32x2 ep;
    if (doS0) {
        const rsrc_t rc = mkrs(p_c, M_ * 2), rm = mkrs(p_wmod, M_ * 6 * M_ * 2);
        const u32 row0 = 160 * kp + 32 * wave + 2 * (lane >> 2);
        const u32 off = row0 * 7680u + cg * 64u + (lane & 3) * 16u;
        m0v = ld128<NT_S0>(rm, off); m1v = ld128<NT_S0>(rm, off + 7680u);
        cv = ld32<0>(rc, row0 * 2);
    }
    if (wave == 5) { xa = ld128<0>(rX, lane * 16); xb = ld32<0>(rX, 1024 + lane * 4); }
    ep[0] = 0; ep[1] = 0;
    if (lane == 0 && wave != 8) ep = ld64<SC1>(rWS, W_EPOCH);
    if (tid >= 384 && tid < 401) { u32x4 z; z[0] = 0; z[1] = 0; z[2] = 0; z[3] = 0; lds_st<u32x4>(tid < 400 ? L_FLG + (tid - 384) * 16 : L_LAND, z); }
    asm volatile("s_waitcnt lgkmcnt(0)\n\ts_barrier" ::: "memory");
    TCAP(tbw);

    if (wave == 8) {
        Ldr s; s.issued = 0; s.pub = 0; s.ladr = L_LAND + 4 * lane; s.dead = false;
        __builtin_amdgcn_s_setprio(LPRIO);
        u32 v1[3], vt[3], vu[3];
#pragma unroll
        for (int k = 0; k < 3; ++k) {
            const u32 p = 64 * k + lane;
            const u32 row = p / 12, ch = (p % 12) ^ (((row >> 2) & 1) << 1);
            v1[k] = row * 12288u + ch * 16 - 1024u * k; vt[k] = row * 1536u + ch * 16 - 1024u * k;
            const u32 r2 = p / 24, c2 = (p % 24) ^ (((r2 >> 1) & 3) << 1);
            vu[k] = r2 * 12288u + c2 * 16 - 1024u * k;
        }
        const rsrc_t rq = mkrs(p_wq, M_ * H_ * D_ * 2), ruk = mkrs(p_wuk, DC * H_ * D_ * 2), ruv = mkrs(p_wuv, DC * H_ * D_ * 2), rkv = mkrs(p_ckv, SK * DC * 2);
        const u32 ks = r >> 3, h1 = 4 * x + ((r >> 1) & 3), half = r & 1;
        const u32 so1 = 160u * ks * 12288u + (h1 * 192 + 96 * half) * 2;
        TCAP(tb8);
        seg3<WQ, NT_Q>(s, L_S1W, v1, rq, so1, 16u * 12288u, 10);
        TS(44); TSV(51, t_entry); TSV(54, tb8);
        const u32 so2 = 96u * x * 12288u + (u32)r * 384u;
        const u32 jt = x + 8 * (r & 15);
        const u32 sot = 64u * jt * 1536u;
#pragma unroll 1
        for (int gi = 0; gi < 12; ++gi) {
            if (gi == UKA) { park<WMAX>(s, F_HX, 5); TS(60); }
            dma3<NT_UK>(L_X + 3072u * gi, vu[0], vu[1], vu[2], ruk, so2 + 8u * 12288u * gi);
            step3<WINT>(s);
        }
        TS(45);
#pragma unroll 1
        for (int gi = 0; gi < 32; ++gi) {
            if (gi == 20) park<WMAX>(s, F_S1DONE, 6);
            if (gi == TLH) { park<WMAX>(s, F_HX, 5); TS(60); }
            if (gi == TLA) { park<WMAX>(s, F_QH, QHT); TS(61); }
            dma3<NT_KV>(L_A + BLK * (gi >> 2) + 3072u * (gi & 3), vt[0], vt[1], vt[2], rkv, sot + 192u * (gi >> 2) + 16u * 1536u * (gi & 3));
            step3<WINT>(s);
        }
        TS(46);
        park<WMAX>(s, F_S2DONE, 6);
#pragma unroll 1
        for (int gi = 0; gi < 12; ++gi) {
            if (gi == UVA) { park<WMAX>(s, F_QAOK, 8); TS(47); }
            dma3<NT_UV>(L_X + 3072u * gi, vu[0], vu[1], vu[2], ruv, so2 + 8u * 12288u * gi);
            step3<WUV>(s);
        }
        TS(48);
        park<WMAX>(s, F_BLK, 8);
        if (OGA > 0) { park<WMAX>(s, F_SC, 8); TS(47); }
        if (OST == 1) { wait_ge<true>(F_OST, 8, s.dead); }
        TS(62);
        if (typeA) {
            const rsrc_t ro = mkrs(p_wo, H_ * D_ * M_ * 2);
            u32 vo[2][3];
#pragma unroll
            for (int gp = 0; gp < 2; ++gp)
#pragma unroll
                for (int k = 0; k < 3; ++k) {
                    const u32 f = ((lane >> 4) & 1) | (((gp + k) & 1) << 1);
                    vo[gp][k] = (8u * k + (lane >> 3)) * 1280u + ((((lane & 7) >> 1) ^ f) * 32u) + (lane & 1) * 16u - 1024u * k;
                }
            u32 so = (384u * (2 * x + (r >= 10 ? 1 : 0))) * 1280u + 128u * (r % 10);
#pragma unroll 1
            for (int j = 0; j < 8; ++j) {
                const u32 ld = L_A + 2 * BLK * (j >> 1) + 6144u * (j & 1);
                if (j == 2 * OGA) { park<WMAX>(s, OGI ? F_OGI : F_AM, 8); TS(59); }
                dma3<NT_O>(ld, vo[0][0], vo[0][1], vo[0][2], ro, so);
                if (j < 2 * OGA) step3<WIN3>(s); else step3<WIN4>(s);
                dma3<NT_O>(ld + 3072u, vo[1][0], vo[1][1], vo[1][2], ro, so + 24u * 1280u);
                if (j < 2 * OGA) step3<WIN3>(s); else step3<WIN4>(s);
                so += 48u * 1280u;
            }
        } else {
            const rsrc_t r1 = mkrs(p_w1, M_ * DFF * 2), r2 = mkrs(p_w2, DFF * M_ * 2);
            const u32 fb = x * 12 + (r - 20);
            const u32 va = (lane >> 1) * 3072u + (lane & 1) * 16;
            const u32 v1a[3] = {va, va + 32u * 3072u - 1024u, lane < 32 ? va + 64u * 3072u - 2048u : OOB};
            u32 vb[3];
#pragma unroll
            for (int k = 0; k < 3; ++k) { const u32 p = 64 * k + lane; vb[k] = p < 160 ? (p / 10) * 1280u + (p % 10) * 16 - 1024u * k : OOB; }
#pragma unroll 1
            for (int w = 0; w < 8; ++w) {
                const u32 ld = L_A + BLK * w;
                if (w == OGB) { park<WMAX>(s, OGI == 1 ? F_OGI : F_AM, 8); TS(59); }
                dma3<NT_W1>(ld + B_W, v1a[0], v1a[1], v1a[2], r1, 80u * w * 3072u + fb * 32u);
                step3<WIN2>(s);
                dma3<NT_W2>(ld + B_W2, vb[0], vb[1], vb[2], r2, 16u * fb * 1280u + 160u * w);
                step3<WIN2>(s);
            }
        }
        TS(49);
        drain<WMAX - 3>(s);
        TS(50);
        TSDUMP();
        return;
    }

    if (wave == 9) {
        u32x4 sv; sv[0] = SENT; sv[1] = SENT; sv[2] = SENT; sv[3] = SENT;
        {
            const u32 c0 = (u32)b * SMALL_PER + lane, c1 = c0 + 64;
            st128<SC1>(sv, rWS, c0 < SMALL_CH ? W_SMALL + c0 * 16 : OOB);
            st128<SC1>(sv, rWS, (lane + 64 < (int)SMALL_PER && c1 < SMALL_CH) ? W_SMALL + c1 * 16 : OOB);
        }
        {
            u64 pc; u32 before, len;
            asm volatile("s_getpc_b64 %0\n.Lpf%=:\n\ts_mov_b32 %1, .Lpf%=-" KNAME "\n\ts_mov_b32 %2, " FEND "-" KNAME : "=s"(pc), "=s"(before), "=s"(len));
            const rsrc_t rcode = mkrs((const void*)(pc - before), len < 65536u ? len : 0u);
            const u32x4 c0 = ld128<0>(rcode, (u32)r * 2048u + lane * 16u), c1 = ld128<0>(rcode, (u32)r * 2048u + 1024u + lane * 16u);
            asm volatile("" ::"v"(c0), "v"(c1));
        }
        const int it = 255 - b;
        const rsrc_t rc = mkrs(p_c, M_ * 2), rm = mkrs(p_wmod, M_ * 6 * M_ * 2);
        u32 cs[5];
        if (it < 160) {
#pragma unroll
            for (int k = 0; k < 5; ++k) cs[k] = ld16(rc, (320 * (it & 1) + 64 * k + lane) * 2);
        }
        TCAP(tb9);
        waitvm<0>();
        TSV(52, t_entry); TSV(55, tb9);
        const u64 tag = (((u64)(u32)__builtin_amdgcn_readfirstlane((int)ep[1]) << 32) | (u32)__builtin_amdgcn_readfirstlane((int)ep[0])) + 1ull;
        u32x2 tg; tg[0] = (u32)tag; tg[1] = (u32)(tag >> 32);
        if (lane == 0) st64<SC1>(tg, rWS, W_FA + 8 * b);
        TS(39);
        wait_ge<true>(F_HX, 5, dead);
        {
            u32 spins = 0;
            for (;;) {
                CBAR();
                const u32x4 a = ld128<SC1>(rWS, W_FA + lane * 16), c = ld128<SC1>(rWS, W_FA + 1024 + lane * 16);
                const bool ok = a[0] == tg[0] && a[1] == tg[1] && a[2] == tg[0] && a[3] == tg[1] && c[0] == tg[0] && c[1] == tg[1] && c[2] == tg[0] && c[3] == tg[1];
                if (all_ok(ok)) break;
                POLL_STEP();
            }
            flag_st(F_RDYA, 1, lane);
            TS(40);
        }
        if (it < 160) {
            const int cb = it >> 1, kh = it & 1;
#pragma unroll
            for (int k = 0; k < 5; ++k) {
                const float cv = __uint_as_float(cs[k] << 16);
                lds_st<float>(L_SIL + (64 * k + lane) * 4, cv * __builtin_amdgcn_rcpf(1.f + __expf(-cv)));
            }
            if (MRAT == 4) { } else if (MRAT == 5) wait_ge<true>(F_QH, QHT, dead); else if (MRAT == 2) wait_ge<true>(F_AM, 8, dead); else if (MRAT == 3) wait_ge<true>(F_SADD, 8, dead); else if (MRAT) wait_ge<true>(F_LAND, T_TILE0 + 96, dead); else wait_ge<true>(F_QAOK, 8, dead);
            TS(42);
            u32x4 wv[20];
            const u32 vo = (u32)(320 * kh + (lane >> 2)) * 7680u + (1280u + 32u * cb) * 2u + (lane & 3) * 16u;
#pragma unroll
            for (int k = 0; k < 20; ++k) wv[k] = ld128<NT_MR>(rm, vo + (u32)k * 16u * 7680u);
            float acc[8];
#pragma unroll
            for (int j = 0; j < 8; ++j) acc[j] = 0.f;
#pragma unroll
            for (int k = 0; k < 20; ++k) {
                const float sl = lds_ld<float>(L_SIL + (16 * k + (lane >> 2)) * 4);
#pragma unroll
                for (int j = 0; j < 4; ++j) { acc[2 * j] += sl * f_lo(wv[k][j]); acc[2 * j + 1] += sl * f_hi(wv[k][j]); }
            }
            float t4[4], t2[2];
#pragma unroll
            for (int j = 0; j < 4; ++j) t4[j] = sc32(acc[j], acc[j + 4]);
#pragma unroll
            for (int j = 0; j < 2; ++j) t2[j] = sc16(t4[j], t4[j + 2]);
#pragma unroll
            for (int j = 0; j < 2; ++j) { t2[j] += dppf<ROR(8)>(t2[j]); t2[j] += dppf<ROR(4)>(t2[j]); }
            const u32 pm = cb / 20;
#pragma unroll
            for (int j = 0; j < 2; ++j) {
                const u32 col = (cb % 20) * 32u + 8 * (lane & 3) + 4 * (lane >> 5) + 2 * ((lane >> 4) & 1) + j;
                const u32 cw = col / 80, cc = col % 80;
                const u32 pos = cc < 64 ? (cc & 7) * 8 + (cc >> 3) : 64 + (cc & 7) * 2 + ((cc >> 3) & 1);
                st32<SC1>(san(__float_as_uint(t2[j])), rWS, (lane & 12) == 0 ? W_MR + ((kh * 8u + cw) * 80u + pos) * 16u + pm * 4u : OOB);
            }
            TS(43);
        }
        TSDUMP();
        return;
    }

    const int w = wave;
    const u32 ks = r >> 3;
    const bool front = w < 5;
    if (w == 0) TSV(53, tbw);
    if (w == 5) TSV(56, tbw);
    if (w == 7) { TSV(57, tbw); TSV(58, t_entry); }
    if (w == 0) TSV(30, t_entry);
    if (w == 0) TS(0);
    const u64 tag = (((u64)(u32)__builtin_amdgcn_readfirstlane((int)ep[1]) << 32) | (u32)__builtin_amdgcn_readfirstlane((int)ep[0])) + 1ull;
    const u32 T1 = (u32)tag, T2 = (u32)(tag >> 32);
    const u32 tagl = T1 & 0xFFu, tg12 = T1 & 0xFFFu;

    if (doS0) {
        const float cA = f_lo(cv), cB = f_hi(cv);
        const float sA = cA * __builtin_amdgcn_rcpf(1.f + __expf(-cA)), sB = cB * __builtin_amdgcn_rcpf(1.f + __expf(-cB));
        const u32 sph = pk2(sA, sB), spl = pk2(sA - f_lo(sph), sB - f_hi(sph));
        float acc[8];
#pragma unroll
        for (int j = 0; j < 4; ++j) {
            const u32 pa = __builtin_amdgcn_perm(m1v[j], m0v[j], 0x05040100u), pb = __builtin_amdgcn_perm(m1v[j], m0v[j], 0x07060302u);
            acc[2 * j] = dot2(pa, spl, dot2(pa, sph, 0.f));
            acc[2 * j + 1] = dot2(pb, spl, dot2(pb, sph, 0.f));
        }
        if (w == 0) TS(31);
        float t4[4]; f32x2 t2;
#pragma unroll
        for (int j = 0; j < 4; ++j) t4[j] = sc32(acc[j], acc[j + 4]);
#pragma unroll
        for (int j = 0; j < 2; ++j) { float t = sc16(t4[j], t4[j + 2]); t += dppf<ROR(8)>(t); t += dppf<ROR(4)>(t); t2[j] = t; }
        if ((lane & 12) == 0) lds_st<f32x2>(L_S0P + (w * 32 + 8 * (lane & 3) + 4 * (lane >> 5) + 2 * ((lane >> 4) & 1)) * 4, t2);
        if (flag_add_ret(F_S0CNT, lane) == 4) {
            f32x4 v = lds_ld<f32x4>(L_S0P + (lane & 7) * 16);
#pragma unroll
            for (int p = 1; p < 5; ++p) v += lds_ld<f32x4>(L_S0P + p * 128 + (lane & 7) * 16);
            u32x4 o;
#pragma unroll
            for (int j = 0; j < 4; ++j) o[j] = tagw(v[j], tg12);
            const u32 wh = cg >= 20 ? 1u : 0u, m0 = 32 * (cg - 20 * wh);
            st128<SC1>(o, rWS, lane < 8 ? W_G1 + ((wh * 4 + kp) * 640u + m0 + 4 * lane) * 4u : OOB);
            TS(1);
        }
    }

    const u32 h1 = 4 * x + ((r >> 1) & 3), half = r & 1;
    u32x4 bw[5];
    auto read_bw = [&]() {
        wait_ge<true>(F_LAND, T_S1, dead);
        const u32 ab = L_S1W + (4 * g + q4) * 192 + (((2 * w + (p4 >> 1)) ^ ((g & 1) << 1)) * 16) + 8 * (p4 & 1);
#pragma unroll
        for (int t = 0; t < 5; ++t) bw[t] = cat(trd(ab + (32 * t) * 192), trd(ab + (32 * t + 16) * 192));
        asm volatile("s_waitcnt lgkmcnt(0)" ::: "memory");
        flag_add(F_S1DONE, lane);
        if (w == 0) TS(32);
    };
    if (w == 5) {
        float xv[10];
#pragma unroll
        for (int k = 0; k < 4; ++k) { xv[2 * k] = f_lo(xa[k]); xv[2 * k + 1] = f_hi(xa[k]); }
        xv[8] = f_lo(xb); xv[9] = f_hi(xb);
        float sum = 0.f;
#pragma unroll
        for (int k = 0; k < 10; ++k) sum += xv[k];
        const float mu = wave_sum(sum) * (1.f / 640.f);
        float sq = 0.f;
#pragma unroll
        for (int k = 0; k < 10; ++k) { xv[k] -= mu; sq += xv[k] * xv[k]; }
        const float rstd = 1.f / sqrtf(wave_sum(sq) * (1.f / 640.f) + 1e-5f);
        f32x4 n0, n1; f32x2 n2;
#pragma unroll
        for (int k = 0; k < 4; ++k) { n0[k] = xv[k] * rstd; n1[k] = xv[4 + k] * rstd; }
        n2[0] = xv[8] * rstd; n2[1] = xv[9] * rstd;
        lds_st<f32x4>(L_XN + lane * 32, n0); lds_st<f32x4>(L_XN + lane * 32 + 16, n1); lds_st<f32x2>(L_XN + 2048 + lane * 8, n2);
        flag_add(F_XN, lane);
        read_bw();
    }
    if (front) {
        read_bw();
        wait_ge<true>(F_XN, 1, dead);
        const f32x4 xn4 = lds_ld<f32x4>(L_XN + (160 * ks + 32 * w + 4 * (lane & 7)) * 4);
        float aj[4], bj[4];
#pragma unroll
        for (int j = 0; j < 4; ++j) { aj[j] = lane >= 32 ? xn4[j] : 1.f; bj[j] = lane < 8 ? xn4[j] : 0.f; }
        const u32 goff = W_G1 + ((u32)(lane >> 3) * 640u + 160 * ks + 32 * w + 4 * (lane & 7)) * 4u;
        const u32 hadr = L_HX + ((lane >> 3) & 1) * 320 + (32 * w + 8 * (lane & 3) + 4 * ((lane >> 2) & 1) + 2 * (lane >> 5) + ((lane >> 4) & 1)) * 2;
        if (G1DLY) for (u32 n = 0; n < CAP_L && (u32)__builtin_amdgcn_s_memtime() - t0c < G1DLY * 64u; ++n) __builtin_amdgcn_s_sleep(1);
        PSet<1> gv, pa, pb, pc;
        ppoll<1, STG>(gv, pa, pb, pc,
            [&](PSet<1>& s) { s.v[0] = ld128<SC1>(rWS, goff); },
            [&](const PSet<1>& s) { return all_ok(tag_ok(s.v[0], tg12)); }, dead);
        if (w == 4) TS(2);
        if (w == 0) TS(19);
        float t[4];
#pragma unroll
        for (int j = 0; j < 4; ++j) t[j] = aj[j] * untag(gv.v[0][j]) + bj[j];
        float hv = sc16(sc32(t[0], t[2]), sc32(t[1], t[3]));
        hv += dppf<ROR(8)>(hv);
        const u32 hi = bf1(hv), lo = bf1(hv - __uint_as_float(hi << 16));
        lds_st<unsigned short>(hadr, (unsigned short)((lane & 8) ? lo : hi));
        flag_add(F_HX, lane);
        retire(pa, pb, pc);
    }
    u32x4 bw2[6];
    u32 tq = 0;
    if (w < 6) {
        wait_ge<false>(F_HX, 5, dead);
        if (w == 0) TS(18);
        f32x4 acc = {0.f, 0.f, 0.f, 0.f};
#pragma unroll
        for (int t = 0; t < 5; ++t) acc = mfma32(lds_ld<u32x4>(L_HX + (lane & 1) * 320 + 64 * t + 16 * g), bw[t], acc);
        const u32 qb = tagw(acc[0] + acc[1], tg12);
        u32x4 o; o[0] = qb; o[1] = dppu<SHL(1)>(qb); o[2] = dppu<SHL(2)>(qb); o[3] = dppu<SHL(3)>(qb);
        st128<SC1>(o, rWS, (lane & 51) == 0 ? W_Q2 + (ks * 6144u + h1 * 192 + 96 * half + 16 * w + lane) * 4u : OOB);
        if (w == 0) TS(3);
        wait_ge<true>(F_LAND, T_S2, dead);
        const u32 rowb = L_X + (16 * w + i16) * 384, sw = ((i16 >> 1) & 3) << 1;
#pragma unroll
        for (int t = 0; t < 6; ++t) bw2[t] = lds_ld<u32x4>(rowb + (((4 * t + g) ^ sw) * 16));
        flag_add(F_S2DONE, lane);
        if (w == 0) TS(33);
    }
    if (w >= 6) {
        wait_ge<true>(F_HX, 5, dead);
        const u32 c0 = 128 * (w - 6) + 4 * i16;
        const u32 qo = W_Q2 + ((u32)g * 6144u + (u32)r * 192u + c0) * 4u, qo2 = w == 6 ? qo + 256 : qo;
        const u32 qadr = L_QH + (c0 + 2 * (lane >> 5) + ((lane >> 4) & 1)) * 2;
        if (Q2DLY) { const u32 th = (u32)__builtin_amdgcn_s_memtime(); for (u32 n = 0; n < CAP_L && (u32)__builtin_amdgcn_s_memtime() - th < Q2DLY * 64u; ++n) __builtin_amdgcn_s_sleep(1); }
        PSet<2> qv1, pa, pb, pc;
        ppoll<2, STG>(qv1, pa, pb, pc,
            [&](PSet<2>& s) { s.v[0] = ld128<SC1>(rWS, qo); if (w == 6) s.v[1] = ld128<SC1>(rWS, qo2); else s.v[1] = s.v[0]; },
            [&](const PSet<2>& s) { return all_ok(tag_ok(s.v[0], tg12) && tag_ok(s.v[1], tg12)); }, dead);
        if (w == 7) TS(4);
        if (w == 6) TS(34);
#pragma unroll
        for (int k = 0; k < 2; ++k) {
            if (k == 0 || w == 6) {
                const float q = sc16(sc32(untag(qv1.v[k][0]), untag(qv1.v[k][2])), sc32(untag(qv1.v[k][1]), untag(qv1.v[k][3])));
                const u32 hi = bf1(q), lo = bf1(q - __uint_as_float(hi << 16));
                lds_st<unsigned short>(qadr + 128 * k, (unsigned short)hi); lds_st<unsigned short>(qadr + 128 * k + 384, (unsigned short)lo);
            }
        }
        flag_add(F_QH, lane);
        retire(pa, pb, pc);
    }
    if (w < 6) {
        wait_ge<false>(F_QH, QHT, dead);
        if (w == 0) TS(37);
        f32x4 a0 = {0.f, 0.f, 0.f, 0.f}, a1 = {0.f, 0.f, 0.f, 0.f};
#pragma unroll
        for (int t = 0; t < 6; t += 2) {
            a0 = mfma32(lds_ld<u32x4>(L_QH + (lane & 1) * 384 + 64 * t + 16 * g), bw2[t], a0);
            a1 = mfma32(lds_ld<u32x4>(L_QH + (lane & 1) * 384 + 64 * (t + 1) + 16 * g), bw2[t + 1], a1);
        }
        const float qa = ((a0[0] + a0[1]) + (a1[0] + a1[1])) * (0.07216878364870322f * 1.4426950408889634f);
        const u32 qh = bf1(qa), ql = bf1(qa - __uint_as_float(qh << 16));
        const u32x4 o = pack_row((QLO && (g & 1)) ? ql : qh);
        const u32 dst = QLO ? W_QA + ((u32)r * 8 + x) * 384u + (w >> 1) * 128 + g * 64 + (w & 1) * 32 + ((lane >> 3) & 1) * 16
                            : W_QA + ((u32)r * 8 + x) * 256u + w * 32 + ((lane >> 3) & 1) * 16;
        wait_ge<false>(F_RDYA, 1, dead);
        if (w == 0) TS(38);
        st128<SC1>(o, rWS, (lane & (QLO ? 39 : 55)) == 0 ? dst : OOB);
        tq = (u32)__builtin_amdgcn_s_memtime();
        flag_add(F_QAST, lane);
        if (w == 0) TS(5);
    }

    const u32 g2 = r >> 4;
    const u32 blk = L_A + BLK * w;
    const u32 t_blk = T_TILE0 + 12 * (w + 1);
    u32x4 vt[6][2];
    {
        constexpr int NQ = QLO ? 6 : 3;
        u32x4 bq[NQ];
        u32x4 av[4][3];
        const u32 qoff = W_QA + ((16 * g2 + i16) * 8 + w) * (QLO ? 384u : 256u) + g * 16u;
        auto issue_q = [&]() {
            CBAR();
#pragma unroll
            for (int k = 0; k < NQ; ++k) bq[k] = ld128<SC1>(rWS, qoff + k * 64);
            CBAR();
        };
        if (w >= 6) { wait_ge<true>(F_QAST, 6, dead); tq = (u32)__builtin_amdgcn_s_memtime(); }
        wait_ge<false>(F_RDYA, 1, dead);
        bool landed = false;
        for (u32 n = 0; !dead;) {
            landed = flag_ld(F_LAND) >= t_blk;
            if (landed || ((u32)__builtin_amdgcn_s_memtime() - tq >= QDLY * 64u && (u32)__builtin_amdgcn_s_memrealtime() - t0r >= QABS)) break;
            if (++n > CAP_L) dead = true;
            __builtin_amdgcn_s_sleep(1);
        }
        if (!landed) {
            if (w == 0) TS(24);
            if (w == 7) TS(25);
            issue_q(); wait_ge<true>(F_LAND, t_blk, dead);
        }
        if (w == 0) TS(6);
        if (w == 7) TS(27);
        const u32 ar = blk + i16 * 192, asw = ((i16 >> 2) & 1) << 1;
#pragma unroll
        for (int kb = 0; kb < 4; ++kb)
#pragma unroll
            for (int t = 0; t < 3; ++t) av[kb][t] = lds_ld<u32x4>(ar + kb * 16 * 192 + ((4 * t + g) ^ asw) * 16);
        const u32 vb = blk + (4 * g + q4) * 192 + (p4 >> 1) * 16 + 8 * (p4 & 1);
#pragma unroll
        for (int u = 0; u < 6; ++u) {
            const u32 cu = vb + (u ^ (g & 1)) * 32;
#pragma unroll
            for (int t = 0; t < 2; ++t) vt[u][t] = cat(trd(cu + (32 * t) * 192), trd(cu + (32 * t + 16) * 192));
        }
        asm volatile("s_waitcnt lgkmcnt(0)" ::: "memory");
        flag_add(F_BLK, lane);
        if (w == 7) TS(28);
        if (landed) {
            for (u32 n = 0; n < CAP_L && ((u32)__builtin_amdgcn_s_memtime() - tq < QDLY * 64u || (u32)__builtin_amdgcn_s_memrealtime() - t0r < QABS); ++n) __builtin_amdgcn_s_sleep(1);
            if (w == 0) TS(24);
            if (w == 7) TS(25);
            issue_q();
        }
        for (u32 spins = 0;;) {
            u32 mx = 0;
#pragma unroll
            for (int k = 0; k < NQ; ++k) mx = mx4(mx, bq[k]);
            if (none_sent(mx)) break;
            POLL_STEP();
            if (QMASK) { if (mx == SENT) issue_q(); } else issue_q();
        }
        flag_add(F_QAOK, lane);
        if (w == 0) TS(7);
        if (w == 7) TS(26);
#pragma unroll
        for (int kb = 0; kb < 4; ++kb) {
            f32x4 acc = {0.f, 0.f, 0.f, 0.f};
#pragma unroll
            for (int t = 0; t < 3; ++t) { if (QLO) { acc = mfma32(av[kb][t], bq[(2 * t + 1) % NQ], acc); acc = mfma32(av[kb][t], bq[(2 * t) % NQ], acc); } else acc = mfma32(av[kb][t], bq[t], acc); }
            f32x2 lo2, hi2; lo2[0] = acc[0]; lo2[1] = acc[1]; hi2[0] = acc[2]; hi2[1] = acc[3];
            lds_st<f32x2>(blk + (kb * 2) * 512 + lane * 8, lo2);
            lds_st<f32x2>(blk + (kb * 2 + 1) * 512 + lane * 8, hi2);
        }
        flag_add(F_SADD, lane);
        if (w == 0) TS(20);
    }
    {
        wait_ge<false>(F_SADD, 8, dead);
        if (w == 0) TS(21);
        f32x2 s = {0.f, 0.f};
#pragma unroll
        for (int p = 0; p < 8; ++p) s += lds_ld<f32x2>(L_A + BLK * p + w * 512 + lane * 8);
        lds_st<f32x2>(L_SC + (w >> 1) * 1024 + lane * 16 + (w & 1) * 8, s);
        flag_add(F_SC, lane);
        wait_ge<false>(F_SC, 8, dead);
        if (w == 0) TS(22);
        f32x4 sc[4];
#pragma unroll
        for (int q = 0; q < 4; ++q) sc[q] = lds_ld<f32x4>(L_SC + q * 1024 + lane * 16);
        float mloc = fmaxf(fmaxf(fmaxf(sc[0][0], sc[0][1]), fmaxf(sc[0][2], sc[0][3])), fmaxf(fmaxf(sc[1][0], sc[1][1]), fmaxf(sc[1][2], sc[1][3])));
        mloc = fmaxf(mloc, fmaxf(fmaxf(fmaxf(sc[2][0], sc[2][1]), fmaxf(sc[2][2], sc[2][3])), fmaxf(fmaxf(sc[3][0], sc[3][1]), fmaxf(sc[3][2], sc[3][3]))));
        mloc = xg_max(mloc);
        u32x4 pf[2];
#pragma unroll
        for (int t = 0; t < 2; ++t)
#pragma unroll
            for (int e = 0; e < 4; ++e) {
                const f32x4 sq = sc[2 * t + (e >> 1)];
                pf[t][e] = pk2(__builtin_amdgcn_exp2f(sq[2 * (e & 1)] - mloc), __builtin_amdgcn_exp2f(sq[2 * (e & 1) + 1] - mloc));
            }
        f32x4 oa[6], lz;
#pragma unroll
        for (int u = 0; u < 6; ++u) { f32x4 z = {0.f, 0.f, 0.f, 0.f}; oa[u] = mfma32(vt[u][0], pf[0], z); oa[u] = mfma32(vt[u][1], pf[1], oa[u]); }
        {
            u32x4 one; one[0] = 0x3F803F80u; one[1] = 0x3F803F80u; one[2] = 0x3F803F80u; one[3] = 0x3F803F80u;
            f32x4 z = {0.f, 0.f, 0.f, 0.f};
            lz = mfma32(one, pf[0], z); lz = mfma32(one, pf[1], lz);
        }
        if (w == 0) TS(23);
        const u32 jj = x * 16 + (r & 15);
        const u32 ob = W_O + ((w * 128 + jj) * 2 + g2) * 3072u + (i16 * 4 + g) * 16;
        u32 cx = 0;
#pragma unroll
        for (int k = 0; k < 3; ++k) {
            u32x4 d;
            d[0] = pk2(oa[2 * k][0], oa[2 * k][1]); d[1] = pk2(oa[2 * k][2], oa[2 * k][3]);
            d[2] = pk2(oa[2 * k + 1][0], oa[2 * k + 1][1]); d[3] = pk2(oa[2 * k + 1][2], oa[2 * k + 1][3]);
            st128<SC1>(d, rWS, ob + k * 1024);
            cx ^= (d[0] ^ d[1]) ^ (d[2] ^ d[3]);
        }
        cx = xg_xor(cx);
        u32x4 pk;
        pk[0] = __float_as_uint(mloc); pk[1] = __float_as_uint(lz[0]);
        pk[2] = cx ^ pk[0] ^ pk[1] ^ T2; pk[3] = T1;
        st128<SC1>(pk, rWS, g == 0 ? W_PK + ((w * 128 + jj) * 32 + 16 * g2 + i16) * 16 : OOB);
        if (OST) flag_add(F_OST, lane);
        if (w == 0) TS(8);
    }

    {
        const u32 poff = W_PK + ((x * 128 + 16 * w + 4 * g + (i16 & 3)) * 32 + r) * 16;
        const u32 ooff = i16 < 12 ? W_O + ((x * 128 + 16 * w + 4 * g) * 2 + g2) * 3072u + (i16 >> 2) * 1024 + ((r & 15) * 4 + (i16 & 3)) * 16 : OOB;
        u32x4 bw[2][3];
        if (w < 6) {
            wait_ge<true>(F_LAND, T_UV, dead);
            const u32 sw = 2 * (g & 1) + (q4 >> 1);
#pragma unroll
            for (int d = 0; d < 2; ++d) {
                const u32 ub = L_X + (4 * g + q4) * 384 + (2 * ((2 * w + d) ^ sw) + (p4 >> 1)) * 16 + 8 * (p4 & 1);
#pragma unroll
                for (int t = 0; t < 3; ++t) bw[d][t] = cat(trd(ub + (32 * t) * 384), trd(ub + (32 * t + 16) * 384));
            }
        }
        if (ODLY) __builtin_amdgcn_s_sleep(ODLY);
        if (OABS) for (u32 n = 0; n < CAP_L && (u32)__builtin_amdgcn_s_memrealtime() - t0r < OABS; ++n) __builtin_amdgcn_s_sleep(1);
        u32x4 pk, od[4];
        {
            u32 spins = 0;
            for (;;) {
                CBAR(); pk = ld128<SC1>(rWS, poff); CBAR();
                if (all_ok(pk[3] == T1)) break;
                POLL_STEP();
            }
        }
        if (w == 0) TS(35);
        {
            u32 spins = 0;
            u64 bad = ~0ull;
            for (;;) {
                CBAR();
#pragma unroll
                for (int k = 0; k < 4; ++k) if (bad & (0x1111111111111111ull << k)) od[k] = ld128<SC1>(rWS, ooff + k * 6144);
                if (!ELIGHT || spins) pk = ld128<SC1>(rWS, poff);
                CBAR();
                if (OGI && spins == 0) flag_add(F_OGI, lane);
                u32 c[4];
#pragma unroll
                for (int k = 0; k < 4; ++k) c[k] = (od[k][0] ^ od[k][1]) ^ (od[k][2] ^ od[k][3]);
                const bool b0 = lane & 1, b1 = lane & 2;
                const u32 t01 = (b0 ? c[1] : c[0]) ^ dppu<QX1>(b0 ? c[0] : c[1]), t23 = (b0 ? c[3] : c[2]) ^ dppu<QX1>(b0 ? c[2] : c[3]);
                u32 t = (b1 ? t23 : t01) ^ dppu<QX2>(b1 ? t01 : t23);
                t ^= dppu<ROR(4)>(t); t ^= dppu<ROR(8)>(t);
                const bool ok = pk[3] == T1 && (t ^ pk[0] ^ pk[1] ^ T2) == pk[2];
                bad = __builtin_amdgcn_ballot_w64(!ok);
                if (bad == 0ull) break;
                POLL_STEP();
            }
        }
        if (w == 0) TS(9);
        const float mj = __uint_as_float(pk[0]);
        float Mw = fmaxf(mj, dppf<QX1>(mj)); Mw = fmaxf(Mw, dppf<QX2>(Mw)); Mw = xg_max(Mw);
        const u32 wb = bf1(__builtin_amdgcn_exp2f(mj - Mw));
        const u32 sh = (lane & 1) ? 65536u : 1u;
        u32x4 aw;
        aw[0] = __umul24(dppu<0x00>(wb), sh); aw[1] = __umul24(dppu<0x55>(wb), sh); aw[2] = __umul24(dppu<0xAA>(wb), sh); aw[3] = __umul24(dppu<0xFF>(wb), sh);
        u32x4 yv;
#pragma unroll
        for (int u = 0; u < 4; ++u) {
            u32x4 bo; bo[0] = od[0][u]; bo[1] = od[1][u]; bo[2] = od[2][u]; bo[3] = od[3][u];
            f32x4 z = {0.f, 0.f, 0.f, 0.f};
            z = mfma32(aw, bo, z);
            yv[u] = pk2(z[0], z[1]);
        }
        float lw = __uint_as_float(wb << 16) * __uint_as_float(pk[1]);
        lw += dppf<QX1>(lw); lw += dppf<QX2>(lw); lw = xg_sum(lw);
        if (lane < 12) lds_st<u32x4>(L_AM + w * 208 + lane * 16, yv);
        { f32x2 ml; ml[0] = Mw; ml[1] = lw; if (lane == 0) lds_st<f32x2>(L_EML + w * 8, ml); }
        flag_add(F_AM, lane);
        if (w < 6) {
            wait_ge<false>(F_AM, 8, dead);
            u32x4 aa[3];
#pragma unroll
            for (int t = 0; t < 3; ++t) aa[t] = lds_ld<u32x4>(L_AM + (i16 & 7) * 208 + (4 * t + g) * 16);
            const f32x4 e0 = lds_ld<f32x4>(L_EML + (g & 1) * 32), e1 = lds_ld<f32x4>(L_EML + (g & 1) * 32 + 16);
            float Mx = fmaxf(fmaxf(e0[0], e0[2]), fmaxf(e1[0], e1[2]));
            { float a = Mx, bb = Mx; swap16(a, bb); Mx = fmaxf(a, bb); }
            const float f0 = __builtin_amdgcn_exp2f(e0[0] - Mx), f1 = __builtin_amdgcn_exp2f(e0[2] - Mx), f2 = __builtin_amdgcn_exp2f(e1[0] - Mx), f3 = __builtin_amdgcn_exp2f(e1[2] - Mx);
            const float ls = (f0 * e0[1] + f1 * e0[3]) + (f2 * e1[1] + f3 * e1[3]);
            const float rl = __builtin_amdgcn_rcpf(sc16(ls, ls));
            float res[2];
#pragma unroll
            for (int d = 0; d < 2; ++d) {
                f32x4 z = {0.f, 0.f, 0.f, 0.f};
#pragma unroll
                for (int t = 0; t < 3; ++t) z = mfma32(aa[t], bw[d][t], z);
                res[d] = (f0 * z[0] + f1 * z[1]) + (f2 * z[2] + f3 * z[3]);
            }
            const u32x4 o = pack_row(bf1(sc16(res[0], res[1]) * rl));
            const u32 dst = W_CTX + ((u32)r * 8 + x) * 384u + w * 64 + g * 32 + ((lane >> 3) & 1) * 16;
            st128<SC1>(o, rWS, (lane & 39) == 0 ? dst : OOB);
            if (w == 0) TS(10);
        }
    }

    if (typeA) {
        const u32 ks16 = 2 * x + (r >= 10 ? 1 : 0), j = w >> 1;
        const u32 coff = i16 < 8 ? W_CTX + ((2 * ks16 + (j >> 1)) * 8 + i16) * 384u + (j & 1) * 192 + g * 16 : OOB;
        wait_ge<true>(F_LAND, T_UV + 12 * (j + 1), dead);
        u32x4 bw[2][3];
        const u32 fbx = L_A + 2 * BLK * j + (8 * g + q4) * 128 + 8 * p4, fsw = (q4 >> 1) | ((g & 1) << 1);
#pragma unroll
        for (int u = 0; u < 2; ++u) {
            const u32 ub = fbx + ((2 * (w & 1) + u) ^ fsw) * 32;
#pragma unroll
            for (int t = 0; t < 3; ++t) bw[u][t] = cat(trd(ub + (32 * t) * 128), trd(ub + (32 * t + 4) * 128));
        }
        if (w == 0) TS(36);
        PSet<3> ca, pa, pb, pc;
        ppoll<3, STG2>(ca, pa, pb, pc,
            [&](PSet<3>& s) { s.v[0] = ld128<SC1>(rWS, coff); s.v[1] = ld128<SC1>(rWS, coff + 64); s.v[2] = ld128<SC1>(rWS, coff + 128); },
            [&](const PSet<3>& s) { return none_sent(mx4(mx4(mx4(0, s.v[0]), s.v[1]), s.v[2])); }, dead);
        if (w == 0) TS(11);
        float res[2];
#pragma unroll
        for (int u = 0; u < 2; ++u) {
            f32x4 z = {0.f, 0.f, 0.f, 0.f};
#pragma unroll
            for (int t = 0; t < 3; ++t) z = mfma32(ca.v[t], bw[u][t], z);
            res[u] = xg_sum((z[0] + z[1]) + (z[2] + z[3]));
        }
        if (g < 2) lds_st<float>(L_FRP + (w * 32 + 16 * g + i16) * 4, g == 0 ? res[0] : res[1]);
        if (flag_add_ret(F_FRC, lane) == 7) {
            float v = 0.f;
#pragma unroll
            for (int p = 0; p < 4; ++p) v += lds_ld<float>(L_FRP + ((2 * p + (lane >> 5)) * 32 + (lane & 31)) * 4);
            const u32x4 o = pack_row(bf1(v));
            const u32 dst = W_AT + (ks16 * 640u + 64u * (r % 10) + 16 * g + (lane & 8)) * 2u;
            st128<SC1>(o, rWS, (lane & 7) == 0 ? dst : OOB);
            TS(12);
        }
        retire(pa, pb, pc);
    } else {
        const u32 fb = x * 12 + (r - 20);
        const u32 cA = 8 * (lane & 7) + (lane >> 3);
        const bool hasB = (lane & 7) < 2;
        const u32 colA = 80 * w + cA, colB = colA + 64;
        float ga[2], sb[2], cb1[2], gb[2], xv[2];
        {
            xv[0] = __uint_as_float(ld16(rX, colA * 2) << 16); xv[1] = __uint_as_float(ld16(rX, hasB ? colB * 2 : OOB) << 16);
            u32x4 mr[2][2]; u32 spins = 0;
            const u32 posB = 64 + (lane >> 3) * 2 + (lane & 1);
            for (;;) {
                CBAR();
                u32 mx = 0;
#pragma unroll
                for (int kh = 0; kh < 2; ++kh) {
                    mr[kh][0] = ld128<SC1>(rWS, W_MR + ((kh * 8u + w) * 80u + lane) * 16u);
                    mr[kh][1] = ld128<SC1>(rWS, hasB ? W_MR + ((kh * 8u + w) * 80u + posB) * 16u : OOB);
                    mx = mx4(mx4(mx, mr[kh][0]), mr[kh][1]);
                }
                if (none_sent(mx)) break;
                POLL_STEP();
            }
#pragma unroll
            for (int c = 0; c < 2; ++c) {
                ga[c] = __uint_as_float(mr[0][c][0]) + __uint_as_float(mr[1][c][0]);
                sb[c] = __uint_as_float(mr[0][c][1]) + __uint_as_float(mr[1][c][1]);
                cb1[c] = 1.f + (__uint_as_float(mr[0][c][2]) + __uint_as_float(mr[1][c][2]));
                gb[c] = __uint_as_float(mr[0][c][3]) + __uint_as_float(mr[1][c][3]);
            }
        }
        if (w == 0) TS(13);
        const u32 gab = blk + B_GA;
#pragma unroll
        for (int c = 0; c < 2; ++c) {
            if (c == 0 || hasB) {
                const u32 cc = cA + 64 * c;
                const u32 h = bf1(cb1[c]), l = bf1(cb1[c] - __uint_as_float(h << 16)), h2 = bf1(sb[c]), l2 = bf1(sb[c] - __uint_as_float(h2 << 16));
                lds_st<unsigned short>(gab + 2 * 160 + cc * 2, (unsigned short)h); lds_st<unsigned short>(gab + 3 * 160 + cc * 2, (unsigned short)l);
                lds_st<unsigned short>(gab + 4 * 160 + cc * 2, (unsigned short)h2); lds_st<unsigned short>(gab + 5 * 160 + cc * 2, (unsigned short)l2);
                lds_st<float>(blk + B_GB + cc * 4, gb[c]);
            }
        }
        wait_ge<true>(F_LAND, T_UV + 6 * (w + 1), dead);
        u32x2 b1[5], b2[5];
#pragma unroll
        for (int t = 0; t < 5; ++t) b1[t] = trd(blk + B_W + 512 * t + 8 * lane);
#pragma unroll
        for (int u = 0; u < 5; ++u) b2[u] = trd(blk + B_W2 + (4 * g + q4) * 160 + 32 * u + 8 * p4);
        if (w == 0) TS(36);
        const u32 aoff = W_AT + ((lane >> 3) * 640u + 80u * w + 8 * (lane & 7)) * 2u, aoff2 = hasB ? aoff + 128 : OOB;
        PSet<4> at, pa, pb, pc;
        ppoll<4, STG2>(at, pa, pb, pc,
            [&](PSet<4>& s) { s.v[0] = ld128<SC1>(rWS, aoff); s.v[1] = ld128<SC1>(rWS, aoff2); s.v[2] = ld128<SC1>(rWS, aoff + 10240u); s.v[3] = ld128<SC1>(rWS, aoff2 + 10240u); },
            [&](const PSet<4>& s) { return none_sent(mx4(mx4(mx4(mx4(0, s.v[0]), s.v[1]), s.v[2]), s.v[3])); }, dead);
        if (w == 0) TS(14);
        float x1[2];
#pragma unroll
        for (int c = 0; c < 2; ++c) {
            const u32x4 a = at.v[c], a2 = at.v[2 + c];
            float v[8];
#pragma unroll
            for (int j = 0; j < 4; ++j) { v[2 * j] = f_lo(a[j]) + f_lo(a2[j]); v[2 * j + 1] = f_hi(a[j]) + f_hi(a2[j]); }
            float t4[4], t2[2];
#pragma unroll
            for (int j = 0; j < 4; ++j) t4[j] = sc32(v[j], v[j + 4]);
#pragma unroll
            for (int j = 0; j < 2; ++j) t2[j] = sc16(t4[j], t4[j + 2]);
            const bool b3 = (lane & 8) != 0;
            const float keep = b3 ? t2[1] : t2[0], give = b3 ? t2[0] : t2[1];
            const float attn = keep + dppf<ROR(8)>(give);
            x1[c] = xv[c] + ga[c] * attn;
        }
        if (!hasB) x1[1] = 0.f;
#pragma unroll
        for (int c = 0; c < 2; ++c) {
            if (c == 0 || hasB) {
                const u32 cc = cA + 64 * c;
                const float uv = x1[c] * cb1[c];
                const u32 h = bf1(uv), l = bf1(uv - __uint_as_float(h << 16));
                lds_st<unsigned short>(gab + cc * 2, (unsigned short)h); lds_st<unsigned short>(gab + 160 + cc * 2, (unsigned short)l);
                lds_st<float>(blk + B_X1 + cc * 4, x1[c]);
            }
        }
        CBAR();
        f32x4 z = {0.f, 0.f, 0.f, 0.f};
        const u32 arow = (q4 < 2 ? q4 : 2) * 2 + (lane & 1);
#pragma unroll
        for (int t = 0; t < 5; ++t) z = mfma16(lds_ld<u32x2>(gab + arow * 160 + 32 * t + 8 * g), b1[t], z);
        const float s1 = wave_sum(x1[0] + x1[1]), s2 = wave_sum(x1[0] * x1[0] + x1[1] * x1[1]);
        lds_st<float>(L_GRP + w * 256 + lane * 4, g < 3 ? z[0] + z[1] : (p4 == 0 ? s1 : (p4 == 1 ? s2 : 0.f)));
        flag_add(F_GRC, lane);
        wait_ge<false>(F_GRC, 8, dead);
        f32x4 gs = lds_ld<f32x4>(L_GRP + (2 * p4) * 256 + q4 * 64 + g * 16) + lds_ld<f32x4>(L_GRP + (2 * p4 + 1) * 256 + q4 * 64 + g * 16);
#pragma unroll
        for (int e = 0; e < 4; ++e) { gs[e] += dppf<QX1>(gs[e]); gs[e] += dppf<QX2>(gs[e]); }
        const float mu = dppf<NBC(12)>(gs[0]) * (1.f / 640.f);
        const float var = fmaxf(dppf<NBC(12)>(gs[1]) * (1.f / 640.f) - mu * mu, 0.f);
        const float rstd = 1.f / sqrtf(var + 1e-5f);
        float av[4];
#pragma unroll
        for (int e = 0; e < 4; ++e) av[e] = fmaxf(rstd * (dppf<NBC(0)>(gs[e]) - mu * dppf<NBC(4)>(gs[e])) + dppf<NBC(8)>(gs[e]), 0.f);
        u32x2 af; { const u32 h0 = pk2(av[0], av[1]), h1b = pk2(av[2], av[3]);
            const u32 l0 = pk2(av[0] - f_lo(h0), av[1] - f_hi(h0)), l1 = pk2(av[2] - f_lo(h1b), av[3] - f_hi(h1b));
            af[0] = (lane & 1) ? l0 : h0; af[1] = (lane & 1) ? l1 : h1b; }
        float fo[5];
#pragma unroll
        for (int u = 0; u < 5; ++u) { f32x4 y = {0.f, 0.f, 0.f, 0.f}; y = mfma16(af, b2[u], y); fo[u] = y[0] + y[1]; }
        {
            const float v = g == 0 ? fo[0] : (g == 1 ? fo[1] : (g == 2 ? fo[2] : fo[3]));
            const u32x4 o = pack_row(bf1(v)), o2 = pack_row(bf1(fo[4]));
            st128<SC1>(o, rWS, (lane & 7) == 0 ? W_FP + (((5 * w + g) * 96u + fb) * 16 + (lane & 8)) * 2u : OOB);
            st128<SC1>(o2, rWS, (lane & 55) == 0 ? W_FP + (((5 * w + 4) * 96u + fb) * 16 + (lane & 8)) * 2u : OOB);
        }
        if (w == 0) TS(15);
        const int ci = (r - 20) * 8 + x;
        if (ci < 40 && ci / 5 == w) {
            const u32 fo0 = W_FP + (u32)ci * 3072u + lane * 16;
            PSet<3> fv, qa, qb, qc;
            ppoll<3, STG2>(fv, qa, qb, qc,
                [&](PSet<3>& s) { s.v[0] = ld128<SC1>(rWS, fo0); s.v[1] = ld128<SC1>(rWS, fo0 + 1024); s.v[2] = ld128<SC1>(rWS, fo0 + 2048); },
                [&](const PSet<3>& s) { return none_sent(mx4(mx4(mx4(0, s.v[0]), s.v[1]), s.v[2])); }, dead);
            TS(16);
            float v[8];
#pragma unroll
            for (int j = 0; j < 4; ++j) { v[2 * j] = (f_lo(fv.v[0][j]) + f_lo(fv.v[1][j])) + f_lo(fv.v[2][j]); v[2 * j + 1] = (f_hi(fv.v[0][j]) + f_hi(fv.v[1][j])) + f_hi(fv.v[2][j]); }
            float t4[4], t2[2];
#pragma unroll
            for (int j = 0; j < 4; ++j) t4[j] = sc32(v[j], v[j + 4]);
#pragma unroll
            for (int j = 0; j < 2; ++j) t2[j] = sc16(t4[j], t4[j + 2]);
            const bool b3 = (lane & 8) != 0;
            const float keep = b3 ? t2[1] : t2[0], give = b3 ? t2[0] : t2[1];
            float f = keep + dppf<ROR(8)>(give);
            f += dppf<SHL(4)>(f);
            f += dppf<SHL(2)>(f);
            const u32 cc = 16 * (ci % 5) + 8 * (lane & 1) + (lane >> 3);
            const float o = lds_ld<float>(blk + B_X1 + cc * 4) + lds_ld<float>(blk + B_GB + cc * 4) * f;
            const rsrc_t rO = mkrs(p_out, M_ * 2);
            __builtin_amdgcn_raw_buffer_store_b16((unsigned short)bf1(o), rO, (lane & 6) == 0 ? (16 * ci + 8 * (lane & 1) + (lane >> 3)) * 2 : OOB, 0, 0);
            if (ci == 0 && lane == 0) { u32x2 e; e[0] = T1; e[1] = T2; st64<SC1>(e, rWS, W_EPOCH); }
            TS(17);
        }
    }
    TSDUMP();
}

extern "C" __device__ __attribute__((noinline, used)) void adaln_code_end() { asm volatile("s_nop 0"); }

extern "C" void kernel_launch(void* const* d_in, const int* in_sizes, int n_in, void* d_out, int out_size, void* d_ws, size_t ws_size, hipStream_t stream)
{
    (void)in_sizes; (void)n_in; (void)out_size;
    const size_t ws_off = WSOFF < 0 ? ((ws_size - 8388608u) & ~(size_t)4095) : (size_t)WSOFF;
    (void)hipFuncSetAttribute((const void*)adaln_mla_ffn_kernel, hipFuncAttributeMaxDynamicSharedMemorySize, (int)LDS_BYTES);
    hipLaunchKernelGGL(adaln_mla_ffn_kernel, dim3(256), dim3(640), LDS_BYTES, stream,
                       (const void*)d_in[0], (const void*)d_in[1], (const void*)d_in[2], (const void*)d_in[3], (const void*)d_in[4], (const void*)d_in[5],
                       (const void*)d_in[7], (const void*)d_in[8], (const void*)d_in[9], (const void*)d_in[10], d_out, (void*)((char*)d_ws + ws_off));
}
```

```cpp
#include <hip/hip_runtime.h>
#include <hip/hip_bf16.h>
#include <stdint.h>

#define LAS __attribute__((address_space(3)))
#define DI __device__ __forceinline__
typedef unsigned int u32;
typedef unsigned long long u64;
typedef u32 u32x2 __attribute__((ext_vector_type(2)));
typedef u32 u32x3 __attribute__((ext_vector_type(3)));
typedef u32 u32x4 __attribute__((ext_vector_type(4)));
typedef float f32x2 __attribute__((ext_vector_type(2)));
typedef float f32x4 __attribute__((ext_vector_type(4)));
typedef short s16x4 __attribute__((ext_vector_type(4)));
typedef __bf16 bf16x2 __attribute__((ext_vector_type(2)));
typedef __bf16 bf16x8 __attribute__((ext_vector_type(8)));
typedef __amdgpu_buffer_rsrc_t rsrc_t;

#define WQ 30
#define UKA 6
#define TLA 18
#define TLH 99
#define WUV 18
#define OST 0
#define UVA 12
#define WIN3 18
#define WIN4 48
#define OGI 2
#define QABS 0
#define OABS 0
#define G1DLY 96
#define Q2DLY 32
#define WIN2 18
#define PAUSE 1
#define STG 6
#define STG2 3
#define NT_S0 0
#define NT_MR 2
#define NT_Q 1
#define NT_UK 1
#define NT_KV 1
#define NT_UV 1
#define NT_O 1
#define NT_W1 1
#define NT_W2 1
#define LPRIO 3
#define CPF 1
#define FEND "adaln_code_end"
#define KNAME "adaln_mla_ffn_kernel"
#define QDLY 15
#define ODLY 50
#define ELIGHT 1
#define MRAT 0
#define WINT 24
#define QLO 1
#define OGA 0
#define OGB 0
#define QMASK 1
#define WSOFF -1
#define LSLP 0
#define QLIN 1
#define OLIN 0
#define PKM 1
#define CLIN 0
#define BST 4
#define BW2 0
#define BW3 0
#define W2L 1
#define W2AT 1
#define SATM 0
#define GATM 0
#define BLKW 0
#define BPACE 0
#define WINB 48
#define FPF 0
#define FPFAT 0
#define WSM1 0
#define WSM2 0
#define O_BASE 1048576u

constexpr int M_ = 640, H_ = 32, D_ = 192, DC = 768, SK = 8192, DFF = 1536;
constexpr u32 SENT = 0xFFFFFFFFu;
constexpr u32 OOB = 0x80000000u;

constexpr u32 W_EPOCH = 0;
constexpr u32 W_FA = 256;
constexpr u32 W_G1 = 4096;
constexpr u32 W_Q2 = 32768;
constexpr u32 W_SMALL = W_Q2 + 98304;
constexpr u32 W_QA = W_SMALL;
constexpr u32 W_CTX = W_QA + 98304;
constexpr u32 W_AT = W_CTX + 98304;
constexpr u32 W_FP = W_AT + 20480;
constexpr u32 W_MR = W_FP + 122880;
constexpr u32 W_SMALL_END = W_MR + 20480;
constexpr u32 SMALL_CH = (W_SMALL_END - W_SMALL) / 16;
constexpr u32 SMALL_PER = (SMALL_CH + 255) / 256;
constexpr u32 W_O = O_BASE;
constexpr u32 W_PK = W_O + 8u * 128 * 2 * 3072;
constexpr u32 O_BYTES = 8u * 128 * 2 * 3072 + 8u * 128 * 32 * 16;
constexpr u32 W_PROF = W_SMALL_END;
constexpr u32 W_END = W_O + O_BYTES;
static_assert(W_PROF + 256 * 64 * 8 <= W_O, "ws map");
static_assert(SMALL_PER <= 128, "prefill");
static_assert(!QLIN || QLO, "layout");
static_assert(!OLIN || OGA == 0, "the transposition scratch is overwritten by early output-projection blocks");
static_assert(W_END <= 268435456u, "ws map");

constexpr u32 L_A = 0;
constexpr u32 BLK = 12288;
constexpr u32 L_S1W = 5 * BLK;
constexpr u32 L_X = 98304;
constexpr u32 L_Y = 135168;
constexpr u32 L_FLG = L_Y;
constexpr u32 L_S0P = L_Y + 13568;
constexpr u32 L_LAND = L_Y + 14336;
constexpr u32 L_EML = L_Y + 448;
constexpr u32 L_FRP = L_Y + 512;
constexpr u32 L_GRP = L_Y + 1536;
constexpr u32 L_SC = L_Y + 3584;
constexpr u32 L_HX = L_Y + 7680;
constexpr u32 L_QH = L_Y + 8448;
constexpr u32 L_AM = L_Y + 9216;
constexpr u32 L_SIL = L_Y + 12288;
constexpr u32 L_XN = L_Y + 17408;
constexpr u32 LDS_BYTES = 163840;
constexpr u32 B_W = 4096, B_W2 = 7168, B_GA = 10240, B_X1 = 11200, B_GB = 11520;

enum { F_LAND = 0, F_RDYA, F_S0CNT, F_S1DONE, F_HX, F_QH, F_S2DONE, F_BLK, F_QAOK, F_SADD, F_SC, F_AM, F_FRC, F_GRC, F_QAST, F_XN, F_OST, F_OGI, F_CTXST };

constexpr u32 CAP_G = 20000;
constexpr u32 CAP_L = 400000;

constexpr u32 T_S1 = 30, T_S2 = 66, T_TILE0 = 66, T_UV = 198;

DI rsrc_t mkrs(const void* p, u32 bytes) { return __builtin_amdgcn_make_buffer_rsrc((void*)p, (short)0, (int)bytes, 0x00020000); }
template <int AUX> DI u32x4 ld128(rsrc_t r, u32 off) { return __builtin_amdgcn_raw_buffer_load_b128(r, off, 0, AUX); }
template <int AUX> DI u32x3 ld96(rsrc_t r, u32 off) { return __builtin_amdgcn_raw_buffer_load_b96(r, off, 0, AUX); }
template <int AUX> DI u32x2 ld64(rsrc_t r, u32 off) { return __builtin_amdgcn_raw_buffer_load_b64(r, off, 0, AUX); }
template <int AUX> DI u32 ld32(rsrc_t r, u32 off) { return __builtin_amdgcn_raw_buffer_load_b32(r, off, 0, AUX); }
DI u32 ld16(rsrc_t r, u32 off) { return (u32)__builtin_amdgcn_raw_buffer_load_b16(r, off, 0, 0); }
template <int AUX> DI void st128(u32x4 v, rsrc_t r, u32 off) { __builtin_amdgcn_raw_buffer_store_b128(v, r, off, 0, AUX); }
template <int AUX> DI void st96(u32x3 v, rsrc_t r, u32 off) { __builtin_amdgcn_raw_buffer_store_b96(v, r, off, 0, AUX); }
template <int AUX> DI void st64(u32x2 v, rsrc_t r, u32 off) { __builtin_amdgcn_raw_buffer_store_b64(v, r, off, 0, AUX); }
template <int AUX> DI void st32(u32 v, rsrc_t r, u32 off) { __builtin_amdgcn_raw_buffer_store_b32(v, r, off, 0, AUX); }
constexpr int SC1 = 16, NT = 2;
#define PAUX 16
#define PAUXL PAUX
#define G1O 0
#define QHX 135
#define OHX 0
#define CHX 410
#define AHX 470
#define CXDLY 0
#define ATDLY 0
#define FPDLY 12

DI float f_lo(u32 v) { return __uint_as_float(v << 16); }
DI float f_hi(u32 v) { return __uint_as_float(v & 0xFFFF0000u); }
DI u32 pk2(float a, float b) { bf16x2 v; v[0] = (__bf16)a; v[1] = (__bf16)b; return __builtin_bit_cast(u32, v); }
DI u32 bf1(float a) { return pk2(a, 0.f) & 0xFFFFu; }
DI u32 san(u32 v) { return v < 0xFFFFFFFEu ? v : 0xFFFFFFFEu; }
DI u32x4 san4(u32x4 v) { u32x4 r; r[0] = san(v[0]); r[1] = san(v[1]); r[2] = san(v[2]); r[3] = san(v[3]); return r; }
DI u32 umax(u32 a, u32 b) { return a > b ? a : b; }
DI u32 mx4(u32 acc, u32x4 v) { return umax(umax(acc, umax(v[0], v[1])), umax(v[2], v[3])); }

DI u32 tagw(float v, u32 tg) { return ((__float_as_uint(v) + 0x800u) & 0xFFFFF000u) | tg; }
DI float untag(u32 v) { return __uint_as_float(v & 0xFFFFF000u); }
DI bool tag_ok(u32x4 v, u32 tg) { return ((((v[0] ^ tg) | (v[1] ^ tg)) | ((v[2] ^ tg) | (v[3] ^ tg))) & 0xFFFu) == 0u; }
DI float dot2(u32 a, u32 b, float c) { return __builtin_amdgcn_fdot2_f32_bf16(__builtin_bit_cast(bf16x2, a), __builtin_bit_cast(bf16x2, b), c, false); }
template <int CTRL> DI u32 dppu(u32 v) { return (u32)__builtin_amdgcn_update_dpp(0, (int)v, CTRL, 0xF, 0xF, true); }
template <int CTRL> DI float dppf(float v) { return __uint_as_float(dppu<CTRL>(__float_as_uint(v))); }
constexpr int ROR(int n) { return 0x120 + n; }
constexpr int SHL(int n) { return 0x100 + n; }
constexpr int NBC(int n) { return 0x150 + n; }
constexpr int QX1 = 0xB1, QX2 = 0x4E;
DI float row_sum(float v) { v += dppf<ROR(8)>(v); v += dppf<ROR(4)>(v); v += dppf<ROR(2)>(v); v += dppf<ROR(1)>(v); return v; }
DI u32 row_xor(u32 v) { v ^= dppu<ROR(8)>(v); v ^= dppu<ROR(4)>(v); v ^= dppu<ROR(2)>(v); v ^= dppu<ROR(1)>(v); return v; }
DI u32 row_add(u32 v) { v += dppu<ROR(8)>(v); v += dppu<ROR(4)>(v); v += dppu<ROR(2)>(v); v += dppu<ROR(1)>(v); return v; }
DI void swap16(float& a, float& b) { auto r = __builtin_amdgcn_permlane16_swap(__float_as_uint(a), __float_as_uint(b), false, false); a = __uint_as_float(r[0]); b = __uint_as_float(r[1]); }
DI void swap32(float& a, float& b) { auto r = __builtin_amdgcn_permlane32_swap(__float_as_uint(a), __float_as_uint(b), false, false); a = __uint_as_float(r[0]); b = __uint_as_float(r[1]); }
DI float xg_sum(float v) { float a = v, b = v; swap16(a, b); v = a + b; a = v; b = v; swap32(a, b); return a + b; }
DI float xg_max(float v) { float a = v, b = v; swap16(a, b); v = fmaxf(a, b); a = v; b = v; swap32(a, b); return fmaxf(a, b); }
DI u32 xg_xor(u32 v) {
    auto r = __builtin_amdgcn_permlane16_swap(v, v, false, false); v = r[0] ^ r[1];
    auto q = __builtin_amdgcn_permlane32_swap(v, v, false, false); return q[0] ^ q[1];
}
DI float wave_sum(float v) { return xg_sum(row_sum(v)); }
DI float sc32(float a, float b) { swap32(a, b); return a + b; }
DI float sc16(float a, float b) { swap16(a, b); return a + b; }
DI u32 sel4(int e, u32 a, u32 b, u32 c, u32 d) { const u32 lo = (e & 1) ? b : a, hi = (e & 1) ? d : c; return (e & 2) ? hi : lo; }

DI f32x4 mfma32(u32x4 a, u32x4 b, f32x4 c) { return __builtin_amdgcn_mfma_f32_16x16x32_bf16(__builtin_bit_cast(bf16x8, a), __builtin_bit_cast(bf16x8, b), c, 0, 0, 0); }
DI f32x4 mfma16(u32x2 a, u32x2 b, f32x4 c) { return __builtin_amdgcn_mfma_f32_16x16x16bf16_1k(__builtin_bit_cast(s16x4, a), __builtin_bit_cast(s16x4, b), c, 0, 0, 0); }
DI u32x2 trd(u32 addr) { return __builtin_bit_cast(u32x2, __builtin_amdgcn_ds_read_tr16_b64_v4i16((LAS s16x4*)addr)); }
DI u32x4 cat(u32x2 a, u32x2 b) { u32x4 r; r[0] = a[0]; r[1] = a[1]; r[2] = b[0]; r[3] = b[1]; return r; }
template <class T> DI T lds_ld(u32 addr) { return *(LAS T*)addr; }
template <class T> DI void lds_st(u32 addr, T v) { *(LAS T*)addr = v; }
DI void lds_fadd(u32 addr, float v) { __hip_atomic_fetch_add((LAS float*)addr, v, __ATOMIC_RELAXED, __HIP_MEMORY_SCOPE_WORKGROUP); }
#define CBAR() asm volatile("" ::: "memory")

DI u32 flag_ld(int i) { return (u32)__builtin_amdgcn_readfirstlane((int)*(volatile LAS u32*)(i == F_LAND ? L_LAND : L_FLG + 4 * i)); }
DI void flag_st(int i, u32 v, int lane) { CBAR(); if (lane == 0) *(volatile LAS u32*)(L_FLG + 4 * i) = v; CBAR(); }
DI void flag_add(int i, int) {
    u64 sv;
    asm volatile("s_mov_b64 %0, exec\n\ts_and_b64 exec, exec, 1\n\tds_add_u32 %1, %2\n\ts_mov_b64 exec, %0" : "=&s"(sv) : "v"(L_FLG + 4 * i), "v"(1u) : "memory", "scc");
}
DI u32 flag_add_ret(int i, int) {
    u64 sv; u32 o;
    asm volatile("s_mov_b64 %0, exec\n\ts_and_b64 exec, exec, 1\n\tds_add_rtn_u32 %1, %2, %3\n\ts_mov_b64 exec, %0\n\ts_waitcnt lgkmcnt(0)"
                 : "=&s"(sv), "=&v"(o) : "v"(L_FLG + 4 * i), "v"(1u) : "memory", "scc");
    return (u32)__builtin_amdgcn_readfirstlane((int)o);
}
template <bool SLEEP> DI void wait_ge(int i, u32 target, bool& dead) {
    u32 n = 0;
    while (!dead && flag_ld(i) < target) { if (++n > CAP_L) dead = true; if (SLEEP || LSLP) __builtin_amdgcn_s_sleep(1); }
    CBAR();
}
#define WLM 0
#define WLS 0
template <class LD> DI void wait_ld(int i, u32 target, bool& dead, LD ld) {
    if (!((WLM >> i) & 1)) { wait_ge<false>(i, target, dead); ld(); return; }
    for (u32 n = 0;;) {
        CBAR();
        const u32 f = *(volatile LAS u32*)(L_FLG + 4 * i);
        CBAR(); ld(); CBAR();
        if ((u32)__builtin_amdgcn_readfirstlane((int)f) >= target) break;
        if (dead || ++n > CAP_L) { dead = true; break; }
        if (WLS) __builtin_amdgcn_s_sleep(WLS);
    }
}
DI bool all_ok(bool ok) { return __builtin_amdgcn_ballot_w64(!ok) == 0ull; }
DI bool none_sent(u32 mx) { return __builtin_amdgcn_ballot_w64(mx == SENT) == 0ull; }
#define POLL_STEP() { if (++spins > CAP_G) { dead = true; break; } __builtin_amdgcn_s_sleep(1); }

template <int N> struct PSet { u32x4 v[N]; };
template <int N> DI void take(PSet<N>& res, const PSet<N>& s) {
#pragma unroll
    for (int k = 0; k < N; ++k) res.v[k] = s.v[k];
}
#define PSLP 0

template <int N> DI void keep(PSet<N>& s) {
#pragma unroll
    for (int k = 0; k < N; ++k) asm volatile("" : "+v"(s.v[k]));
}
template <int N> DI void hold(const PSet<N>& s) {
#pragma unroll
    for (int k = 0; k < N; ++k) asm volatile("" ::"v"(s.v[k]));
}
template <int N, int STAGGER, class LD, class OK>
DI void ppoll(PSet<N>& res, PSet<N>& a, PSet<N>& b, PSet<N>& c, LD ld, OK ok, bool& dead) {
    (void)c;
    if constexpr (STAGGER > 0) {
        CBAR(); ld(a); CBAR();
        __builtin_amdgcn_s_sleep(STAGGER);
        for (u32 spins = 0;;) {
            CBAR(); ld(b); CBAR();
            if (ok(a)) { take(res, a); keep(res); break; }
            CBAR(); ld(a); CBAR();
            if (ok(b)) { take(res, b); keep(res); break; }
            if (++spins > CAP_G) { dead = true; take(res, b); keep(res); break; }
        }
    } else {
        for (u32 spins = 0;;) {
            CBAR(); ld(res); CBAR();
            if (ok(res)) break;
            if (++spins > CAP_G) { dead = true; break; }
            if (PSLP) __builtin_amdgcn_s_sleep(PSLP);
        }
    }
    CBAR();
}
template <int STAGGER, int N> DI void retire(PSet<N>& a, PSet<N>& b, PSet<N>&) { if constexpr (STAGGER > 0) { hold(a); hold(b); } }
#define PG1 0
#define PQ2 0
#define PCX 0
#define PAT 0
#define PFP 0

#define TS(i) do { } while (0)
#define TCAP(v) do { } while (0)
#define TSV(i, t_) do { } while (0)
#define TSDUMP() do { } while (0)

DI u32x4 pack_row(u32 v16) {
    u32 pr = v16 | (dppu<SHL(1)>(v16) << 16);
    u32x4 r; r[0] = pr; r[1] = dppu<SHL(2)>(pr); r[2] = dppu<SHL(4)>(pr); r[3] = dppu<SHL(6)>(pr);
    return san4(r);
}

DI u32x4 pack_rowf(float v) {
    const u32 pr = san(pk2(v, dppf<SHL(1)>(v)));
    u32x4 r; r[0] = pr; r[1] = dppu<SHL(2)>(pr); r[2] = dppu<SHL(4)>(pr); r[3] = dppu<SHL(6)>(pr);
    return r;
}
constexpr u32 SEL_0 = 0x0C0C0C0Cu, SEL_A = 0x01000C0Cu, SEL_B = 0x03020C0Cu;
DI u32 split2(float a, float b, u32 selA, u32 selB) {
    const u32 h = pk2(a, b);
    return pk2(a - __uint_as_float(__builtin_amdgcn_perm(h, h, selA)), b - __uint_as_float(__builtin_amdgcn_perm(h, h, selB)));
}
DI float resid(float a, u32 sel) { const u32 h = pk2(a, a); return a - __uint_as_float(__builtin_amdgcn_perm(h, h, sel)); }
DI u32x4 sel8(int i16, bool on) {
    const u32 one = on ? ((i16 & 1) ? 0x3F800000u : 0x00003F80u) : 0u, q = (i16 >> 1) & 3;
    u32x4 r; r[0] = q == 0 ? one : 0u; r[1] = q == 1 ? one : 0u; r[2] = q == 2 ? one : 0u; r[3] = q == 3 ? one : 0u;
    return r;
}
constexpr int HMIR = 0x141;

template <int NTL> DI void dma3(u32 lds, u32 v0, u32 v1, u32 v2, rsrc_t rs, u32 soff) {
    if constexpr (NTL) asm volatile("s_mov_b32 m0, %0\n\ts_nop 3\n\tbuffer_load_dwordx4 %1, %4, %5 offen nt lds\n\tbuffer_load_dwordx4 %2, %4, %5 offen offset:1024 nt lds\n\t"
                                    "buffer_load_dwordx4 %3, %4, %5 offen offset:2048 nt lds" ::"s"(lds), "v"(v0), "v"(v1), "v"(v2), "s"(rs), "s"(soff) : "memory");
    else asm volatile("s_mov_b32 m0, %0\n\ts_nop 3\n\tbuffer_load_dwordx4 %1, %4, %5 offen lds\n\tbuffer_load_dwordx4 %2, %4, %5 offen offset:1024 lds\n\t"
                      "buffer_load_dwordx4 %3, %4, %5 offen offset:2048 lds" ::"s"(lds), "v"(v0), "v"(v1), "v"(v2), "s"(rs), "s"(soff) : "memory");
}
template <int N> DI void waitvm() { asm volatile("s_waitcnt vmcnt(%0)" ::"i"(N) : "memory"); }
constexpr int WM0 = WQ > WUV ? WQ : WUV, WM1 = WM0 > WIN2 ? WM0 : WIN2, WM2 = WM1 > WINT ? WM1 : WINT, WM3 = WM2 > WIN3 ? WM2 : WIN3, WM4 = WM3 > WIN4 ? WM3 : WIN4, WMAX = WM4 > WINB ? WM4 : WINB;
struct Ldr { int issued; int pub; u32 ladr; bool dead; };
DI void publish(Ldr& s, int landed) {
    s.pub = landed > s.pub ? landed : s.pub; CBAR(); *(volatile LAS u32*)s.ladr = (u32)s.pub; CBAR(); }
template <int W> DI void step3(Ldr& s) { s.issued += 3; waitvm<W>(); publish(s, s.issued - W); }
constexpr u32 QHT = 2;
template <int N> DI void drain(Ldr& s) { waitvm<N>(); publish(s, s.issued - N); if constexpr (N >= 3) drain<N - 3>(s); }
template <int W> DI void park(Ldr& s, int flag, u32 target) {
    if (flag_ld(flag) < target) { drain<W - 3>(s); wait_ge<true>(flag, target, s.dead); }
    CBAR();
}
template <int W, int NTL> DI void seg3(Ldr& s, u32 lds0, const u32 (&v)[3], rsrc_t rs, u32 soff0, u32 sstep, int ng) {
    u32 so = soff0, ld = lds0;
#pragma unroll 1
    for (int g = 0; g < ng; ++g) {
        dma3<NTL>(ld, v[0], v[1], v[2], rs, so);
        so += sstep; ld += 3072u;
        step3<W>(s);
    }
}

extern __shared__ __attribute__((aligned(16))) char smem_dyn[];

extern "C" __global__ void __launch_bounds__(640, 3)
adaln_mla_ffn_kernel(const void* __restrict__ p_x, const void* __restrict__ p_ckv, const void* __restrict__ p_wq, const void* __restrict__ p_wuk,
                     const void* __restrict__ p_wuv, const void* __restrict__ p_wo, const void* __restrict__ p_w1, const void* __restrict__ p_w2,
                     const void* __restrict__ p_c, const void* __restrict__ p_wmod, void* __restrict__ p_out, void* __restrict__ p_ws)
{
    const int tid = threadIdx.x, lane = tid & 63;
    const int wave = __builtin_amdgcn_readfirstlane(tid >> 6);
    const int b = blockIdx.x, x = b & 7, r = b >> 3;
    const bool typeA = r < 20;
    const int g = lane >> 4, i16 = lane & 15, q4 = i16 >> 2, p4 = lane & 3;
    bool dead = false;
    const u32 t0c = G1DLY ? (u32)__builtin_amdgcn_s_memtime() : 0u;
    (void)smem_dyn;
    asm volatile("" ::"s"(p_x), "s"(p_ckv), "s"(p_wq), "s"(p_wuk), "s"(p_wuv), "s"(p_wo), "s"(p_w1), "s"(p_w2), "s"(p_c), "s"(p_wmod), "s"(p_out), "s"(p_ws));
    const rsrc_t rWS = mkrs(p_ws, W_END);
    const rsrc_t rX = mkrs(p_x, M_ * 2);
    const bool doS0 = typeA && wave < 5;
    const u32 cg = (x & 1) * 20 + r, kp = x >> 1;
    u32x4 m0v, m1v, xa; u32 cv = 0, xb = 0; u32x2 ep;
    if (doS0) {
        const rsrc_t rc = mkrs(p_c, M_ * 2), rm = mkrs(p_wmod, M_ * 6 * M_ * 2);
        const u32 row0 = 160 * kp + 32 * wave + 2 * (lane >> 2);
        const u32 off = row0 * 7680u + cg * 64u + (lane & 3) * 16u;
        m0v = ld128<NT_S0>(rm, off); m1v = ld128<NT_S0>(rm, off + 7680u);
        cv = ld32<0>(rc, row0 * 2);
    }
    if (wave == 5) { xa = ld128<0>(rX, lane * 16); xb = ld32<0>(rX, 1024 + lane * 4); }
    ep[0] = 0; ep[1] = 0;
    if (lane == 0 && wave != 8) ep = ld64<SC1>(rWS, W_EPOCH);
    if (tid >= 384 && tid < 401) { u32x4 z; z[0] = 0; z[1] = 0; z[2] = 0; z[3] = 0; lds_st<u32x4>(tid < 400 ? L_FLG + (tid - 384) * 16 : L_LAND, z); }
    if (tid < 272) { u32x4 z; z[0] = 0; z[1] = 0; z[2] = 0; z[3] = 0; lds_st<u32x4>(tid < 256 ? L_SC + tid * 16 : L_GRP + (tid - 256) * 16, z); }
    asm volatile("s_waitcnt lgkmcnt(0)\n\ts_barrier" ::: "memory");
    TCAP(tbw);

    if (wave == 8) {
        Ldr s; s.issued = 0; s.pub = 0; s.ladr = L_LAND + 4 * lane; s.dead = false;
        __builtin_amdgcn_s_setprio(LPRIO);
        u32 v1[3], vt[3], vu[3];
#pragma unroll
        for (int k = 0; k < 3; ++k) {
            const u32 p = 64 * k + lane;
            const u32 row = p / 12, ch = (p % 12) ^ (((row >> 2) & 1) << 1);
            v1[k] = row * 12288u + ch * 16 - 1024u * k; vt[k] = row * 1536u + ch * 16 - 1024u * k;
            const u32 r2 = p / 24, c2 = (p % 24) ^ (((r2 >> 1) & 3) << 1);
            vu[k] = r2 * 12288u + c2 * 16 - 1024u * k;
        }
        const rsrc_t rq = mkrs(p_wq, M_ * H_ * D_ * 2), ruk = mkrs(p_wuk, DC * H_ * D_ * 2), ruv = mkrs(p_wuv, DC * H_ * D_ * 2), rkv = mkrs(p_ckv, SK * DC * 2);
        const u32 ks = r >> 3, h1 = 4 * x + ((r >> 1) & 3), half = r & 1;
        const u32 so1 = 160u * ks * 12288u + (h1 * 192 + 96 * half) * 2;
        TCAP(tb8);
        seg3<WQ, NT_Q>(s, L_S1W, v1, rq, so1, 16u * 12288u, 10);
        TS(44); TSV(51, t_entry); TSV(54, tb8);
        const u32 so2 = 96u * x * 12288u + (u32)r * 384u;
        const u32 jt = x + 8 * (r & 15);
        const u32 sot = 64u * jt * 1536u;
        bool small = true;
#pragma unroll 1
        for (int gi = 0; gi < 12; ++gi) {
            if (!WSM1 && gi == UKA) { park<WMAX>(s, F_HX, 5); TS(60); }
            if (WSM1 && gi >= UKA - 1 && small) {
                CBAR(); const u32 fr = *(volatile LAS u32*)(L_FLG + 4 * F_HX); CBAR();
                dma3<NT_UK>(L_X + 3072u * gi, vu[0], vu[1], vu[2], ruk, so2 + 8u * 12288u * gi);
                step3<WSM1>(s);
                small = (u32)__builtin_amdgcn_readfirstlane((int)fr) < 5;
            } else {
                dma3<NT_UK>(L_X + 3072u * gi, vu[0], vu[1], vu[2], ruk, so2 + 8u * 12288u * gi);
                step3<WINT>(s);
            }
        }
        TS(45);
#pragma unroll 1
        for (int gi = 0; gi < 32; ++gi) {
            if (gi == 20) park<WMAX>(s, F_S1DONE, 6);
            if (gi == TLH) { park<WMAX>(s, F_HX, 5); TS(60); }
            if (gi == TLA) { park<WMAX>(s, F_QH, QHT); TS(61); }
            if (WSM1 && small) {
                CBAR(); const u32 fr = *(volatile LAS u32*)(L_FLG + 4 * F_HX); CBAR();
                dma3<NT_KV>(L_A + BLK * (gi >> 2) + 3072u * (gi & 3), vt[0], vt[1], vt[2], rkv, sot + 192u * (gi >> 2) + 16u * 1536u * (gi & 3));
                step3<WSM1>(s);
                small = (u32)__builtin_amdgcn_readfirstlane((int)fr) < 5;
                if (!small) TS(60);
            } else {
                dma3<NT_KV>(L_A + BLK * (gi >> 2) + 3072u * (gi & 3), vt[0], vt[1], vt[2], rkv, sot + 192u * (gi >> 2) + 16u * 1536u * (gi & 3));
                step3<WINT>(s);
            }
        }
        TS(46);
        park<WMAX>(s, F_S2DONE, 6);
#pragma unroll 1
        for (int gi = 0; gi < 12; ++gi) {
            if (gi == UVA) { park<WMAX>(s, F_QAOK, 8); TS(47); }
            dma3<NT_UV>(L_X + 3072u * gi, vu[0], vu[1], vu[2], ruv, so2 + 8u * 12288u * gi);
            step3<WUV>(s);
        }
        TS(48);
        park<WMAX>(s, F_BLK, 8);
        if (OGA > 0) { park<WMAX>(s, F_SC, 8); TS(47); }
        if (OST == 1) { wait_ge<true>(F_OST, 8, s.dead); }
        TS(62);
        if (typeA) {
            const rsrc_t ro = mkrs(p_wo, H_ * D_ * M_ * 2);
            u32 vo[2][3];
#pragma unroll
            for (int gp = 0; gp < 2; ++gp)
#pragma unroll
                for (int k = 0; k < 3; ++k) {
                    const u32 f = ((lane >> 4) & 1) | (((gp + k) & 1) << 1);
                    vo[gp][k] = (8u * k + (lane >> 3)) * 1280u + ((((lane & 7) >> 1) ^ f) * 32u) + (lane & 1) * 16u - 1024u * k;
                }
            u32 so = (384u * (2 * x + (r >= 10 ? 1 : 0))) * 1280u + 128u * (r % 10);
#pragma unroll 1
            for (int j = 0; j < 8; ++j) {
                const u32 ld = L_A + 2 * BLK * (j >> 1) + 6144u * (j & 1);
                if (j == 2 * OGA) { park<WMAX>(s, OGI ? F_OGI : F_AM, 8); TS(59); }
                dma3<NT_O>(ld, vo[0][0], vo[0][1], vo[0][2], ro, so);
                if (j < 2 * OGA) step3<WIN3>(s); else step3<WIN4>(s);
                dma3<NT_O>(ld + 3072u, vo[1][0], vo[1][1], vo[1][2], ro, so + 24u * 1280u);
                if (j < 2 * OGA) step3<WIN3>(s); else step3<WIN4>(s);
                so += 48u * 1280u;
            }
        } else {
            const rsrc_t r1 = mkrs(p_w1, M_ * DFF * 2), r2 = mkrs(p_w2, DFF * M_ * 2);
            const u32 fb = x * 12 + (r - 20);
            const u32 va = (lane >> 1) * 3072u + (lane & 1) * 16;
            const u32 v1a[3] = {va, va + 32u * 3072u - 1024u, lane < 32 ? va + 64u * 3072u - 2048u : OOB};
            u32 vb[3];
#pragma unroll
            for (int k = 0; k < 3; ++k) { const u32 p = 64 * k + lane; vb[k] = p < 160 ? (p / 10) * 1280u + (p % 10) * 16 - 1024u * k : OOB; }
            u32 vc[2][3];
#pragma unroll
            for (int par = 0; par < 2; ++par)
#pragma unroll
                for (int k = 0; k < 3; ++k) {
                    const u32 p = 64 * k + lane;
                    vc[par][k] = p < 160 ? (p ^ ((2 * par + (p >= 80 ? 1 : 0)) * 4)) * 16 - 1024u * k : OOB;
                }
            auto load_w2 = [&]() {
#pragma unroll 1
                for (int w = 0; w < 8; w += 2) {
                    dma3<NT_W2>(L_A + BLK * w + B_W2, vc[0][0], vc[0][1], vc[0][2], r2, 16u * fb * 1280u + 2560u * w);
                    step3<WINB>(s);
                    dma3<NT_W2>(L_A + BLK * (w + 1) + B_W2, vc[1][0], vc[1][1], vc[1][2], r2, 16u * fb * 1280u + 2560u * (w + 1));
                    step3<WINB>(s);
                }
            };
            if (W2AT == 1) park<WMAX>(s, F_OST, 8);
            if (W2AT == 2) park<WMAX>(s, F_QAOK, 8);
            if (W2AT) load_w2();
            park<WMAX>(s, F_CTXST, 6); TS(59);
#pragma unroll 1
            for (int w = 0; w < 8; ++w) {
                dma3<NT_W1>(L_A + BLK * w + B_W, v1a[0], v1a[1], v1a[2], r1, 80u * w * 3072u + fb * 32u);
                step3<WINB>(s);
            }
            if (!W2AT) load_w2();
        }
        TS(49);
        drain<WMAX - 3>(s);
        TS(50);
        TSDUMP();
        return;
    }

    if (wave == 9) {
        u32x4 sv; sv[0] = SENT; sv[1] = SENT; sv[2] = SENT; sv[3] = SENT;
        {
            const u32 c0 = (u32)b * SMALL_PER + lane, c1 = c0 + 64;
            st128<SC1>(sv, rWS, c0 < SMALL_CH ? W_SMALL + c0 * 16 : OOB);
            st128<SC1>(sv, rWS, (lane + 64 < (int)SMALL_PER && c1 < SMALL_CH) ? W_SMALL + c1 * 16 : OOB);
        }
        {
            u64 pc; u32 before, len;
            asm volatile("s_getpc_b64 %0\n.Lpf%=:\n\ts_mov_b32 %1, .Lpf%=-" KNAME "\n\ts_mov_b32 %2, " FEND "-" KNAME : "=s"(pc), "=s"(before), "=s"(len));
            const rsrc_t rcode = mkrs((const void*)(pc - before), len < 65536u ? len : 0u);
            const u32x4 c0 = ld128<0>(rcode, (u32)r * 2048u + lane * 16u), c1 = ld128<0>(rcode, (u32)r * 2048u + 1024u + lane * 16u);
            asm volatile("" ::"v"(c0), "v"(c1));
        }
        const int it = 255 - b;
        const rsrc_t rc = mkrs(p_c, M_ * 2), rm = mkrs(p_wmod, M_ * 6 * M_ * 2);
        u32 cs[5];
        if (it < 160) {
#pragma unroll
            for (int k = 0; k < 5; ++k) cs[k] = ld16(rc, (320 * (it & 1) + 64 * k + lane) * 2);
        }
        TCAP(tb9);
        waitvm<0>();
        TSV(52, t_entry); TSV(55, tb9);
        const u64 tag = (((u64)(u32)__builtin_amdgcn_readfirstlane((int)ep[1]) << 32) | (u32)__builtin_amdgcn_readfirstlane((int)ep[0])) + 1ull;
        u32x2 tg; tg[0] = (u32)tag; tg[1] = (u32)(tag >> 32);
        if (lane == 0) st64<SC1>(tg, rWS, W_FA + 8 * b);
        TS(39);
        wait_ge<true>(F_HX, 5, dead);
        {
            u32 spins = 0;
            for (;;) {
                CBAR();
                const u32x4 a = ld128<PAUX>(rWS, W_FA + lane * 16), c = ld128<PAUX>(rWS, W_FA + 1024 + lane * 16);
                const bool ok = a[0] == tg[0] && a[1] == tg[1] && a[2] == tg[0] && a[3] == tg[1] && c[0] == tg[0] && c[1] == tg[1] && c[2] == tg[0] && c[3] == tg[1];
                if (all_ok(ok)) break;
                POLL_STEP();
            }
            flag_st(F_RDYA, 1, lane);
            TS(40);
        }
        if (it < 160) {
            const int cb = it >> 1, kh = it & 1;
#pragma unroll
            for (int k = 0; k < 5; ++k) {
                const float cv = __uint_as_float(cs[k] << 16);
                lds_st<float>(L_SIL + (64 * k + lane) * 4, cv * __builtin_amdgcn_rcpf(1.f + __expf(-cv)));
            }
            if (MRAT == 4) { } else if (MRAT == 5) wait_ge<true>(F_QH, QHT, dead); else if (MRAT == 2) wait_ge<true>(F_AM, 8, dead); else if (MRAT == 3) wait_ge<true>(F_SADD, 8, dead); else if (MRAT) wait_ge<true>(F_LAND, T_TILE0 + 96, dead); else wait_ge<true>(F_QAOK, 8, dead);
            TS(42);
            u32x4 wv[20];
            const u32 vo = (u32)(320 * kh + (lane >> 2)) * 7680u + (1280u + 32u * cb) * 2u + (lane & 3) * 16u;
#pragma unroll
            for (int k = 0; k < 20; ++k) wv[k] = ld128<NT_MR>(rm, vo + (u32)k * 16u * 7680u);
            float acc[8];
#pragma unroll
            for (int j = 0; j < 8; ++j) acc[j] = 0.f;
#pragma unroll
            for (int k = 0; k < 20; ++k) {
                const float sl = lds_ld<float>(L_SIL + (16 * k + (lane >> 2)) * 4);
#pragma unroll
                for (int j = 0; j < 4; ++j) { acc[2 * j] += sl * f_lo(wv[k][j]); acc[2 * j + 1] += sl * f_hi(wv[k][j]); }
            }
            float t4[4], t2[2];
#pragma unroll
            for (int j = 0; j < 4; ++j) t4[j] = sc32(acc[j], acc[j + 4]);
#pragma unroll
            for (int j = 0; j < 2; ++j) t2[j] = sc16(t4[j], t4[j + 2]);
#pragma unroll
            for (int j = 0; j < 2; ++j) { t2[j] += dppf<ROR(8)>(t2[j]); t2[j] += dppf<ROR(4)>(t2[j]); }
            const u32 pm = cb / 20;
#pragma unroll
            for (int j = 0; j < 2; ++j) {
                const u32 col = (cb % 20) * 32u + 8 * (lane & 3) + 4 * (lane >> 5) + 2 * ((lane >> 4) & 1) + j;
                const u32 cw = col / 80, cc = col % 80;
                const u32 pos = cc < 64 ? (cc & 3) * 16 + ((cc >> 2) & 1) * 8 + (cc >> 3) : 64 + (cc & 3) * 4 + ((cc >> 2) & 1) * 2 + ((cc >> 3) & 1);
                st32<SC1>(san(__float_as_uint(t2[j])), rWS, (lane & 12) == 0 ? W_MR + ((kh * 8u + cw) * 80u + pos) * 16u + pm * 4u : OOB);
            }
            TS(43);
        }
        TSDUMP();
        return;
    }

    const int w = wave;
    const u32 ks = r >> 3;
    const bool front = w < 5;
    if (w == 7) { TSV(57, tbw); TSV(58, t_entry); }
    if (w == 0) TSV(30, t_entry);
    if (w == 0) TS(0);
    const u64 tag = (((u64)(u32)__builtin_amdgcn_readfirstlane((int)ep[1]) << 32) | (u32)__builtin_amdgcn_readfirstlane((int)ep[0])) + 1ull;
    const u32 T1 = (u32)tag, T2 = (u32)(tag >> 32);
    const u32 tagl = T1 & 0xFFu, tg12 = T1 & 0xFFFu;

    if (doS0) {
        const float cA = f_lo(cv), cB = f_hi(cv);
        const float sA = cA * __builtin_amdgcn_rcpf(1.f + __expf(-cA)), sB = cB * __builtin_amdgcn_rcpf(1.f + __expf(-cB));
        const u32 sph = pk2(sA, sB), spl = pk2(sA - f_lo(sph), sB - f_hi(sph));
        float acc[8];
#pragma unroll
        for (int j = 0; j < 4; ++j) {
            const u32 pa = __builtin_amdgcn_perm(m1v[j], m0v[j], 0x05040100u), pb = __builtin_amdgcn_perm(m1v[j], m0v[j], 0x07060302u);
            acc[2 * j] = dot2(pa, spl, dot2(pa, sph, 0.f));
            acc[2 * j + 1] = dot2(pb, spl, dot2(pb, sph, 0.f));
        }
        if (w == 0) TS(31);
        float t4[4]; f32x2 t2;
#pragma unroll
        for (int j = 0; j < 4; ++j) t4[j] = sc32(acc[j], acc[j + 4]);
#pragma unroll
        for (int j = 0; j < 2; ++j) { float t = sc16(t4[j], t4[j + 2]); t += dppf<ROR(8)>(t); t += dppf<ROR(4)>(t); t2[j] = t; }
        if ((lane & 12) == 0) lds_st<f32x2>(L_S0P + (w * 32 + 8 * (lane & 3) + 4 * (lane >> 5) + 2 * ((lane >> 4) & 1)) * 4, t2);
        if (flag_add_ret(F_S0CNT, lane) == 4) {
            f32x4 v = lds_ld<f32x4>(L_S0P + (lane & 7) * 16);
#pragma unroll
            for (int p = 1; p < 5; ++p) v += lds_ld<f32x4>(L_S0P + p * 128 + (lane & 7) * 16);
            u32x4 o;
#pragma unroll
            for (int j = 0; j < 4; ++j) o[j] = tagw(v[j], tg12);
            const u32 wh = cg >= 20 ? 1u : 0u, m0 = 32 * (cg - 20 * wh);
            st128<SC1>(o, rWS, lane < 8 ? W_G1 + ((wh * 4 + kp) * 640u + m0 + 4 * lane) * 4u : OOB);
            TS(1);
        }
    }

    const u32 h1 = 4 * x + ((r >> 1) & 3), half = r & 1;
    u32x4 bw[5];
    auto read_bw = [&]() {
        wait_ge<true>(F_LAND, T_S1, dead);
        const u32 ab = L_S1W + (4 * g + q4) * 192 + (((2 * w + (p4 >> 1)) ^ ((g & 1) << 1)) * 16) + 8 * (p4 & 1);
#pragma unroll
        for (int t = 0; t < 5; ++t) bw[t] = cat(trd(ab + (32 * t) * 192), trd(ab + (32 * t + 16) * 192));
        asm volatile("s_waitcnt lgkmcnt(0)" ::: "memory");
        flag_add(F_S1DONE, lane);
        if (w == 0) TS(32);
    };
    if (w == 5) {
        float xv[10];
#pragma unroll
        for (int k = 0; k < 4; ++k) { xv[2 * k] = f_lo(xa[k]); xv[2 * k + 1] = f_hi(xa[k]); }
        xv[8] = f_lo(xb); xv[9] = f_hi(xb);
        float sum = 0.f;
#pragma unroll
        for (int k = 0; k < 10; ++k) sum += xv[k];
        const float mu = wave_sum(sum) * (1.f / 640.f);
        float sq = 0.f;
#pragma unroll
        for (int k = 0; k < 10; ++k) { xv[k] -= mu; sq += xv[k] * xv[k]; }
        const float rstd = __builtin_amdgcn_rsqf(wave_sum(sq) * (1.f / 640.f) + 1e-5f);
        f32x4 n0, n1; f32x2 n2;
#pragma unroll
        for (int k = 0; k < 4; ++k) { n0[k] = xv[k] * rstd; n1[k] = xv[4 + k] * rstd; }
        n2[0] = xv[8] * rstd; n2[1] = xv[9] * rstd;
        lds_st<f32x4>(L_XN + lane * 32, n0); lds_st<f32x4>(L_XN + lane * 32 + 16, n1); lds_st<f32x2>(L_XN + 2048 + lane * 8, n2);
        flag_add(F_XN, lane);
        read_bw();
    }
    if (front) {
        bool got = false;
        if (!G1O) { read_bw(); got = true; }
        wait_ge<true>(F_XN, 1, dead);
        const f32x4 xn4 = lds_ld<f32x4>(L_XN + (160 * ks + 32 * w + 4 * (lane & 7)) * 4);
        float aj[4], bj[4];
#pragma unroll
        for (int j = 0; j < 4; ++j) { aj[j] = lane >= 32 ? xn4[j] : 1.f; bj[j] = lane < 8 ? xn4[j] : 0.f; }
        const u32 goff = W_G1 + ((u32)(lane >> 3) * 640u + 160 * ks + 32 * w + 4 * (lane & 7)) * 4u;
        const u32 hadr = L_HX + ((lane >> 3) & 1) * 320 + (32 * w + 8 * (lane & 3) + 4 * ((lane >> 2) & 1) + 2 * (lane >> 5) + ((lane >> 4) & 1)) * 2;
        PSet<1> gv, pa, pb, pc;
        if (G1O) {
            for (u32 n = 0; n < CAP_L && (u32)__builtin_amdgcn_s_memtime() - t0c < G1DLY * 64u; ++n) {
                if (!got && flag_ld(F_LAND) >= T_S1) { read_bw(); got = true; }
                __builtin_amdgcn_s_sleep(1);
            }
            for (u32 spins = 0;;) {
                CBAR(); gv.v[0] = ld128<PAUX>(rWS, goff); CBAR();
                if (G1O == 1 && !got && flag_ld(F_LAND) >= T_S1) { read_bw(); got = true; }
                if (all_ok(tag_ok(gv.v[0], tg12))) break;
                if (++spins > CAP_G) { dead = true; break; }
            }
            CBAR();
        } else {
        if (G1DLY) for (u32 n = 0; n < CAP_L && (u32)__builtin_amdgcn_s_memtime() - t0c < G1DLY * 64u; ++n) __builtin_amdgcn_s_sleep(1);
        ppoll<1, PG1>(gv, pa, pb, pc,
            [&](PSet<1>& s) { s.v[0] = ld128<PAUX>(rWS, goff); },
            [&](const PSet<1>& s) { return all_ok(tag_ok(s.v[0], tg12)); }, dead);
        }
        if (w == 4) TS(2);
        if (w == 0) TS(19);
        float t[4];
#pragma unroll
        for (int j = 0; j < 4; ++j) t[j] = aj[j] * untag(gv.v[0][j]) + bj[j];
        float hv = sc16(sc32(t[0], t[2]), sc32(t[1], t[3]));
        hv += dppf<ROR(8)>(hv);
        const float hr = resid(hv, (lane & 8) ? SEL_A : SEL_0);
        lds_st<unsigned short>(hadr, (unsigned short)pk2(hr, hr));
        flag_add(F_HX, lane);
        if (!got) read_bw();
        retire<PG1>(pa, pb, pc);
    }
    u32x4 bw2[6];
    u32 tq = 0, thx = 0;
    if (w < 6) {
        u32x4 hx[5];
        wait_ld(F_HX, 5, dead, [&]() {
#pragma unroll
            for (int t = 0; t < 5; ++t) hx[t] = lds_ld<u32x4>(L_HX + (lane & 1) * 320 + 64 * t + 16 * g); });
        thx = (u32)__builtin_amdgcn_s_memtime();
        if (w == 0) TS(18);
        f32x4 acc = {0.f, 0.f, 0.f, 0.f};
#pragma unroll
        for (int t = 0; t < 5; ++t) acc = mfma32(hx[t], bw[t], acc);
        const u32 qb = tagw(acc[0] + acc[1], tg12);
        u32x4 o; o[0] = qb; o[1] = dppu<SHL(1)>(qb); o[2] = dppu<SHL(2)>(qb); o[3] = dppu<SHL(3)>(qb);
        st128<SC1>(o, rWS, (lane & 51) == 0 ? W_Q2 + (ks * 6144u + h1 * 192 + 96 * half + 16 * w + lane) * 4u : OOB);
        if (w == 0) TS(3);
        wait_ge<true>(F_LAND, T_S2, dead);
        const u32 rowb = L_X + (16 * w + i16) * 384, sw = ((i16 >> 1) & 3) << 1;
#pragma unroll
        for (int t = 0; t < 6; ++t) bw2[t] = lds_ld<u32x4>(rowb + (((4 * t + g) ^ sw) * 16));
        flag_add(F_S2DONE, lane);
        if (w == 0) TS(33);
    }
    if (w >= 6) {
        wait_ge<true>(F_HX, 5, dead);
        thx = (u32)__builtin_amdgcn_s_memtime();
        const u32 c0 = 128 * (w - 6) + 4 * i16;
        const u32 qo = W_Q2 + ((u32)g * 6144u + (u32)r * 192u + c0) * 4u, qo2 = w == 6 ? qo + 256 : qo;
        const u32 qadr = L_QH + (c0 + 2 * (lane >> 5) + ((lane >> 4) & 1)) * 2;
        if (Q2DLY) { const u32 th = (u32)__builtin_amdgcn_s_memtime(); for (u32 n = 0; n < CAP_L && (u32)__builtin_amdgcn_s_memtime() - th < Q2DLY * 64u; ++n) __builtin_amdgcn_s_sleep(1); }
        PSet<2> qv1, pa, pb, pc;
        ppoll<2, PQ2>(qv1, pa, pb, pc,
            [&](PSet<2>& s) { s.v[0] = ld128<PAUX>(rWS, qo); if (w == 6) s.v[1] = ld128<PAUX>(rWS, qo2); else s.v[1] = s.v[0]; },
            [&](const PSet<2>& s) { return all_ok(tag_ok(s.v[0], tg12) && tag_ok(s.v[1], tg12)); }, dead);
        if (w == 7) TS(4);
        if (w == 6) TS(34);
#pragma unroll
        for (int k = 0; k < 2; ++k) {
            if (k == 0 || w == 6) {
                const float q = sc16(sc32(untag(qv1.v[k][0]), untag(qv1.v[k][2])), sc32(untag(qv1.v[k][1]), untag(qv1.v[k][3])));
                const u32 hi = pk2(q, q), lo = pk2(q - f_lo(hi), q);
                lds_st<unsigned short>(qadr + 128 * k, (unsigned short)hi); lds_st<unsigned short>(qadr + 128 * k + 384, (unsigned short)lo);
            }
        }
        flag_add(F_QH, lane);
        retire<PQ2>(pa, pb, pc);
    }
    if (w < 6) {
        u32x4 qx[6];
        wait_ld(F_QH, QHT, dead, [&]() {
#pragma unroll
            for (int t = 0; t < 6; ++t) qx[t] = lds_ld<u32x4>(L_QH + (lane & 1) * 384 + 64 * t + 16 * g); });
        if (w == 0) TS(37);
        f32x4 a0 = {0.f, 0.f, 0.f, 0.f}, a1 = {0.f, 0.f, 0.f, 0.f};
#pragma unroll
        for (int t = 0; t < 6; t += 2) { a0 = mfma32(qx[t], bw2[t], a0); a1 = mfma32(qx[t + 1], bw2[t + 1], a1); }
        const float qa = ((a0[0] + a0[1]) + (a1[0] + a1[1])) * (0.07216878364870322f * 1.4426950408889634f);
        const u32x4 o = pack_rowf(resid(qa, (QLO && (g & 1)) ? SEL_A : SEL_0));
        const u32 dst = QLIN ? W_QA + ((((u32)r >> 4) * 8 + x) * 6 + (w >> 1) * 2 + g) * 1024u + (2 * (w & 1) + ((lane >> 3) & 1)) * 256 + (r & 15) * 16
                      : QLO ? W_QA + ((u32)r * 8 + x) * 384u + (w >> 1) * 128 + g * 64 + (w & 1) * 32 + ((lane >> 3) & 1) * 16
                            : W_QA + ((u32)r * 8 + x) * 256u + w * 32 + ((lane >> 3) & 1) * 16;
        wait_ge<false>(F_RDYA, 1, dead);
        if (w == 0) TS(38);
        st128<SC1>(o, rWS, (lane & (QLO ? 39 : 55)) == 0 ? dst : OOB);
        tq = (u32)__builtin_amdgcn_s_memtime();
        flag_add(F_QAST, lane);
        if (w == 0) TS(5);
    }

    const u32 g2 = r >> 4;
    const u32 blk = L_A + BLK * w;
    const u32 t_blk = T_TILE0 + 12 * (w + 1);
    u32x4 vt[6][2];
    {
        constexpr int NQ = QLO ? 6 : 3;
        u32x4 bq[NQ];
        u32x4 av[4][3];
        const u32 qoff = QLIN ? W_QA + (g2 * 8 + w) * 6144u + lane * 16u : W_QA + ((16 * g2 + i16) * 8 + w) * (QLO ? 384u : 256u) + g * 16u;
        auto issue_q = [&]() {
            CBAR();
#pragma unroll
            for (int k = 0; k < NQ; ++k) bq[k] = ld128<PAUX>(rWS, qoff + k * (QLIN ? 1024 : 64));
            CBAR();
        };
        if (w >= 6) { wait_ge<true>(F_QAST, 6, dead); tq = (u32)__builtin_amdgcn_s_memtime(); }
        wait_ge<false>(F_RDYA, 1, dead);
        bool landed = false;
        for (u32 n = 0; !dead;) {
            landed = flag_ld(F_LAND) >= t_blk;
            if (landed || ((u32)__builtin_amdgcn_s_memtime() - tq >= QDLY * 64u && (u32)__builtin_amdgcn_s_memtime() - thx >= QHX * 64u)) break;
            if (++n > CAP_L) dead = true;
            __builtin_amdgcn_s_sleep(1);
        }
        if (!landed) {
            if (w == 0) TS(24);
            if (w == 7) TS(25);
            issue_q(); wait_ge<true>(F_LAND, t_blk, dead);
        }
        if (w == 0) TS(6);
        if (w == 7) TS(27);
        const u32 ar = blk + i16 * 192, asw = ((i16 >> 2) & 1) << 1;
#pragma unroll
        for (int kb = 0; kb < 4; ++kb)
#pragma unroll
            for (int t = 0; t < 3; ++t) av[kb][t] = lds_ld<u32x4>(ar + kb * 16 * 192 + ((4 * t + g) ^ asw) * 16);
        const u32 vb = blk + (4 * g + q4) * 192 + (p4 >> 1) * 16 + 8 * (p4 & 1);
#pragma unroll
        for (int u = 0; u < 6; ++u) {
            const u32 cu = vb + (u ^ (g & 1)) * 32;
#pragma unroll
            for (int t = 0; t < 2; ++t) vt[u][t] = cat(trd(cu + (32 * t) * 192), trd(cu + (32 * t + 16) * 192));
        }
        if (BLKW) asm volatile("s_waitcnt lgkmcnt(0)" ::: "memory");
        flag_add(F_BLK, lane);
        if (w == 7) TS(28);
        if (landed) {
            for (u32 n = 0; n < CAP_L && ((u32)__builtin_amdgcn_s_memtime() - tq < QDLY * 64u || (u32)__builtin_amdgcn_s_memtime() - thx < QHX * 64u); ++n) __builtin_amdgcn_s_sleep(1);
            if (w == 0) TS(24);
            if (w == 7) TS(25);
            issue_q();
        }
        for (u32 spins = 0;;) {
            u32 mx = 0;
#pragma unroll
            for (int k = 0; k < NQ; ++k) mx = mx4(mx, bq[k]);
            if (none_sent(mx)) break;
            POLL_STEP();
            if (QMASK) { if (mx == SENT) issue_q(); } else issue_q();
        }
        flag_add(F_QAOK, lane);
        if (w == 0) TS(7);
        if (w == 7) TS(26);
#pragma unroll
        for (int kb = 0; kb < 4; ++kb) {
            f32x4 acc = {0.f, 0.f, 0.f, 0.f};
#pragma unroll
            for (int t = 0; t < 3; ++t) { if (QLO) { acc = mfma32(av[kb][t], bq[(2 * t + 1) % NQ], acc); acc = mfma32(av[kb][t], bq[(2 * t) % NQ], acc); } else acc = mfma32(av[kb][t], bq[t], acc); }
            if (SATM) {
#pragma unroll
                for (int j = 0; j < 4; ++j) lds_fadd(L_SC + (kb * 4 + j) * 256 + lane * 4, acc[j]);
            } else {
                f32x2 lo2, hi2; lo2[0] = acc[0]; lo2[1] = acc[1]; hi2[0] = acc[2]; hi2[1] = acc[3];
                lds_st<f32x2>(blk + (kb * 2) * 512 + lane * 8, lo2);
                lds_st<f32x2>(blk + (kb * 2 + 1) * 512 + lane * 8, hi2);
            }
        }
        flag_add(F_SADD, lane);
        if (w == 0) TS(20);
    }
    {
        f32x4 sc[4];
        if (SATM) {
            wait_ld(F_SADD, 8, dead, [&]() {
#pragma unroll
                for (int q = 0; q < 4; ++q)
#pragma unroll
                    for (int j = 0; j < 4; ++j) sc[q][j] = lds_ld<float>(L_SC + (q * 4 + j) * 256 + lane * 4); });
            if (w == 0) TS(21);
        } else {
            f32x2 sp[8];
            wait_ld(F_SADD, 8, dead, [&]() {
#pragma unroll
                for (int p = 0; p < 8; ++p) sp[p] = lds_ld<f32x2>(L_A + BLK * p + w * 512 + lane * 8); });
            if (w == 0) TS(21);
            const f32x2 s = ((sp[0] + sp[1]) + (sp[2] + sp[3])) + ((sp[4] + sp[5]) + (sp[6] + sp[7]));
            lds_st<f32x2>(L_SC + (w >> 1) * 1024 + lane * 16 + (w & 1) * 8, s);
            flag_add(F_SC, lane);
            wait_ld(F_SC, 8, dead, [&]() {
#pragma unroll
                for (int q = 0; q < 4; ++q) sc[q] = lds_ld<f32x4>(L_SC + q * 1024 + lane * 16); });
        }
        if (w == 0) TS(22);
        float mloc = fmaxf(fmaxf(fmaxf(sc[0][0], sc[0][1]), fmaxf(sc[0][2], sc[0][3])), fmaxf(fmaxf(sc[1][0], sc[1][1]), fmaxf(sc[1][2], sc[1][3])));
        mloc = fmaxf(mloc, fmaxf(fmaxf(fmaxf(sc[2][0], sc[2][1]), fmaxf(sc[2][2], sc[2][3])), fmaxf(fmaxf(sc[3][0], sc[3][1]), fmaxf(sc[3][2], sc[3][3]))));
        mloc = xg_max(mloc);
        u32x4 pf[2];
#pragma unroll
        for (int t = 0; t < 2; ++t)
#pragma unroll
            for (int e = 0; e < 4; ++e) {
                const f32x4 sq = sc[2 * t + (e >> 1)];
                pf[t][e] = pk2(__builtin_amdgcn_exp2f(sq[2 * (e & 1)] - mloc), __builtin_amdgcn_exp2f(sq[2 * (e & 1) + 1] - mloc));
            }
        f32x4 oa[6], lz;
#pragma unroll
        for (int u = 0; u < 6; ++u) { f32x4 z = {0.f, 0.f, 0.f, 0.f}; oa[u] = mfma32(vt[u][0], pf[0], z); oa[u] = mfma32(vt[u][1], pf[1], oa[u]); }
        {
            u32x4 one; one[0] = 0x3F803F80u; one[1] = 0x3F803F80u; one[2] = 0x3F803F80u; one[3] = 0x3F803F80u;
            f32x4 z = {0.f, 0.f, 0.f, 0.f};
            lz = mfma32(one, pf[0], z); lz = mfma32(one, pf[1], lz);
        }
        if (w == 0) TS(23);
        const u32 jj = x * 16 + (r & 15);
        const u32 ob = W_O + ((w * 128 + jj) * 2 + g2) * 3072u + (OLIN ? (u32)lane : (u32)(i16 * 4 + g)) * 16;
        u32 cx = 0;
        if (OLIN) {
            const u32 wa = blk + (i16 * 4 + (g ^ q4)) * 16, ra = blk + ((lane & 60) + ((lane & 3) ^ (lane >> 4))) * 16;
#pragma unroll
            for (int k = 0; k < 3; ++k) {
                u32x4 d;
                d[0] = pk2(oa[2 * k][0], oa[2 * k][1]); d[1] = pk2(oa[2 * k][2], oa[2 * k][3]);
                d[2] = pk2(oa[2 * k + 1][0], oa[2 * k + 1][1]); d[3] = pk2(oa[2 * k + 1][2], oa[2 * k + 1][3]);
                lds_st<u32x4>(wa + k * 1024, d);
                cx ^= (d[0] ^ d[1]) ^ (d[2] ^ d[3]);
            }
            CBAR();
            u32x4 e[3];
#pragma unroll
            for (int k = 0; k < 3; ++k) e[k] = lds_ld<u32x4>(ra + k * 1024);
#pragma unroll
            for (int k = 0; k < 3; ++k) st128<SC1>(e[k], rWS, ob + k * 1024);
        } else {
#pragma unroll
        for (int k = 0; k < 3; ++k) {
            u32x4 d;
            d[0] = pk2(oa[2 * k][0], oa[2 * k][1]); d[1] = pk2(oa[2 * k][2], oa[2 * k][3]);
            d[2] = pk2(oa[2 * k + 1][0], oa[2 * k + 1][1]); d[3] = pk2(oa[2 * k + 1][2], oa[2 * k + 1][3]);
            st128<SC1>(d, rWS, ob + k * 1024);
            cx ^= (d[0] ^ d[1]) ^ (d[2] ^ d[3]);
        }
        }
        cx = xg_xor(cx);
        u32x4 pk;
        pk[0] = __float_as_uint(mloc); pk[1] = __float_as_uint(lz[0]);
        pk[2] = cx ^ pk[0] ^ pk[1] ^ T2; pk[3] = T1;
        st128<SC1>(pk, rWS, g == 0 ? W_PK + ((w * 128 + jj) * 32 + 16 * g2 + i16) * 16 : OOB);
        if (OST || BST == 2 || BW3 > BW2 || (W2L && W2AT == 1)) flag_add(F_OST, lane);
        if (w == 0) TS(8);
        if (w == 7) TS(29);
    }

    {
        const bool pkl = !PKM || i16 < 4;
        const u32 poff = pkl ? W_PK + ((x * 128 + 16 * w + 4 * g + (i16 & 3)) * 32 + r) * 16 : OOB;
        const u32 ooff = i16 < 12 ? W_O + ((x * 128 + 16 * w + 4 * g) * 2 + g2) * 3072u + (i16 >> 2) * 1024 + ((r & 15) * 4 + (i16 & 3)) * 16 : OOB;
        u32x4 bw[2][3];
        if (w < 6) {
            wait_ge<true>(F_LAND, T_UV, dead);
            const u32 sw = 2 * (g & 1) + (q4 >> 1);
#pragma unroll
            for (int d = 0; d < 2; ++d) {
                const u32 ub = L_X + (4 * g + q4) * 384 + (2 * ((2 * w + d) ^ sw) + (p4 >> 1)) * 16 + 8 * (p4 & 1);
#pragma unroll
                for (int t = 0; t < 3; ++t) bw[d][t] = cat(trd(ub + (32 * t) * 384), trd(ub + (32 * t + 16) * 384));
            }
        }
        if (ODLY) __builtin_amdgcn_s_sleep(ODLY);
        if (OHX) for (u32 n = 0; n < CAP_L && (u32)__builtin_amdgcn_s_memtime() - thx < OHX * 64u; ++n) __builtin_amdgcn_s_sleep(1);
        u32x4 pk, od[4];
        if (w == 0) TS(53);
        {
            u32 spins = 0;
            for (;;) {
                CBAR(); pk = ld128<PAUXL>(rWS, poff); CBAR();
                if (all_ok(!pkl || pk[3] == T1)) break;
                if (w == 0 && spins == 0) TS(56);
                POLL_STEP();
            }
        }
        if (w == 0) TS(35);
        if (w == 7) TS(41);
        {
            u32 spins = 0;
            u64 bad = ~0ull;
            for (;;) {
                CBAR();
#pragma unroll
                for (int k = 0; k < 4; ++k) if (bad & (0x1111111111111111ull << k)) od[k] = ld128<PAUXL>(rWS, ooff + k * 6144);
                if (!ELIGHT || spins) pk = ld128<PAUXL>(rWS, poff);
                CBAR();
                if (OGI && spins == 0) flag_add(F_OGI, lane);
                u32 c[4];
#pragma unroll
                for (int k = 0; k < 4; ++k) c[k] = (od[k][0] ^ od[k][1]) ^ (od[k][2] ^ od[k][3]);
                const bool b0 = lane & 1, b1 = lane & 2;
                const u32 t01 = (b0 ? c[1] : c[0]) ^ dppu<QX1>(b0 ? c[0] : c[1]), t23 = (b0 ? c[3] : c[2]) ^ dppu<QX1>(b0 ? c[2] : c[3]);
                u32 t = (b1 ? t23 : t01) ^ dppu<QX2>(b1 ? t01 : t23);
                t ^= dppu<ROR(4)>(t); t ^= dppu<ROR(8)>(t);
                const bool ok = !pkl || (pk[3] == T1 && (t ^ pk[0] ^ pk[1] ^ T2) == pk[2]);
                bad = __builtin_amdgcn_ballot_w64(!ok);
                if (bad == 0ull) break;
                POLL_STEP();
            }
        }
        if (w == 0) TS(9);
        if (w == 7) TS(63);
        const float mj = __uint_as_float(pk[0]);
        float Mw = fmaxf(mj, dppf<QX1>(mj)); Mw = fmaxf(Mw, dppf<QX2>(Mw)); Mw = xg_max(Mw);
        const u32 wb = bf1(__builtin_amdgcn_exp2f(mj - Mw));
        const u32 sh = (lane & 1) ? 65536u : 1u;
        u32x4 aw;
        aw[0] = __umul24(dppu<0x00>(wb), sh); aw[1] = __umul24(dppu<0x55>(wb), sh); aw[2] = __umul24(dppu<0xAA>(wb), sh); aw[3] = __umul24(dppu<0xFF>(wb), sh);
        u32x4 yv;
#pragma unroll
        for (int u = 0; u < 4; ++u) {
            u32x4 bo; bo[0] = od[0][u]; bo[1] = od[1][u]; bo[2] = od[2][u]; bo[3] = od[3][u];
            f32x4 z = {0.f, 0.f, 0.f, 0.f};
            z = mfma32(aw, bo, z);
            yv[u] = pk2(z[0], z[1]);
        }
        float lw = __uint_as_float(wb << 16) * __uint_as_float(pk[1]);
        lw += dppf<QX1>(lw); lw += dppf<QX2>(lw); lw = xg_sum(lw);
        if (lane < 12) lds_st<u32x4>(L_AM + w * 208 + lane * 16, yv);
        { f32x2 ml; ml[0] = Mw; ml[1] = lw; if (lane == 0) lds_st<f32x2>(L_EML + w * 8, ml); }
        flag_add(F_AM, lane);
        if (w < 6) {
            u32x4 aa[3]; f32x4 e0, e1;
            wait_ld(F_AM, 8, dead, [&]() {
#pragma unroll
                for (int t = 0; t < 3; ++t) aa[t] = lds_ld<u32x4>(L_AM + (i16 & 7) * 208 + (4 * t + g) * 16);
                e0 = lds_ld<f32x4>(L_EML + (g & 1) * 32); e1 = lds_ld<f32x4>(L_EML + (g & 1) * 32 + 16); });
            float Mx = fmaxf(fmaxf(e0[0], e0[2]), fmaxf(e1[0], e1[2]));
            { float a = Mx, bb = Mx; swap16(a, bb); Mx = fmaxf(a, bb); }
            const float f0 = __builtin_amdgcn_exp2f(e0[0] - Mx), f1 = __builtin_amdgcn_exp2f(e0[2] - Mx), f2 = __builtin_amdgcn_exp2f(e1[0] - Mx), f3 = __builtin_amdgcn_exp2f(e1[2] - Mx);
            const float ls = (f0 * e0[1] + f1 * e0[3]) + (f2 * e1[1] + f3 * e1[3]);
            const float rl = __builtin_amdgcn_rcpf(sc16(ls, ls));
            float res[2];
#pragma unroll
            for (int d = 0; d < 2; ++d) {
                f32x4 z = {0.f, 0.f, 0.f, 0.f};
#pragma unroll
                for (int t = 0; t < 3; ++t) z = mfma32(aa[t], bw[d][t], z);
                res[d] = (f0 * z[0] + f1 * z[1]) + (f2 * z[2] + f3 * z[3]);
            }
            const u32x4 o = pack_rowf(sc16(res[0], res[1]) * rl);
            const u32 dst = CLIN ? W_CTX + ((u32)r * 24 + 4 * w + 2 * g + ((lane >> 3) & 1)) * 128u + x * 16
                                 : W_CTX + ((u32)r * 8 + x) * 384u + w * 64 + g * 32 + ((lane >> 3) & 1) * 16;
            st128<SC1>(o, rWS, (lane & 39) == 0 ? dst : OOB);
            if (BST == 4) flag_add(F_CTXST, lane);
            if (w == 0) TS(10);
        }
    }

    if (typeA) {
        const u32 ks16 = 2 * x + (r >= 10 ? 1 : 0), j = w >> 1;
        const u32 coff = i16 >= 8 ? OOB : CLIN ? W_CTX + ((2 * ks16 + (j >> 1)) * 24 + (j & 1) * 12 + g) * 128u + i16 * 16
                                               : W_CTX + ((2 * ks16 + (j >> 1)) * 8 + i16) * 384u + (j & 1) * 192 + g * 16;
        wait_ge<true>(F_LAND, T_UV + 12 * (j + 1), dead);
        u32x4 bw[2][3];
        const u32 fbx = L_A + 2 * BLK * j + (8 * g + q4) * 128 + 8 * p4, fsw = (q4 >> 1) | ((g & 1) << 1);
#pragma unroll
        for (int u = 0; u < 2; ++u) {
            const u32 ub = fbx + ((2 * (w & 1) + u) ^ fsw) * 32;
#pragma unroll
            for (int t = 0; t < 3; ++t) bw[u][t] = cat(trd(ub + (32 * t) * 128), trd(ub + (32 * t + 4) * 128));
        }
        if (w == 0) TS(36);
        if (CXDLY) __builtin_amdgcn_s_sleep(CXDLY);
        if (CHX) for (u32 n = 0; n < CAP_L && (u32)__builtin_amdgcn_s_memtime() - thx < CHX * 64u; ++n) __builtin_amdgcn_s_sleep(1);
        PSet<3> ca, pa, pb, pc;
        ppoll<3, PCX>(ca, pa, pb, pc,
            [&](PSet<3>& s) { s.v[0] = ld128<PAUXL>(rWS, coff); s.v[1] = ld128<PAUXL>(rWS, coff + (CLIN ? 512 : 64)); s.v[2] = ld128<PAUXL>(rWS, coff + (CLIN ? 1024 : 128)); },
            [&](const PSet<3>& s) { return none_sent(mx4(mx4(mx4(0, s.v[0]), s.v[1]), s.v[2])); }, dead);
        if (w == 0) TS(11);
        float res[2];
#pragma unroll
        for (int u = 0; u < 2; ++u) {
            f32x4 z = {0.f, 0.f, 0.f, 0.f};
#pragma unroll
            for (int t = 0; t < 3; ++t) z = mfma32(ca.v[t], bw[u][t], z);
            res[u] = (z[0] + z[1]) + (z[2] + z[3]);
        }
        const float rs = sc16(res[0], res[1]);
        if (g < 2) lds_st<float>(L_FRP + (w * 32 + 16 * g + i16) * 4, rs);
        if (flag_add_ret(F_FRC, lane) == 7) {
            float v = 0.f;
#pragma unroll
            for (int p = 0; p < 4; ++p) v += lds_ld<float>(L_FRP + ((2 * p + (lane >> 5)) * 32 + (lane & 31)) * 4);
            const u32x4 o = pack_rowf(v);
            const u32 dst = W_AT + (ks16 * 640u + 64u * (r % 10) + 16 * g + (lane & 8)) * 2u;
            st128<SC1>(o, rWS, (lane & 7) == 0 ? dst : OOB);
            TS(12);
        }
        retire<PCX>(pa, pb, pc);
    } else {
        const u32 fb = x * 12 + (r - 20);
        const u32 ch = i16 & 7, up = i16 >> 3;
        const bool valid = g < 2 || ch < 2;
        const u32 cc = g < 2 ? 8 * ch + 4 * g : 64 + 8 * ch + 4 * (g - 2);
        const u32 sA8 = up ? SEL_A : SEL_0, sB8 = up ? SEL_B : SEL_0, sA1 = (lane & 1) ? SEL_A : SEL_0, sB1 = (lane & 1) ? SEL_B : SEL_0;
        const u32 gab = blk + B_GA;
        float ga[4], cb1[4], xv[4];
        {
            const u32x2 xr = ld64<0>(rX, valid ? (80 * w + cc) * 2 : OOB);
            const u32 mo = valid ? W_MR + ((up * 8u + w) * 80u + (g < 2 ? g * 8 + ch : 64 + (g - 2) * 2 + ch)) * 16u : OOB, ms = g < 2 ? 256 : 64;
            u32x4 mr[4]; u32 spins = 0;
            for (;;) {
                CBAR();
                u32 mx = 0;
#pragma unroll
                for (int j = 0; j < 4; ++j) { mr[j] = ld128<PAUXL>(rWS, mo + j * ms); mx = mx4(mx, mr[j]); }
                if (none_sent(mx)) break;
                POLL_STEP();
            }
            float sb[4]; f32x4 gb;
#pragma unroll
            for (int j = 0; j < 4; ++j) {
                float t[4];
#pragma unroll
                for (int e = 0; e < 4; ++e) { t[e] = __uint_as_float(mr[j][e]); t[e] += dppf<ROR(8)>(t[e]); }
                ga[j] = t[0]; sb[j] = t[1]; cb1[j] = 1.f + t[2]; gb[j] = t[3];
            }
            xv[0] = f_lo(xr[0]); xv[1] = f_hi(xr[0]); xv[2] = f_lo(xr[1]); xv[3] = f_hi(xr[1]);
            if (w == 0) TS(13);
            u32x2 c2, s2;
            c2[0] = split2(cb1[0], cb1[1], sA8, sB8); c2[1] = split2(cb1[2], cb1[3], sA8, sB8);
            s2[0] = split2(sb[0], sb[1], sA8, sB8); s2[1] = split2(sb[2], sb[3], sA8, sB8);
            if (valid) {
                lds_st<u32x2>(gab + (2 + up) * 160 + cc * 2, c2); lds_st<u32x2>(gab + (4 + up) * 160 + cc * 2, s2);
                lds_st<f32x4>(blk + B_GB + cc * 4, gb);
            }
        }
        u32x4 selA = sel8(i16, i16 < 8), selB = sel8(i16, i16 >= 8);
        u32 ua = gab + up * 160 + cc * 2, xa1 = blk + B_X1 + cc * 4;
        u32 ara = gab + ((q4 < 2 ? q4 : 2) * 2 + (lane & 1)) * 160 + 8 * g;
        u32 gra = GATM ? L_GRP + g * 16 : L_GRP + (2 * p4) * 256 + q4 * 64 + g * 16, gwa = GATM ? L_GRP + lane * 4 : L_GRP + w * 256 + lane * 4;
        u32 fpo = (i16 & 1) == 0 && i16 < 10 ? W_FP + ((5 * w + (i16 >> 1)) * 96u + fb) * 32u + 8 * g : OOB;
        u32x2 b1[5], b2[5];
        u32 a2[5];
#pragma unroll
        for (int u = 0; u < 5; ++u) a2[u] = L_A + BLK * (2 * g + (q4 >> 1)) + B_W2 + (q4 & 1) * 1280 + ((10 * w + 2 * u + (p4 >> 1)) ^ (4 * q4)) * 16 + 8 * (p4 & 1);
        asm volatile("" : "+v"(selA), "+v"(selB), "+v"(ua), "+v"(xa1), "+v"(ara), "+v"(gra), "+v"(gwa), "+v"(fpo));
        asm volatile("" : "+v"(a2[0]), "+v"(a2[1]), "+v"(a2[2]), "+v"(a2[3]), "+v"(a2[4]));
        if (W2AT) {
            wait_ge<true>(F_LAND, T_UV + 24, dead);
#pragma unroll
            for (int u = 0; u < 5; ++u) b2[u] = trd(a2[u]);
        }
        wait_ge<true>(F_LAND, T_UV + (W2AT ? 24 : 0) + 3 * (w + 1), dead);
#pragma unroll
        for (int t = 0; t < 5; ++t) b1[t] = trd(blk + B_W + 512 * t + 8 * lane);
        if (w == 0) TS(36);
        const u32 aoff = W_AT + ((lane >> 3) * 640u + 80u * w + 8 * (lane & 7)) * 2u, aoff2 = (lane & 7) < 2 ? aoff + 128 : OOB;
        if (ATDLY) __builtin_amdgcn_s_sleep(ATDLY);
        if (AHX) for (u32 n = 0; n < CAP_L && (u32)__builtin_amdgcn_s_memtime() - thx < AHX * 64u; ++n) __builtin_amdgcn_s_sleep(1);
        PSet<4> at, pa, pb, pc;
        ppoll<4, PAT>(at, pa, pb, pc,
            [&](PSet<4>& s) { s.v[0] = ld128<PAUXL>(rWS, aoff); s.v[1] = ld128<PAUXL>(rWS, aoff2); s.v[2] = ld128<PAUXL>(rWS, aoff + 10240u); s.v[3] = ld128<PAUXL>(rWS, aoff2 + 10240u); },
            [&](const PSet<4>& s) { return none_sent(mx4(mx4(mx4(mx4(0, s.v[0]), s.v[1]), s.v[2]), s.v[3])); }, dead);
        if (w == 0) TS(14);
        f32x4 sa = {0.f, 0.f, 0.f, 0.f};
        sa = mfma32(selA, at.v[0], sa); sa = mfma32(selA, at.v[2], sa); sa = mfma32(selB, at.v[1], sa); sa = mfma32(selB, at.v[3], sa);
        f32x4 x1; float uv[4];
#pragma unroll
        for (int j = 0; j < 4; ++j) { const float attn = sa[j] + dppf<ROR(8)>(sa[j]); x1[j] = xv[j] + ga[j] * attn; uv[j] = x1[j] * cb1[j]; }
        {
            u32x2 u2; u2[0] = split2(uv[0], uv[1], sA8, sB8); u2[1] = split2(uv[2], uv[3], sA8, sB8);
            if (valid) { lds_st<u32x2>(ua, u2); lds_st<f32x4>(xa1, x1); }
        }
        CBAR();
        f32x4 z = {0.f, 0.f, 0.f, 0.f};
#pragma unroll
        for (int t = 0; t < 5; ++t) z = mfma16(lds_ld<u32x2>(ara + 32 * t), b1[t], z);
        float st = up ? (x1[0] * x1[0] + x1[1] * x1[1]) + (x1[2] * x1[2] + x1[3] * x1[3]) : (x1[0] + x1[1]) + (x1[2] + x1[3]);
        st += dppf<HMIR>(st); st += dppf<QX1>(st); st += dppf<QX2>(st);
        st = xg_sum(st);
        { const float ot = dppf<ROR(8)>(st); st = (up == (u32)(lane & 1)) ? st : ot; }
        if (GATM) lds_fadd(gwa, g < 3 ? z[0] + z[1] : st); else lds_st<float>(gwa, g < 3 ? z[0] + z[1] : st);
        flag_add(F_GRC, lane);
        if (!W2AT) {
            wait_ge<false>(F_LAND, T_UV + 48, dead);
#pragma unroll
            for (int u = 0; u < 5; ++u) b2[u] = trd(a2[u]);
        }
        float av[4];
        if (GATM) {
            f32x4 tu, tc, ts, tt;
            wait_ld(F_GRC, 8, dead, [&]() { tt = lds_ld<f32x4>(gra + 192); tu = lds_ld<f32x4>(gra); tc = lds_ld<f32x4>(gra + 64); ts = lds_ld<f32x4>(gra + 128); });
            const float mu = tt[0] * (1.f / 640.f);
            const float var = fmaxf(tt[1] * (1.f / 640.f) - mu * mu, 0.f);
            const float rstd = __builtin_amdgcn_rsqf(var + 1e-5f);
#pragma unroll
            for (int e = 0; e < 4; ++e) av[e] = fmaxf(rstd * (tu[e] - mu * tc[e]) + ts[e], 0.f);
        } else {
            f32x4 gs, gs2;
            wait_ld(F_GRC, 8, dead, [&]() { gs = lds_ld<f32x4>(gra); gs2 = lds_ld<f32x4>(gra + 256); });
            gs += gs2;
#pragma unroll
            for (int e = 0; e < 4; ++e) { gs[e] += dppf<QX1>(gs[e]); gs[e] += dppf<QX2>(gs[e]); }
            const float mu = dppf<NBC(12)>(gs[0]) * (1.f / 640.f);
            const float var = fmaxf(dppf<NBC(12)>(gs[1]) * (1.f / 640.f) - mu * mu, 0.f);
            const float rstd = __builtin_amdgcn_rsqf(var + 1e-5f);
#pragma unroll
            for (int e = 0; e < 4; ++e) av[e] = fmaxf(rstd * (dppf<NBC(0)>(gs[e]) - mu * dppf<NBC(4)>(gs[e])) + dppf<NBC(8)>(gs[e]), 0.f);
        }
        u32x2 af; af[0] = split2(av[0], av[1], sA1, sB1); af[1] = split2(av[2], av[3], sA1, sB1);
        f32x4 y = {0.f, 0.f, 0.f, 0.f};
#pragma unroll
        for (int u = 0; u < 5; ++u) { u32x2 m; m[0] = (i16 >> 1) == u ? af[0] : 0u; m[1] = (i16 >> 1) == u ? af[1] : 0u; y = mfma16(b2[u], m, y); }
        {
#pragma unroll
            for (int j = 0; j < 4; ++j) y[j] += dppf<QX1>(y[j]);
            u32x2 o; o[0] = san(pk2(y[0], y[1])); o[1] = san(pk2(y[2], y[3]));
            st64<SC1>(o, rWS, fpo);
        }
        if (w == 0) TS(15);
        retire<PAT>(pa, pb, pc);
        const int ci = (r - 20) * 8 + x;
        if (ci < 40 && ci / 5 == w) {
            const u32 fo0 = W_FP + (u32)ci * 3072u + lane * 16;
            const u32 cf = 16 * (ci % 5) + 8 * (lane >> 5) + (lane & 7);
            const float xf = lds_ld<float>(blk + B_X1 + cf * 4), gf = lds_ld<float>(blk + B_GB + cf * 4);
            u32x4 sel = sel8(i16, true);
            u32 oo = (lane & 24) == 0 ? (16 * ci + 8 * (lane >> 5) + (lane & 7)) * 2 : OOB;
            asm volatile("" : "+v"(sel), "+v"(oo) : "v"(xf), "v"(gf));
            if (FPDLY) __builtin_amdgcn_s_sleep(FPDLY);
            PSet<3> fv, qa, qb, qc;
            ppoll<3, PFP>(fv, qa, qb, qc,
                [&](PSet<3>& s) { s.v[0] = ld128<PAUXL>(rWS, fo0); s.v[1] = ld128<PAUXL>(rWS, fo0 + 1024); s.v[2] = ld128<PAUXL>(rWS, fo0 + 2048); },
                [&](const PSet<3>& s) { return none_sent(mx4(mx4(mx4(0, s.v[0]), s.v[1]), s.v[2])); }, dead);
            TS(16);
            f32x4 d = {0.f, 0.f, 0.f, 0.f};
#pragma unroll
            for (int k = 0; k < 3; ++k) d = mfma32(fv.v[k], sel, d);
            float f = sc32(d[0] + d[2], d[1] + d[3]);
            { float a = f, bb = f; swap16(a, bb); f = a + bb; }
            const float o = xf + gf * f;
            const rsrc_t rO = mkrs(p_out, M_ * 2);
            __builtin_amdgcn_raw_buffer_store_b16((unsigned short)bf1(o), rO, oo, 0, 0);
            if (ci == 0 && lane == 0) { u32x2 e; e[0] = T1; e[1] = T2; st64<SC1>(e, rWS, W_EPOCH); }
            TS(17);
            retire<PFP>(qa, qb, qc);
        }
    }
    TSDUMP();
}

extern "C" __device__ __attribute__((noinline, used)) void adaln_code_end() { asm volatile("s_nop 0"); }

extern "C" void kernel_launch(void* const* d_in, const int* in_sizes, int n_in, void* d_out, int out_size, void* d_ws, size_t ws_size, hipStream_t stream)
{
    (void)in_sizes; (void)n_in; (void)out_size;
    const size_t ws_off = WSOFF < 0 ? ((ws_size - 8388608u) & ~(size_t)4095) : (size_t)WSOFF;
    (void)hipFuncSetAttribute((const void*)adaln_mla_ffn_kernel, hipFuncAttributeMaxDynamicSharedMemorySize, (int)LDS_BYTES);
    hipLaunchKernelGGL(adaln_mla_ffn_kernel, dim3(256), dim3(640), LDS_BYTES, stream,
                       (const void*)d_in[0], (const void*)d_in[1], (const void*)d_in[2], (const void*)d_in[3], (const void*)d_in[4], (const void*)d_in[5],
                       (const void*)d_in[7], (const void*)d_in[8], (const void*)d_in[9], (const void*)d_in[10], d_out, (void*)((char*)d_ws + ws_off));
}
```
